```python
import torch

def _softmax(x, axis=-1):
    return torch.softmax(x, dim=axis)
def _gelu(x):
    return torch.nn.functional.gelu(x, approximate="tanh")
def _silu(x):
    return torch.nn.functional.silu(x)
def _sigmoid(x):
    return torch.sigmoid(x)
def _layernorm(x, axis=-1, eps=1e-5):
    m = x.mean(dim=axis, keepdim=True)
    v = x.var(dim=axis, keepdim=True, unbiased=False)
    return (x - m) / torch.sqrt(v + eps)
def _scatter_add(tgt, idx, src, axis=0):
    return tgt.clone().index_add_(axis, idx, src)
def _segment_sum(src, seg, n):
    out = torch.zeros((n,) + src.shape[1:], dtype=src.dtype)
    return out.index_add_(0, seg, src)
def _pad(x, pw):
    flat = tuple(v for pair in reversed(pw) for v in pair)
    return torch.nn.functional.pad(x, flat)

kernel_name = 'gated_mix__linear_kernel_hedgehog__grouped_kv_mqa__prompt'

def setup_inputs():

    _w = (lambda a: (lambda t: t.to(torch.bfloat16) if t.is_floating_point() else t)(torch.as_tensor(a)))
    def _A_setup():
        B, S, D, H = 1, 2048, 576, 32
        Dh = D // H
        return {
            "x": _w((lambda *s: torch.randn(*s, dtype=torch.bfloat16) * 0.05)(B, S, D)),
            "wqkv": _w((lambda *s: torch.randn(*s, dtype=torch.bfloat16) * 1.6)(D, 3 * D)),
            "w_phi": _w((lambda *s: torch.randn(*s, dtype=torch.bfloat16) * 1.6)(H, Dh, Dh) * (Dh ** -0.5)),
            "wo": _w((lambda *s: torch.randn(*s, dtype=torch.bfloat16) * 0.05)(D, D)),
        }
    _A_in = _A_setup()

    def _B_setup():
        B, S, D, H = 1, 2048, 576, 32
        Dh = D // H
        return {
            "x": _w((lambda *s: torch.randn(*s, dtype=torch.bfloat16) * 0.05)(B, S, D)),
            "wq": _w((lambda *s: torch.randn(*s, dtype=torch.bfloat16) * 1.6)(D, H * Dh)),
            "wk": _w((lambda *s: torch.randn(*s, dtype=torch.bfloat16) * 1.6)(D, Dh)),
            "wv": _w((lambda *s: torch.randn(*s, dtype=torch.bfloat16) * 0.05)(D, Dh)),
            "wo": _w((lambda *s: torch.randn(*s, dtype=torch.bfloat16) * 0.05)(H * Dh, D)),
        }
    _B_in = _B_setup()

    return {
        "A_x": _A_in["x"],
        "A_wqkv": _A_in["wqkv"],
        "A_w_phi": _A_in["w_phi"],
        "A_wo": _A_in["wo"],
        "B_x": _B_in["x"],
        "B_wq": _B_in["wq"],
        "B_wk": _B_in["wk"],
        "B_wv": _B_in["wv"],
        "B_wo": _B_in["wo"],
        "gate_w": _w((lambda *s: torch.randn(*s, dtype=torch.bfloat16) * 0.05)(576) * 16.0),
    }

def reference(A_x, A_wqkv, A_w_phi, A_wo, B_x, B_wq, B_wk, B_wv, B_wo, gate_w):
    def _A_ref(x, wqkv, w_phi, wo):
        B, S, D = x.shape
        H = 32; Dh = D // H
        qkv = (lambda x, *d: x.permute(*d))((x @ wqkv).reshape(B, S, 3, H, Dh), 2, 0, 3, 1, 4)
        q, k, v = qkv[0], qkv[1], qkv[2]
        pq = torch.einsum("bhsd,hdf->bhsf", q, w_phi)
        pk = torch.einsum("bhsd,hdf->bhsf", k, w_phi)
        phi_q = torch.concatenate([_softmax(pq, axis=-1), _softmax(-pq, axis=-1)], axis=-1)
        phi_k = torch.concatenate([_softmax(pk, axis=-1), _softmax(-pk, axis=-1)], axis=-1)
        kv = torch.einsum("bhsf,bhsd->bhfd", phi_k, v)
        z  = torch.einsum("bhsf->bhf", phi_k)
        num = torch.einsum("bhqf,bhfd->bhqd", phi_q, kv)
        den = torch.einsum("bhqf,bhf->bhq", phi_q, z)[..., None] + 1e-6
        o = num / den
        return (lambda x, *d: x.permute(*d))(o, 0, 2, 1, 3).reshape(B, S, D) @ wo

    def _B_ref(x, wq, wk, wv, wo):
        B, S, D = x.shape
        H = 32
        Dh = D // H
        q = (lambda x, *d: x.permute(*d))((x @ wq).reshape(B, S, H, Dh), 0, 2, 1, 3)
        k = x @ wk
        v = x @ wv
        scores = torch.einsum("bhqd,bkd->bhqk", q, k) * (Dh ** -0.5)
        p = _softmax(scores, axis=-1)
        o = torch.einsum("bhqk,bkd->bhqd", p, v)
        return (lambda x, *d: x.permute(*d))(o, 0, 2, 1, 3).reshape(B, S, H * Dh) @ wo

    x = A_x
    g = _sigmoid((x * gate_w).sum(-1, keepdims=True))
    a = _A_ref(x, A_wqkv, A_w_phi, A_wo)
    b = _B_ref(x, B_wq, B_wk, B_wv, B_wo)
    return g * a + (1 - g) * b
```

```cpp
#include <hip/hip_runtime.h>
#include <hip/hip_bf16.h>
typedef __hip_bfloat16 bf16;
constexpr int S = 2048, D = 576, H = 32, DH = 18;
__device__ inline float b2f(bf16 x) { return __bfloat162float(x); }

__global__ void gemm_naive(const bf16* A, const bf16* B, float* C, int M, int N, int K, int ldc) {
  int n = blockIdx.x * 64 + (threadIdx.x & 63), m = blockIdx.y * 4 + (threadIdx.x >> 6);
  if (n >= N || m >= M) return;
  float acc = 0;
  for (int k = 0; k < K; ++k) acc += b2f(A[(size_t)m * K + k]) * b2f(B[(size_t)k * N + n]);
  C[(size_t)m * ldc + n] = acc;
}
__global__ void gemm_naive_f(const float* A, const bf16* B, float* C, int M, int N, int K) {
  int n = blockIdx.x * 64 + (threadIdx.x & 63), m = blockIdx.y * 4 + (threadIdx.x >> 6);
  if (n >= N || m >= M) return;
  float acc = 0;
  for (int k = 0; k < K; ++k) acc += A[(size_t)m * K + k] * b2f(B[(size_t)k * N + n]);
  C[(size_t)m * N + n] = acc;
}
__global__ void phi_kernel(const float* qkv, int off, const bf16* wphi, float* phi) {
  int idx = blockIdx.x * 256 + threadIdx.x;
  int s = idx / H, h = idx % H;
  float p[DH];
  const float* q = qkv + (size_t)s * 3 * D + off + h * DH;
  for (int f = 0; f < DH; ++f) { float a = 0; for (int d = 0; d < DH; ++d) a += q[d] * b2f(wphi[(h * DH + d) * DH + f]); p[f] = a; }
  float mx = -1e30f, mn = 1e30f;
  for (int f = 0; f < DH; ++f) { mx = fmaxf(mx, p[f]); mn = fminf(mn, p[f]); }
  float s1 = 0, s2 = 0;
  float* o = phi + (size_t)idx * 36;
  for (int f = 0; f < DH; ++f) { float e1 = expf(p[f] - mx), e2 = expf(mn - p[f]); o[f] = e1; o[DH + f] = e2; s1 += e1; s2 += e2; }
  for (int f = 0; f < DH; ++f) { o[f] /= s1; o[DH + f] /= s2; }
}
__global__ void kv_kernel(const float* phik, const float* qkv, float* kv) {
  int h = blockIdx.x; int t = threadIdx.x;
  if (t >= 36 * 19) return;
  int f = t / 19, d = t % 19;
  float a = 0;
  for (int s = 0; s < S; ++s) a += phik[((size_t)s * H + h) * 36 + f] * (d < 18 ? qkv[(size_t)s * 3 * D + 2 * D + h * DH + d] : 1.f);
  kv[(h * 36 + f) * 19 + d] = a;
}
__global__ void oa_kernel(const float* phiq, const float* kv, float* oa) {
  int idx = blockIdx.x * 256 + threadIdx.x; int s = idx / H, h = idx % H;
  const float* p = phiq + (size_t)idx * 36;
  float den = 1e-6f; { float a = 0; for (int f = 0; f < 36; ++f) a += p[f] * kv[(h * 36 + f) * 19 + 18]; den += a; }
  for (int d = 0; d < DH; ++d) { float a = 0; for (int f = 0; f < 36; ++f) a += p[f] * kv[(h * 36 + f) * 19 + d]; oa[(size_t)s * D + h * DH + d] = a / den; }
}
__global__ void attn_kernel(const float* q, const float* k, const float* v, float* ob) {
  int idx = blockIdx.x * 256 + threadIdx.x; int s = idx / H, h = idx % H;
  float qq[DH]; for (int d = 0; d < DH; ++d) qq[d] = q[(size_t)s * D + h * DH + d] * 0.23570226039551584f;
  float m = -1e30f, l = 0, o[DH]; for (int d = 0; d < DH; ++d) o[d] = 0;
  for (int j = 0; j < S; ++j) {
    float a = 0; for (int d = 0; d < DH; ++d) a += qq[d] * k[j * DH + d];
    float mn = fmaxf(m, a); float al = expf(m - mn), p = expf(a - mn);
    l = l * al + p; for (int d = 0; d < DH; ++d) o[d] = o[d] * al + p * v[j * DH + d];
    m = mn;
  }
  for (int d = 0; d < DH; ++d) ob[(size_t)s * D + h * DH + d] = o[d] / l;
}
__global__ void final_kernel(const bf16* x, const bf16* gw, const float* a, const float* b, bf16* out) {
  int s = blockIdx.x; __shared__ float red[256];
  float acc = 0; for (int d = threadIdx.x; d < D; d += 256) acc += b2f(x[(size_t)s * D + d]) * b2f(gw[d]);
  red[threadIdx.x] = acc; __syncthreads();
  for (int o = 128; o > 0; o >>= 1) { if (threadIdx.x < o) red[threadIdx.x] += red[threadIdx.x + o]; __syncthreads(); }
  float g = 1.f / (1.f + expf(-red[0]));
  for (int d = threadIdx.x; d < D; d += 256) out[(size_t)s * D + d] = __float2bfloat16(g * a[(size_t)s * D + d] + (1.f - g) * b[(size_t)s * D + d]);
}
extern "C" void kernel_launch(void* const* d_in, const int* in_sizes, int n_in, void* d_out, int out_size, void* d_ws, size_t ws_size, hipStream_t stream) {
  const bf16* x = (const bf16*)d_in[0]; const bf16* wqkv = (const bf16*)d_in[1]; const bf16* wphi = (const bf16*)d_in[2]; const bf16* woA = (const bf16*)d_in[3];
  const bf16* wq = (const bf16*)d_in[5]; const bf16* wk = (const bf16*)d_in[6]; const bf16* wv = (const bf16*)d_in[7]; const bf16* woB = (const bf16*)d_in[8]; const bf16* gw = (const bf16*)d_in[9];
  float* ws = (float*)d_ws;
  float* qkv = ws; ws += (size_t)S * 3 * D;
  float* phiq = ws; ws += (size_t)S * H * 36;
  float* phik = ws; ws += (size_t)S * H * 36;
  float* kv = ws; ws += H * 36 * 19 + 64;
  float* oa = ws; ws += (size_t)S * D;
  float* qb = ws; ws += (size_t)S * D;
  float* kb = ws; ws += (size_t)S * DH;
  float* vb = ws; ws += (size_t)S * DH;
  float* ob = ws; ws += (size_t)S * D;
  float* a = ws; ws += (size_t)S * D;
  float* b = ws; ws += (size_t)S * D;
  gemm_naive<<<dim3(27, S / 4), 256, 0, stream>>>(x, wqkv, qkv, S, 3 * D, D, 3 * D);
  gemm_naive<<<dim3(9, S / 4), 256, 0, stream>>>(x, wq, qb, S, D, D, D);
  gemm_naive<<<dim3(1, S / 4), 256, 0, stream>>>(x, wk, kb, S, DH, D, DH);
  gemm_naive<<<dim3(1, S / 4), 256, 0, stream>>>(x, wv, vb, S, DH, D, DH);
  phi_kernel<<<S * H / 256, 256, 0, stream>>>(qkv, 0, wphi, phiq);
  phi_kernel<<<S * H / 256, 256, 0, stream>>>(qkv, D, wphi, phik);
  kv_kernel<<<H, 704, 0, stream>>>(phik, qkv, kv);
  oa_kernel<<<S * H / 256, 256, 0, stream>>>(phiq, kv, oa);
  attn_kernel<<<S * H / 256, 256, 0, stream>>>(qb, kb, vb, ob);
  gemm_naive_f<<<dim3(9, S / 4), 256, 0, stream>>>(oa, woA, a, S, D, D);
  gemm_naive_f<<<dim3(9, S / 4), 256, 0, stream>>>(ob, woB, b, S, D, D);
  final_kernel<<<S, 256, 0, stream>>>(x, gw, a, b, (bf16*)d_out);
}
```

```cpp
#include <hip/hip_runtime.h>
#include <hip/hip_bf16.h>
#include <stdint.h>

namespace fk {
typedef __bf16 bf16x2 __attribute__((ext_vector_type(2)));
typedef __bf16 bf16x8 __attribute__((ext_vector_type(8)));
typedef short s16x4 __attribute__((ext_vector_type(4)));
typedef float f32x2 __attribute__((ext_vector_type(2)));
typedef float f32x4 __attribute__((ext_vector_type(4)));
typedef float f32x16 __attribute__((ext_vector_type(16)));
typedef unsigned u32x2 __attribute__((ext_vector_type(2)));
typedef unsigned u32x4 __attribute__((ext_vector_type(4)));
typedef unsigned long long u64;
typedef __attribute__((address_space(1))) u64 gu64;
typedef __attribute__((address_space(1))) unsigned gu32;

#define AS3 __attribute__((address_space(3)))
#define SBAR() __builtin_amdgcn_sched_barrier(0)
#define PIN(x) asm volatile("" : "+v"(x))
#define MFMA(a, b, c) __builtin_amdgcn_mfma_f32_32x32x16_bf16(__builtin_bit_cast(bf16x8, a), __builtin_bit_cast(bf16x8, b), c, 0, 0, 0)
#define MFMA8(a, b, c) __builtin_amdgcn_mfma_f32_32x32x8bf16_1k(__builtin_bit_cast(s16x4, a), __builtin_bit_cast(s16x4, b), c, 0, 0, 0)
#define MFMA16(a, b, c) __builtin_amdgcn_mfma_f32_16x16x32_bf16(__builtin_bit_cast(bf16x8, a), __builtin_bit_cast(bf16x8, b), c, 0, 0, 0)
#define LA(T, a) (*(const AS3 T*)(uintptr_t)(a))
#define LW(T, a) (*(AS3 T*)(uintptr_t)(a))
#define FENCE() asm volatile("" ::: "memory")
#define EX(x) __builtin_amdgcn_exp2f(x)

#define WS_TMO 64
constexpr unsigned WS_FKV = 4096, WS_FKVP = 8192, WS_FOA = 12288, WS_FOB = 16384;
#define WS_G 32768
#define WS_KMAX 40960
#define WS_KVIMG 65536
#define WS_KVP 262144
#define WS_OAF (2 << 20)
#define WS_OBF (5 << 20)

constexpr unsigned SLOT = 36864, WOFF = 16384;
constexpr unsigned L_WKV = 110592;
constexpr unsigned L_RED = 152064, L_RES = 159744;
constexpr unsigned L_CNT = 161024, L_WPR = 161152;
constexpr unsigned L_WPHI = 152064, L_KVT = 157184;
constexpr unsigned L_PHIK = 113664, L_VB = 134144;
constexpr unsigned KB_BYTES = 2240, STEP_BYTES = 9472, KR_OFF = 8960;
constexpr unsigned LDS_BYTES = 163840;
constexpr unsigned C_LAND = L_CNT, C_ASY = L_CNT + 4, C_BPROG = L_CNT + 8, C_BDONE = L_CNT + 12;

#define STAMP(k) do { } while (0)

__device__ __forceinline__ unsigned cvtpk(float lo, float hi) { f32x2 v = {lo, hi}; return __builtin_bit_cast(unsigned, __builtin_convertvector(v, bf16x2)); }
__device__ __forceinline__ int uni(int v) { return __builtin_amdgcn_readfirstlane(v); }

__device__ __forceinline__ void dma_s(unsigned lds, const void* sbase, unsigned voff) {
    asm volatile("s_mov_b32 m0, %0\n\ts_nop 0\n\tglobal_load_lds_dwordx4 %1, %2" :: "s"(lds), "v"(voff), "s"(sbase) : "memory", "m0");
}
__device__ __forceinline__ void dma_s_sc1(unsigned lds, const void* sbase, unsigned voff) {
    asm volatile("s_mov_b32 m0, %0\n\ts_nop 0\n\tglobal_load_lds_dwordx4 %1, %2 sc1" :: "s"(lds), "v"(voff), "s"(sbase) : "memory", "m0");
}
__device__ __forceinline__ void dma_v(unsigned lds, const void* vaddr) {
    asm volatile("s_mov_b32 m0, %0\n\ts_nop 0\n\tglobal_load_lds_dwordx4 %1, off" :: "s"(lds), "v"(vaddr) : "memory", "m0");
}
#define WAITV(n) asm volatile("s_waitcnt vmcnt(" #n ")" ::: "memory")
#define BAR_L() asm volatile("s_waitcnt lgkmcnt(0)\n\ts_barrier" ::: "memory")
template <int N> __device__ __forceinline__ void bar_vl() { asm volatile("s_waitcnt vmcnt(%0) lgkmcnt(0)\n\ts_barrier" :: "n"(N) : "memory"); }

__device__ __forceinline__ u32x2 trd(unsigned a) { return __builtin_bit_cast(u32x2, __builtin_amdgcn_ds_read_tr16_b64_v4i16((AS3 s16x4*)(uintptr_t)a)); }
__device__ __forceinline__ u32x4 trd2(unsigned a0, unsigned a1) { const u32x2 p = trd(a0), q = trd(a1); return (u32x4){p[0], p[1], q[0], q[1]}; }

__device__ __forceinline__ u64 mix64(u64 z) { z += 0x9E3779B97F4A7C15ull; z = (z ^ (z >> 30)) * 0xBF58476D1CE4E5B9ull; z = (z ^ (z >> 27)) * 0x94D049BB133111EBull; return z ^ (z >> 31); }

constexpr unsigned SPIN_MAX = 1u << 20;
__device__ __forceinline__ void poll_flags(const char* ws, unsigned off, int n, u64 tag, int lane, int slp) {
    const gu64* p = (const gu64*)(ws + off) + (lane < n ? lane : 0);
    for (unsigned s = 0;; ++s) {
        const u64 v = __hip_atomic_load(p, __ATOMIC_RELAXED, __HIP_MEMORY_SCOPE_AGENT);
        if (__all(v == tag)) break;
        if (s > SPIN_MAX) { if (lane == 0) __hip_atomic_store((gu32*)(ws + WS_TMO), 0x100u + off, __ATOMIC_RELAXED, __HIP_MEMORY_SCOPE_AGENT); break; }
        if (slp) __builtin_amdgcn_s_sleep(8); else __builtin_amdgcn_s_sleep(1);
    }
    FENCE();
}
__device__ __forceinline__ void put_flag(char* ws, unsigned off, u64 tag) { __hip_atomic_store((gu64*)(ws + off), tag, __ATOMIC_RELAXED, __HIP_MEMORY_SCOPE_AGENT); }
__device__ __forceinline__ void lds_inc(unsigned a, int lane) { if (lane == 0) __hip_atomic_fetch_add((AS3 unsigned*)(uintptr_t)a, 1u, __ATOMIC_RELAXED, __HIP_MEMORY_SCOPE_WORKGROUP); }
template <int SLP> __device__ __forceinline__ void lds_wait(unsigned a, unsigned target, const char* ws) {
    for (unsigned s = 0;; ++s) {
        const unsigned v = uni(__hip_atomic_load((AS3 unsigned*)(uintptr_t)a, __ATOMIC_RELAXED, __HIP_MEMORY_SCOPE_WORKGROUP));
        if (v >= target) break;
        if (s > SPIN_MAX) { __hip_atomic_store((gu32*)(ws + WS_TMO), 0x10u + (a & 15), __ATOMIC_RELAXED, __HIP_MEMORY_SCOPE_AGENT); break; }
        __builtin_amdgcn_s_sleep(SLP);
    }
    FENCE();
}
__device__ __forceinline__ void async_bar(unsigned& gen, int lane, const char* ws) {
    asm volatile("s_waitcnt lgkmcnt(0)" ::: "memory");
    gen += 4; lds_inc(C_ASY, lane); lds_wait<1>(C_ASY, gen, ws);
}

__device__ __forceinline__ void features(const f32x16& a0, float p16, float p17, float p18, float p19, int lane, unsigned (&out)[18]) {
    const u32x4 b0 = {cvtpk(a0[0], a0[1]), cvtpk(a0[2], a0[3]), cvtpk(a0[4], a0[5]), cvtpk(a0[6], a0[7])};
    const u32x4 b1 = {cvtpk(a0[8], a0[9]), cvtpk(a0[10], a0[11]), cvtpk(a0[12], a0[13]), cvtpk(a0[14], a0[15])};
    const u32x2 b2 = {cvtpk(p16, p17), cvtpk(p18, p19)};
    const unsigned wa = L_WPHI + lane * 16, wr = L_WPHI + 2048 + lane * 8;
    f32x16 P0 = {}, P1 = {};
    P0 = MFMA(LA(u32x4, wa), b0, P0); P1 = MFMA(LA(u32x4, wa + 2560), b0, P1);
    P0 = MFMA(LA(u32x4, wa + 1024), b1, P0); P1 = MFMA(LA(u32x4, wa + 2560 + 1024), b1, P1);
    P0 = MFMA8(LA(u32x2, wr), b2, P0); P1 = MFMA8(LA(u32x2, wr + 2560), b2, P1);
    float p[18];
#pragma unroll
    for (int i = 0; i < 16; ++i) p[i] = P0[i];
    p[16] = P1[0]; p[17] = P1[1];
    float mx = p[0], mn = p[0];
#pragma unroll
    for (int i = 1; i < 18; ++i) { mx = __builtin_fmaxf(mx, p[i]); mn = __builtin_fminf(mn, p[i]); }
    const float L = 1.4426950408889634f, mxl = -mx * L, mnl = mn * L;
    float ep[18], en[18], sp = 0.f, sn = 0.f;
#pragma unroll
    for (int i = 0; i < 18; ++i) { ep[i] = EX(__builtin_fmaf(p[i], L, mxl)); en[i] = EX(__builtin_fmaf(p[i], -L, mnl)); sp += ep[i]; sn += en[i]; }
    const float rp = __builtin_amdgcn_rcpf(sp), rn = __builtin_amdgcn_rcpf(sn);
#pragma unroll
    for (int u = 0; u < 9; ++u) { out[u] = cvtpk(ep[2 * u] * rp, ep[2 * u + 1] * rp); out[9 + u] = cvtpk(en[2 * u] * rn, en[2 * u + 1] * rn); }
}

struct Ptrs { const char *x, *wqkv, *wphi, *woA, *wq, *wk, *wv, *woB, *gate; char *out, *ws; };

template <bool ISB>
__device__ __forceinline__ void role_main(const Ptrs& P, const int wid, const int lane) {
    const int b = blockIdx.x, x8 = b & 7, r = b >> 3;
    const int ti = 8 * (x8 & 1) + (r & 7), tj = 4 * (x8 >> 1) + (r >> 3);
    const int tb = wid & 3, n = lane & 31, hi = lane >> 5, n16 = lane & 15, g4 = lane >> 4;
    const int tid = wid * 64 + lane;
    char* const ws = P.ws;
    const __amdgpu_buffer_rsrc_t rws = __builtin_amdgcn_make_buffer_rsrc((void*)ws, (short)0, 0x1000000, 0x00020000);
    STAMP(0);
    u32x2 Ev; u32x4 xf0, xf1, xf2, gf0, gf1, gf2;
    {
        const int kw = 96 * (wid < 6 ? wid : 5) + 8 * g4;
        const unsigned xo = (unsigned)(128 * ti + 8 * tj + (n16 & 7)) * 1152u + kw * 2, go = kw * 2, zo = 0;
        asm volatile("global_load_dwordx2 %0, %7, %10 sc1\n\t"
                     "global_load_dwordx4 %1, %8, %11\n\tglobal_load_dwordx4 %2, %8, %11 offset:64\n\tglobal_load_dwordx4 %3, %8, %11 offset:128\n\t"
                     "global_load_dwordx4 %4, %9, %12\n\tglobal_load_dwordx4 %5, %9, %12 offset:64\n\tglobal_load_dwordx4 %6, %9, %12 offset:128"
                     : "=&v"(Ev), "=&v"(xf0), "=&v"(xf1), "=&v"(xf2), "=&v"(gf0), "=&v"(gf1), "=&v"(gf2)
                     : "v"(zo), "v"(xo), "v"(go), "s"(ws), "s"(P.x), "s"(P.gate) : "memory");
    }
#pragma unroll
    for (int t = 0; t < 5; ++t) {
        const int q = (wid + 8 * t) * 64 + lane;
        const char* src = q < 1296 ? P.wk + q * 16 : P.wv + (q - 1296) * 16;
        dma_v(L_WKV + (wid + 8 * t) * 1024, src);
    }
    if (wid == 0) { if (lane < 32) dma_v(L_WKV + 40 * 1024, P.wv + (40 * 64 + lane - 1296) * 16); }
    if (wid == 6) { dma_s(L_WPR, P.wphi + tj * 1296, lane * 16); if (lane < 17) dma_s(L_WPR + 1024, P.wphi + tj * 1296 + 1024, lane * 16); }
    if (tid == 0) { LW(u32x4, L_CNT) = (u32x4){0u, 0u, 0u, 0u}; }
    const int rot = (r + 3 * x8) % 9;
    constexpr int ND = ISB ? 4 : 5;
    unsigned xo[4]; const char* wb[5]; unsigned wst[5];
    if (ISB) {
#pragma unroll
        for (int i = 0; i < 4; ++i) xo[i] = (unsigned)(128 * ti + 32 * tb + 8 * i + (lane >> 3)) * 1152u + (((lane & 7) ^ (4 * (i & 1) + (lane >> 4))) * 16);
    } else {
        const int dl = 8 * (tj & 1);
#pragma unroll
        for (int i = 0; i < 5; ++i) {
            const int o = (5 * tb + i) * 1024 + lane * 16, row = o / 320, wi = o - row * 320, ty = wi / 80, pc = wi - ty * 80;
            const int col = 72 * tj - dl + pc;
            wb[i] = ty == 3 ? P.wq + row * 1152 + col : P.wqkv + row * 3456 + col + (ty == 0 ? 1152 : (ty == 1 ? 0 : 2304));
            wst[i] = ty == 3 ? 64 * 1152 : 64 * 3456;
        }
    }
    auto issue = [&](int c) {
        int kc = c + rot; if (kc >= 9) kc -= 9;
        const unsigned slot = (c & 3) * SLOT;
        if (ISB) {
            const char* xb = P.x + kc * 128;
#pragma unroll
            for (int i = 0; i < 4; ++i) dma_s(slot + (4 * tb + i) * 1024, xb, xo[i]);
        } else {
#pragma unroll
            for (int i = 0; i < 5; ++i) dma_v(slot + WOFF + (5 * tb + i) * 1024, wb[i] + (u64)kc * wst[i]);
        }
    };
    issue(0); issue(1); issue(2);
    asm volatile("s_waitcnt vmcnt(12) lgkmcnt(0)\n\ts_barrier" : "+v"(Ev), "+v"(xf0), "+v"(xf1), "+v"(xf2), "+v"(gf0), "+v"(gf1), "+v"(gf2) :: "memory");
    STAMP(1);
    const u64 tag = mix64(((u64)(unsigned)uni(Ev[1]) << 32) | (unsigned)uni(Ev[0]));
    u32x4 wf[5]; u32x2 dummy; (void)dummy;
    if (wid < 6) {
        f32x4 d0 = {}, d1 = {}, d2 = {}, d3 = {};
        const unsigned ak = L_WKV + (96 * wid + 8 * g4) * 36 + n16 * 2;
        const unsigned am = L_WKV + ((n16 & 2) ? 20736 : 0) + (96 * wid + 8 * g4) * 36 + 32 + (n16 & 1) * 2;
#pragma unroll
        for (int s = 0; s < 3; ++s) {
            u32x4 a0, a1, a2;
#pragma unroll
            for (int u = 0; u < 4; ++u) {
                const unsigned o = (32 * s + 2 * u) * 36;
                a0[u] = (unsigned)LA(unsigned short, ak + o) | ((unsigned)LA(unsigned short, ak + o + 36) << 16);
                a1[u] = (unsigned)LA(unsigned short, ak + 20736 + o) | ((unsigned)LA(unsigned short, ak + 20736 + o + 36) << 16);
                a2[u] = (unsigned)LA(unsigned short, am + o) | ((unsigned)LA(unsigned short, am + o + 36) << 16);
            }
            const u32x4 xf = s == 0 ? xf0 : (s == 1 ? xf1 : xf2), gf = s == 0 ? gf0 : (s == 1 ? gf1 : gf2);
            d0 = MFMA16(a0, xf, d0); d1 = MFMA16(a1, xf, d1); d2 = MFMA16(a2, xf, d2); d3 = MFMA16(gf, xf, d3);
        }
        const unsigned ra = L_RED + wid * 1280 + (n16 & 7) * 160;
        const bool up = n16 >= 8;
        LW(f32x4, ra + (up ? 64 : 0) + g4 * 16) = up ? d1 : d0;
        if (g4 == 0) LW(f32x4, ra + (up ? 144 : 128)) = up ? d3 : d2;
    } else if (wid == 6) {
        const int hm = (n >> 2) & 1;
#pragma unroll
        for (int mb = 0; mb < 2; ++mb) {
            const int f = 16 * mb + 4 * (n >> 3) + (n & 3);
            const bool vm = hm == hi && f < 18;
            unsigned v[20];
#pragma unroll
            for (int pos = 0; pos < 20; ++pos) {
                const int d = pos - 2 * hi; const bool ok = vm && d >= 0 && d < 18;
                const unsigned e = LA(unsigned short, L_WPR + (ok ? (hi * 324 + d * 18 + f) * 2 : 0));
                v[pos] = ok ? e : 0u;
            }
            wf[2 * mb] = (u32x4){v[0] | (v[1] << 16), v[2] | (v[3] << 16), v[4] | (v[5] << 16), v[6] | (v[7] << 16)};
            wf[2 * mb + 1] = (u32x4){v[8] | (v[9] << 16), v[10] | (v[11] << 16), v[12] | (v[13] << 16), v[14] | (v[15] << 16)};
            if (mb == 0) { wf[4][0] = v[16] | (v[17] << 16); wf[4][1] = v[18] | (v[19] << 16); } else { wf[4][2] = v[16] | (v[17] << 16); wf[4][3] = v[18] | (v[19] << 16); }
        }
    }
    BAR_L();
    STAMP(2);
    if (tid < 320) {
        float s = 0.f;
#pragma unroll
        for (int w = 0; w < 6; ++w) s += LA(float, L_RED + w * 1280 + tid * 4);
        LW(float, L_RES + tid * 4) = s;
    }
    f32x16 acc0 = {}, acc1 = {}, acc2 = {};
    unsigned wa0, wa1, wa2, xa;
    {
        const int i = lane & 15, sl = 2 * (g4 & 1) + ((i & 3) >> 1);
        const unsigned base = WOFF + (8 * (g4 >> 1) + (i >> 2)) * 320 + 8 * (tj & 1) + 32 * (i & 1);
        if (ISB) {
            wa0 = base + 8 * sl;
            wa1 = base + (sl == 0 ? 32 : 240 + 8 * (sl - 1));
            wa2 = base + (sl < 2 ? 240 + 24 + 8 * sl : 160 + 24 + 8 * (sl - 2));
        } else {
            wa0 = base + 80 + 8 * sl;
            wa1 = base + (sl == 0 ? 80 + 32 : 160 + 8 * (sl - 1));
            wa2 = 0;
        }
        xa = (32 * tb + n) * 128 + ((hi ^ ((n >> 1) & 7)) * 16);
    }
#pragma unroll
    for (int c = 0; c < 9; ++c) {
        if (c <= 6) bar_vl<2 * ND>(); else if (c == 7) bar_vl<ND>(); else bar_vl<0>();
        if (c == 0) STAMP(3);
        if (c == 4) STAMP(4);
        if (!ISB && c == 0) {
            if (wid == 7) {
                const int t = lane & 7, bi = tj >> 2, pc = tj & 3;
                const int rho = 16 * (pc >> 1) + 8 * (t >> 2) + 4 * (pc & 1) + (t & 3);
                const int d = lane - 16;
                unsigned base, strd;
                if (lane < 16) { base = t * 40 + 8 * (lane >> 3); strd = 1; }
                else if (lane < 40) { base = d < 16 ? 16 + d : (d < 18 ? 18 + d : 0); strd = 40; }
                else if (lane < 48) { base = t * 40 + 32; strd = 1; }
                else { base = t * 40 + 36; strd = 0; }
                float v[8];
#pragma unroll
                for (int i = 0; i < 8; ++i) v[i] = LA(float, L_RES + (base + i * strd) * 4);
                float ss = v[0] * v[0] + v[1] * v[1];
                float s6 = 0.f;
#pragma unroll
                for (int i = 2; i < 8; ++i) s6 += v[i] * v[i];
                if (lane < 16) ss += s6;
                u32x4 o = {cvtpk(v[0], v[1]), cvtpk(v[2], v[3]), cvtpk(v[4], v[5]), cvtpk(v[6], v[7])};
                if (lane == 34) o = (u32x4){0x3f803f80u, 0x3f803f80u, 0x3f803f80u, 0x3f803f80u};
                const unsigned img = WS_KVIMG + ti * STEP_BYTES;
                const unsigned o16 = lane < 16 ? img + bi * KB_BYTES + ((lane >> 3) * 32 + rho) * 16 : img + bi * KB_BYTES + 1024 + pc * 304 + d * 16;
                if (lane < 35) __builtin_amdgcn_raw_buffer_store_b128(o, rws, o16, 0, 16);
                float tot = __shfl(ss, t) + __shfl(ss, 8 + t) + __shfl(ss, 40 + t);
                tot = __builtin_fmaxf(tot, __shfl_xor(tot, 1)); tot = __builtin_fmaxf(tot, __shfl_xor(tot, 2)); tot = __builtin_fmaxf(tot, __shfl_xor(tot, 4));
                const float gv = __builtin_amdgcn_rcpf(1.f + EX(-1.4426950408889634f * v[0]));
                unsigned o4, d4;
                if (lane < 48) { o4 = img + KR_OFF + (bi >> 1) * 256 + rho * 8 + (bi & 1) * 4; d4 = o[0]; }
                else if (lane < 56) { o4 = WS_G + (128 * ti + 8 * tj + t) * 4; d4 = __float_as_uint(gv); }
                else { o4 = WS_KMAX + (16 * ti + tj) * 4; d4 = __float_as_uint(tot); }
                if (lane >= 40 && lane <= 56) __builtin_amdgcn_raw_buffer_store_b32(d4, rws, o4, 0, 16);
            } else if (wid == 6) {
                LW(u32x4, L_WPHI + lane * 16) = wf[0]; LW(u32x4, L_WPHI + 1024 + lane * 16) = wf[1];
                LW(u32x4, L_WPHI + 2560 + lane * 16) = wf[2]; LW(u32x4, L_WPHI + 2560 + 1024 + lane * 16) = wf[3];
                LW(u32x2, L_WPHI + 2048 + lane * 8) = (u32x2){wf[4][0], wf[4][1]}; LW(u32x2, L_WPHI + 2560 + 2048 + lane * 8) = (u32x2){wf[4][2], wf[4][3]};
            }
        }
        if (!ISB && c == 2) { if (wid == 7 && lane == 0) put_flag(ws, WS_FKV + (16 * ti + tj) * 8, tag); }
        if (c + 3 < 9) issue(c + 3);
        const unsigned slot = (c & 3) * SLOT;
#pragma unroll
        for (int ks = 0; ks < 4; ++ks) {
            const u32x4 xf = LA(u32x4, slot + (xa ^ (ks * 32)));
            const unsigned o = slot + ks * 5120;
            acc0 = MFMA(trd2(wa0 + o, wa0 + o + 1280), xf, acc0);
            acc1 = MFMA(trd2(wa1 + o, wa1 + o + 1280), xf, acc1);
            if (ISB) acc2 = MFMA(trd2(wa2 + o, wa2 + o + 1280), xf, acc2);
        }
    }
    STAMP(5);
    if (!ISB && wid == 7) {
#pragma unroll
        for (int q = 0; q < 4; ++q) poll_flags(ws, WS_FKV + q * 512, 64, tag, lane, 0);
        if (b == 0 && lane == 0) __hip_atomic_store((gu64*)ws, tag, __ATOMIC_RELAXED, __HIP_MEMORY_SCOPE_AGENT);
    }
    BAR_L();
    STAMP(6);
    const float gate = __uint_as_float(__builtin_amdgcn_raw_buffer_load_b32(rws, WS_G + (128 * ti + 32 * tb + n) * 4, 0, 16));
    const unsigned vrow = L_VB + (32 * tb + n) * 112 + hi * 52 + 8;

    if (ISB) {
        const u32x4 km4 = __builtin_bit_cast(u32x4, __builtin_amdgcn_raw_buffer_load_b128(rws, WS_KMAX + lane * 16, 0, 16));
        {
            unsigned ph[18];
            features(acc0, acc1[0], acc1[1], acc1[2], acc1[3], lane, ph);
            const unsigned pa = L_PHIK + (32 * tb + n) * 160 + hi * 80;
#pragma unroll
            for (int u = 0; u < 4; ++u) LW(u32x4, pa + 16 * u) = (u32x4){ph[4 * u], ph[4 * u + 1], ph[4 * u + 2], ph[4 * u + 3]};
            LW(u32x2, pa + 64) = (u32x2){ph[16], ph[17]};
            LW(unsigned, vrow + 24) = cvtpk(acc2[8], acc2[9]); LW(unsigned, vrow + 28) = cvtpk(acc2[10], acc2[11]);
            LW(unsigned, vrow + 32) = cvtpk(acc2[12], acc2[13]); LW(unsigned, vrow + 36) = cvtpk(acc2[14], acc2[15]);
            LW(unsigned, vrow + 36 + 4 * hi) = 0x00003f80u;
        }
        u32x4 QA, QB; u32x2 QRA0, QRA1, QRB0, QRB1; f32x16 MA, MB;
        {
            const float sc = 0.23570226039551584f * 1.4426950408889634f;
            float q[20];
#pragma unroll
            for (int i = 0; i < 12; ++i) q[i] = acc1[4 + i] * sc;
#pragma unroll
            for (int i = 0; i < 8; ++i) q[12 + i] = acc2[i] * sc;
            float n2 = hi ? q[18] * q[18] + q[19] * q[19] : q[0] * q[0] + q[1] * q[1];
#pragma unroll
            for (int i = 2; i < 18; ++i) n2 = __builtin_fmaf(q[i], q[i], n2);
            unsigned cc[10], D[9];
#pragma unroll
            for (int u = 0; u < 10; ++u) cc[u] = cvtpk(q[2 * u], q[2 * u + 1]);
#pragma unroll
            for (int u = 0; u < 9; ++u) D[u] = hi ? cc[u + 1] : cc[u];
#pragma unroll
            for (int u = 0; u < 4; ++u) { const auto sw = __builtin_amdgcn_permlane32_swap(D[u], D[u + 4], false, false); QA[u] = sw[0]; QB[u] = sw[1]; }
            QRA0 = (u32x2){hi ? 0u : D[8], 0u}; QRA1 = (u32x2){0u, hi ? 0u : D[8]};
            QRB0 = (u32x2){hi ? D[8] : 0u, 0u}; QRB1 = (u32x2){0u, hi ? D[8] : 0u};
            SBAR();
            float km = __builtin_fmaxf(__builtin_fmaxf(__uint_as_float(km4[0]), __uint_as_float(km4[1])), __builtin_fmaxf(__uint_as_float(km4[2]), __uint_as_float(km4[3])));
#pragma unroll
            for (int o = 1; o < 64; o <<= 1) km = __builtin_fmaxf(km, __shfl_xor(km, o));
            const float m = -1.01f * __builtin_sqrtf(n2 * km);
            const auto sw = __builtin_amdgcn_permlane32_swap(__float_as_uint(m), __float_as_uint(m), false, false);
            const float ma = __uint_as_float(sw[0]), mb = __uint_as_float(sw[1]);
#pragma unroll
            for (int i = 0; i < 16; ++i) { MA[i] = ma; MB[i] = mb; }
            PIN(MA); PIN(MB); PIN(QA); PIN(QB); PIN(QRA0); PIN(QRA1); PIN(QRB0); PIN(QRB1);
        }
        BAR_L();
        STAMP(7);
        unsigned kp = lane * 16, rp = KR_OFF + n * 8, vp = 1024 + hi * 304 + (n < 18 ? n : 18) * 16;
        PIN(kp); PIN(rp); PIN(vp);
        f32x16 OA = {}, OB = {}, S0A, S0B, S1A, S1B;
        u32x4 KMa, KMb, V0a, V0b, V1a, V1b = {}, P0 = {}, P1 = {}; u32x2 KRX, KRY;
        float e0, e1, e2, e3, f0 = 0.f, f1 = 0.f, f2 = 0.f, f3 = 0.f;
        KRX = LA(u32x2, rp); KMa = LA(u32x4, kp);
        S0A = MFMA(KMa, QA, MA); S0B = MFMA(KMa, QB, MB); S0A = MFMA8(KRX, QRA0, S0A); S0B = MFMA8(KRX, QRB0, S0B);
        SBAR();
        KMb = LA(u32x4, kp + KB_BYTES); V0a = LA(u32x4, vp); V1a = LA(u32x4, vp + 608);
        __builtin_amdgcn_s_waitcnt(0xC07F);
        SBAR();
#define LD(dst, T, p) dst = LA(T, p)
#define NOP_ (void)0
#define SLOTB(MF, POST, d0, d1, d2, d3, S, g, Pq, i, a0, a1, a2, a3) do { MF; SBAR(); POST; SBAR(); \
        d0 = EX(S[4 * (g)]); d1 = EX(S[4 * (g) + 1]); PIN(d0); PIN(d1); SBAR(); \
        Pq[i] = cvtpk(a0, a1); Pq[i + 1] = cvtpk(a2, a3); PIN(Pq); SBAR(); \
        d2 = EX(S[4 * (g) + 2]); d3 = EX(S[4 * (g) + 3]); PIN(d2); PIN(d3); SBAR(); } while (0)
#define E_ e0, e1, e2, e3
#define F_ f0, f1, f2, f3
#define SLOT_(...) SLOTB(__VA_ARGS__)
#define WAITL __builtin_amdgcn_s_waitcnt(0xC07F)
#define PERIOD(SA, SB, NA, NB, KMu, KMl, KRu, QRAx, QRBx, V0u, V1u, V0l, V1l, vb, kb, PA3) do { \
        SLOT_(NA = MFMA(KMu, QA, MA),     LD(KMl, u32x4, kp + (kb)),       E_, SA, 0, P1, 2, F_); \
        SLOT_(OB = MFMA(V1l, P1, OB),     LD(V1l, u32x4, vp + (vb) + 608), F_, SA, 1, P0, 0, E_); \
        SLOT_(NA = MFMA8(KRu, QRAx, NA),  LD(V0l, u32x4, vp + (vb)),       E_, SA, 2, P0, 2, F_); \
        SLOT_(OA = MFMA(V0u, P0, OA),     PA3,                             F_, SA, 3, P1, 0, E_); \
        SLOT_(NB = MFMA(KMu, QB, MB),     NOP_,                            E_, SB, 0, P1, 2, F_); \
        SLOT_(OA = MFMA(V1u, P1, OA),     NOP_,                            F_, SB, 1, P0, 0, E_); \
        SLOT_(NB = MFMA8(KRu, QRBx, NB),  WAITL,                           E_, SB, 2, P0, 2, F_); \
        SLOT_(OB = MFMA(V0u, P0, OB),     NOP_,                            F_, SB, 3, P1, 0, E_); \
    } while (0)
#pragma unroll 1
        for (int seg = 0; seg < 3; ++seg) {
            if (seg == 1) { lds_inc(C_BPROG, lane); STAMP(8); }
            if (seg == 2) lds_wait<1>(C_LAND, 4, ws);
            const int ns = seg == 0 ? 9 : (seg == 1 ? 2 : 5);
#pragma unroll 1
            for (int s = 0; s < ns; ++s) {
                PERIOD(S0A, S0B, S1A, S1B, KMb, KMa, KRX, QRA1, QRB1, V0a, V1a, V0b, V1b, KB_BYTES, 2 * KB_BYTES, LD(KRY, u32x2, rp + 256));
                PERIOD(S1A, S1B, S0A, S0B, KMa, KMb, KRY, QRA0, QRB0, V0b, V1b, V0a, V1a, 2 * KB_BYTES, 3 * KB_BYTES, NOP_);
                PERIOD(S0A, S0B, S1A, S1B, KMb, KMa, KRY, QRA1, QRB1, V0a, V1a, V0b, V1b, 3 * KB_BYTES, STEP_BYTES, LD(KRX, u32x2, rp + STEP_BYTES));
                PERIOD(S1A, S1B, S0A, S0B, KMa, KMb, KRX, QRA0, QRB0, V0b, V1b, V0a, V1a, STEP_BYTES, STEP_BYTES + KB_BYTES, NOP_);
                kp += STEP_BYTES; vp += STEP_BYTES; rp += STEP_BYTES; PIN(kp); PIN(vp); PIN(rp); SBAR();
            }
        }
        P1[2] = cvtpk(f0, f1); P1[3] = cvtpk(f2, f3); PIN(P1); SBAR();
        OB = MFMA(V1b, P1, OB);
        STAMP(10);
        if (__any(!(OA[10] >= 0x1p-80f) || !(OB[10] >= 0x1p-80f))) {
            const unsigned vo = WS_KVIMG + 1024 + hi * 304 + (n < 18 ? n : 18) * 16;
#pragma unroll 1
            for (int which = 0; which < 2; ++which) {
                const u32x4 Q = which ? QB : QA; const u32x2 R0 = which ? QRB0 : QRA0, R1 = which ? QRB1 : QRA1;
                float mx = -__builtin_inff();
#pragma unroll 1
                for (int kb = 0; kb < 64; ++kb) {
                    const unsigned bo = (kb >> 2) * STEP_BYTES;
                    const u32x4 km = __builtin_bit_cast(u32x4, __builtin_amdgcn_raw_buffer_load_b128(rws, WS_KVIMG + bo + (kb & 3) * KB_BYTES + lane * 16, 0, 16));
                    const u32x2 kr = __builtin_bit_cast(u32x2, __builtin_amdgcn_raw_buffer_load_b64(rws, WS_KVIMG + bo + KR_OFF + ((kb >> 1) & 1) * 256 + n * 8, 0, 16));
                    f32x16 S = {}; S = MFMA(km, Q, S); S = MFMA8(kr, (kb & 1) ? R1 : R0, S);
#pragma unroll
                    for (int i = 0; i < 16; ++i) mx = __builtin_fmaxf(mx, S[i]);
                }
                { const auto sw = __builtin_amdgcn_permlane32_swap(__float_as_uint(mx), __float_as_uint(mx), false, false); mx = __builtin_fmaxf(__uint_as_float(sw[0]), __uint_as_float(sw[1])); }
                f32x16 O = {}, C;
#pragma unroll
                for (int i = 0; i < 16; ++i) C[i] = -mx;
#pragma unroll 1
                for (int kb = 0; kb < 64; ++kb) {
                    const unsigned bo = (kb >> 2) * STEP_BYTES, bb = bo + (kb & 3) * KB_BYTES;
                    const u32x4 km = __builtin_bit_cast(u32x4, __builtin_amdgcn_raw_buffer_load_b128(rws, WS_KVIMG + bb + lane * 16, 0, 16));
                    const u32x2 kr = __builtin_bit_cast(u32x2, __builtin_amdgcn_raw_buffer_load_b64(rws, WS_KVIMG + bo + KR_OFF + ((kb >> 1) & 1) * 256 + n * 8, 0, 16));
                    const u32x4 v0 = __builtin_bit_cast(u32x4, __builtin_amdgcn_raw_buffer_load_b128(rws, vo + bb, 0, 16));
                    const u32x4 v1 = __builtin_bit_cast(u32x4, __builtin_amdgcn_raw_buffer_load_b128(rws, vo + bb + 608, 0, 16));
                    f32x16 S = MFMA(km, Q, C); S = MFMA8(kr, (kb & 1) ? R1 : R0, S);
                    float p[16];
#pragma unroll
                    for (int i = 0; i < 16; ++i) p[i] = EX(S[i]);
                    const u32x4 p0 = {cvtpk(p[0], p[1]), cvtpk(p[2], p[3]), cvtpk(p[4], p[5]), cvtpk(p[6], p[7])};
                    const u32x4 p1 = {cvtpk(p[8], p[9]), cvtpk(p[10], p[11]), cvtpk(p[12], p[13]), cvtpk(p[14], p[15])};
                    O = MFMA(v0, p0, O); O = MFMA(v1, p1, O);
                }
                if (which) OB = O; else OA = O;
            }
        }
        {
            const float ga = (1.f - gate) * __builtin_amdgcn_rcpf(OA[10]), gb = (1.f - gate) * __builtin_amdgcn_rcpf(OB[10]);
            const unsigned fo = WS_OBF + (unsigned)(4 * ti + tb) * 36 * 1024;
            const int h0 = 2 * tj;
            const u32x4 fa = {cvtpk(OA[0] * ga, OA[1] * ga), cvtpk(OA[2] * ga, OA[3] * ga), cvtpk(OA[4] * ga, OA[5] * ga), cvtpk(OA[6] * ga, OA[7] * ga)};
            const u32x4 fb = {cvtpk(OB[0] * gb, OB[1] * gb), cvtpk(OB[2] * gb, OB[3] * gb), cvtpk(OB[4] * gb, OB[5] * gb), cvtpk(OB[6] * gb, OB[7] * gb)};
            __builtin_amdgcn_raw_buffer_store_b128(fa, rws, fo + h0 * 1024 + lane * 16, 0, 16);
            __builtin_amdgcn_raw_buffer_store_b128(fb, rws, fo + (h0 + 1) * 1024 + lane * 16, 0, 16);
            const u32x2 rr = {cvtpk(OA[8] * ga, OA[9] * ga), cvtpk(OB[8] * gb, OB[9] * gb)};
            if (!hi) __builtin_amdgcn_raw_buffer_store_b64(rr, rws, fo + (32 + (tj >> 2)) * 1024 + (((tj >> 1) & 1) * 32 + n) * 16 + (tj & 1) * 8, 0, 16);
            WAITV(0);
            unsigned arrived = 0;
            if (lane == 0) arrived = __hip_atomic_fetch_add((AS3 unsigned*)(uintptr_t)C_BDONE, 1u, __ATOMIC_RELAXED, __HIP_MEMORY_SCOPE_WORKGROUP);
            if (lane == 0 && arrived == 3) put_flag(ws, WS_FOB + (16 * ti + tj) * 8, tag);
        }
        STAMP(11);
    } else {
        for (int p = tb; p < 111; p += 4) dma_s_sc1(p * 1024, ws + WS_KVIMG + p * 1024, lane * 16);
#pragma unroll
        for (int u = 0; u < 6; ++u) LW(unsigned, vrow + 4 * u) = cvtpk(acc1[4 + 2 * u], acc1[5 + 2 * u]);
        bar_vl<0>();
        STAMP(7);
        __builtin_amdgcn_s_setprio(2);
        unsigned gen = 0;
        {
            const int hh = tb & 1, i = lane & 15;
            const int tok = 8 * g4 + (i >> 2), gr = i & 3;
            const unsigned va = L_VB + tok * 112 + hh * 56 + 8 + gr * 8;
            const int ntask = tb < 2 ? 2 : 1;
#pragma unroll 1
            for (int t = 0; t < ntask; ++t) {
                const int mbf = tb < 2 ? 2 * t : 1;
                const unsigned pa = L_PHIK + tok * 160 + hh * 80 + mbf * 32 + gr * 8;
                f32x4 d0 = {}, d1 = {};
#pragma unroll
                for (int ks = 0; ks < 4; ++ks) {
                    const u32x4 a = trd2(pa + ks * 32 * 160, pa + ks * 32 * 160 + 4 * 160);
                    const u32x4 b0 = trd2(va + ks * 32 * 112, va + ks * 32 * 112 + 4 * 112);
                    const u32x4 b1 = trd2(va + ks * 32 * 112 + 32, va + ks * 32 * 112 + 4 * 112 + 32);
                    d0 = MFMA16(a, b0, d0); d1 = MFMA16(a, b1, d1);
                }
                const unsigned ko = WS_KVP + (unsigned)(16 * tj + ti) * 5472 + hh * 2736 + (16 * mbf + 4 * g4) * 4;
                if (mbf < 2 || g4 == 0) {
                    __builtin_amdgcn_raw_buffer_store_b128(__builtin_bit_cast(u32x4, d0), rws, ko + n16 * 144, 0, 16);
                    if (n16 < 3) __builtin_amdgcn_raw_buffer_store_b128(__builtin_bit_cast(u32x4, d1), rws, ko + (16 + n16) * 144, 0, 16);
                }
            }
        }
        WAITV(0);
        async_bar(gen, lane, ws);
        if (tb == 0 && lane == 0) put_flag(ws, WS_FKVP + (16 * tj + ti) * 8, tag);
        STAMP(8);
        for (int p = 111 + tb; p < 148; p += 4) dma_s_sc1(p * 1024, ws + WS_KVIMG + p * 1024, lane * 16);
        unsigned pq[18];
        features(acc0, acc1[0], acc1[1], acc1[2], acc1[3], lane, pq);
        WAITV(0);
        lds_inc(C_LAND, lane);
        poll_flags(ws, WS_FKVP + 16 * tj * 8, 16, tag, lane, 1);
        STAMP(9);
        {
            const int at = tb * 64 + lane;
#pragma unroll 1
            for (int rd = 0; rd < 2; ++rd) {
                const int it = rd == 0 ? at : 256 + tb * 22 + lane;
                const bool ok = rd == 0 || (lane < 22 && it < 342);
                const unsigned ko = WS_KVP + (unsigned)(16 * tj) * 5472 + (ok ? it : 0) * 16;
                f32x4 v[16];
#pragma unroll
                for (int i = 0; i < 16; ++i) v[i] = __builtin_bit_cast(f32x4, __builtin_amdgcn_raw_buffer_load_b128(rws, ko + i * 5472, 0, 16));
                f32x4 s = v[0];
#pragma unroll
                for (int i = 1; i < 16; ++i) s += v[i];
                const int hh = it >= 171, rem = it - 171 * hh, d = rem / 9, fq = rem - 9 * d;
                if (ok) LW(u32x2, L_KVT + hh * 1600 + d * 80 + fq * 8) = (u32x2){cvtpk(s[0], s[1]), cvtpk(s[2], s[3])};
            }
            if (tb == 3 && lane < 10) LW(u32x4, L_KVT + (lane >= 5) * 1600 + 19 * 80 + (lane % 5) * 16) = (u32x4){0u, 0u, 0u, 0u};
        }
        async_bar(gen, lane, ws);
        {
            const int hm = (n >> 2) & 1, d0 = 4 * (n >> 3) + (n & 3);
            const unsigned a0 = L_KVT + (hm == hi ? hm * 1600 + d0 * 80 : 19 * 80);
            const unsigned a1 = L_KVT + (hm == hi && d0 < 3 ? hm * 1600 + (16 + d0) * 80 : 19 * 80);
            f32x16 N0 = {}, N1 = {};
#pragma unroll
            for (int s = 0; s < 4; ++s) {
                const u32x4 bq = {pq[4 * s], pq[4 * s + 1], pq[4 * s + 2], pq[4 * s + 3]};
                N0 = MFMA(LA(u32x4, a0 + 16 * s), bq, N0); N1 = MFMA(LA(u32x4, a1 + 16 * s), bq, N1);
            }
            const u32x2 br = {pq[16], pq[17]};
            N0 = MFMA8(LA(u32x2, a0 + 64), br, N0); N1 = MFMA8(LA(u32x2, a1 + 64), br, N1);
            const float sc = gate / (N1[2] + 1e-6f);
            const unsigned fo = WS_OAF + (unsigned)(4 * ti + tb) * 36 * 1024;
            const u32x4 f0 = {cvtpk(N0[0] * sc, N0[1] * sc), cvtpk(N0[2] * sc, N0[3] * sc), cvtpk(N0[4] * sc, N0[5] * sc), cvtpk(N0[6] * sc, N0[7] * sc)};
            const u32x4 f1 = {cvtpk(N0[8] * sc, N0[9] * sc), cvtpk(N0[10] * sc, N0[11] * sc), cvtpk(N0[12] * sc, N0[13] * sc), cvtpk(N0[14] * sc, N0[15] * sc)};
            __builtin_amdgcn_raw_buffer_store_b128(f0, rws, fo + (2 * tj) * 1024 + lane * 16, 0, 16);
            __builtin_amdgcn_raw_buffer_store_b128(f1, rws, fo + (2 * tj + 1) * 1024 + lane * 16, 0, 16);
            __builtin_amdgcn_raw_buffer_store_b32(cvtpk(N1[0] * sc, N1[1] * sc), rws, fo + (32 + (tj >> 2)) * 1024 + (((tj >> 1) & 1) * 32 + n) * 16 + (2 * (tj & 1) + hi) * 4, 0, 16);
        }
        WAITV(0);
        async_bar(gen, lane, ws);
        if (tb == 0 && lane == 0) put_flag(ws, WS_FOA + (16 * ti + tj) * 8, tag);
        STAMP(10);
        const int tt = 4 * x8 + (r & 3), cg = r >> 2;
        unsigned wo[21];
        (void)wo;
        auto stage = [&](const char* w) {
            const char* wsl = w + cg * 144;
#pragma unroll 1
            for (int p = tb; p < 81; p += 4) {
                const int q = p * 64 + lane, row = q / 9, pc = q - row * 9;
                dma_s(p * 1024, wsl, row * 1152 + pc * 16);
            }
        };
        const int i16 = lane & 15, gr = i16 & 3, hk = g4 & 1, fh = g4 >> 1;
        const unsigned fro = (unsigned)(2 * tt + (tb >> 1)) * 36 * 1024 + fh * 1024 + (hk * 32 + n16 + 16 * (tb & 1)) * 16;
        const unsigned remo = (18 * (8 * fh + 4 * hk + (i16 >> 3)) + 16 + ((i16 >> 2) & 1)) * 144 + gr * 8;
        f32x4 T[5] = {};
        auto gemm = [&](unsigned fbase, unsigned maino, unsigned r4s) {
            u32x4 bf[18];
#pragma unroll
            for (int ks = 0; ks < 18; ++ks) bf[ks] = __builtin_bit_cast(u32x4, __builtin_amdgcn_raw_buffer_load_b128(rws, fbase + fro + ks * 2048, 0, 16));
#pragma unroll
            for (int ks = 0; ks < 18; ++ks) {
                const unsigned a = ks < 16 ? maino + ks * 36 * 144 : remo + (ks - 16) * 16 * 18 * 144;
                const unsigned s2 = ks < 16 ? r4s : 2 * 18 * 144;
#pragma unroll
                for (int cb = 0; cb < 5; ++cb) T[cb] = MFMA16(trd2(a + cb * 32, a + cb * 32 + s2), bf[ks], T[cb]);
            }
        };
        lds_wait<16>(C_BPROG, 4, ws);
        stage(P.woA);
        WAITV(0);
        async_bar(gen, lane, ws);
        STAMP(11);
        poll_flags(ws, WS_FOA + 16 * (tt >> 1) * 8, 16, tag, lane, 1);
        gemm(WS_OAF, (18 * hk + 8 * fh + (i16 >> 2)) * 144 + gr * 8, 4 * 144);
        async_bar(gen, lane, ws);
        STAMP(12);
        stage(P.woB);
        WAITV(0);
        async_bar(gen, lane, ws);
        STAMP(13);
        poll_flags(ws, WS_FOB + 16 * (tt >> 1) * 8, 16, tag, lane, 0);
        STAMP(14);
        gemm(WS_OBF, (18 * fh + 4 * hk + (i16 >> 2)) * 144 + gr * 8, 8 * 144);
        char* op = P.out + (size_t)(64 * tt + 16 * tb + n16) * 1152 + cg * 144 + g4 * 8;
#pragma unroll
        for (int cb = 0; cb < 5; ++cb) {
            const u32x2 o = {cvtpk(T[cb][0], T[cb][1]), cvtpk(T[cb][2], T[cb][3])};
            if (cb < 4 || g4 < 2) *(u32x2*)(op + cb * 32) = o;
        }
    }
}

__global__ __launch_bounds__(512, 2) void gated_mix_kernel(const void* x, const void* wqkv, const void* wphi, const void* woA, const void* wq, const void* wk,
                                                           const void* wv, const void* woB, const void* gate, void* out, void* ws) {
    extern __shared__ __attribute__((aligned(16))) char lds[];
    const int lane = threadIdx.x & 63, wid = uni(threadIdx.x >> 6);
    const Ptrs P = {(const char*)x, (const char*)wqkv, (const char*)wphi, (const char*)woA, (const char*)wq, (const char*)wk, (const char*)wv, (const char*)woB, (const char*)gate, (char*)out, (char*)ws};
    if (wid < 4) role_main<true>(P, wid, lane); else role_main<false>(P, wid, lane);
}
}

extern "C" void kernel_launch(void* const* d_in, const int* in_sizes, int n_in, void* d_out, int out_size, void* d_ws, size_t ws_size, hipStream_t stream) {
    (void)in_sizes; (void)n_in; (void)out_size; (void)ws_size;
    (void)hipFuncSetAttribute((const void*)fk::gated_mix_kernel, hipFuncAttributeMaxDynamicSharedMemorySize, fk::LDS_BYTES);
    hipLaunchKernelGGL(fk::gated_mix_kernel, dim3(256), dim3(512), fk::LDS_BYTES, stream,
                       (const void*)d_in[0], (const void*)d_in[1], (const void*)d_in[2], (const void*)d_in[3], (const void*)d_in[5], (const void*)d_in[6],
                       (const void*)d_in[7], (const void*)d_in[8], (const void*)d_in[9], d_out, d_ws);
}
```

```cpp
#include <hip/hip_runtime.h>
#include <hip/hip_bf16.h>
#include <stdint.h>

#define NPASS 1
namespace fk {
typedef __bf16 bf16x2 __attribute__((ext_vector_type(2)));
typedef __bf16 bf16x8 __attribute__((ext_vector_type(8)));
typedef short s16x4 __attribute__((ext_vector_type(4)));
typedef float f32x2 __attribute__((ext_vector_type(2)));
typedef float f32x4 __attribute__((ext_vector_type(4)));
typedef float f32x16 __attribute__((ext_vector_type(16)));
typedef unsigned u32x2 __attribute__((ext_vector_type(2)));
typedef unsigned u32x4 __attribute__((ext_vector_type(4)));
typedef unsigned long long u64;
typedef __attribute__((address_space(1))) u64 gu64;
typedef __attribute__((address_space(1))) unsigned gu32;

#define AS3 __attribute__((address_space(3)))
#define SBAR() __builtin_amdgcn_sched_barrier(0)
#define PIN(x) asm volatile("" : "+v"(x))
#define MFMA(a, b, c) __builtin_amdgcn_mfma_f32_32x32x16_bf16(__builtin_bit_cast(bf16x8, a), __builtin_bit_cast(bf16x8, b), c, 0, 0, 0)
#define MFMA8(a, b, c) __builtin_amdgcn_mfma_f32_32x32x8bf16_1k(__builtin_bit_cast(s16x4, a), __builtin_bit_cast(s16x4, b), c, 0, 0, 0)
#define MFMA16(a, b, c) __builtin_amdgcn_mfma_f32_16x16x32_bf16(__builtin_bit_cast(bf16x8, a), __builtin_bit_cast(bf16x8, b), c, 0, 0, 0)
#define LA(T, a) (*(const AS3 T*)(uintptr_t)(a))
#define LW(T, a) (*(AS3 T*)(uintptr_t)(a))
#define FENCE() asm volatile("" ::: "memory")
#define EX(x) __builtin_amdgcn_exp2f(x)

#define WS_TMO 64
constexpr unsigned WS_FKV = 4096, WS_FKVP = 8192, WS_FOA = 12288, WS_FOB = 16384;
#define WS_G 32768
#define WS_KMAX 40960
#define WS_KVIMG 65536
#define WS_KVP 262144
#define WS_OAF (2 << 20)
#define WS_OBF (5 << 20)

constexpr unsigned SLOT = 36864, WOFF = 16384;
constexpr unsigned L_WKV = 110592;
constexpr unsigned L_RED = 152064, L_RES = 159744;
constexpr unsigned L_CNT = 161024, L_WPR = 161152;
constexpr unsigned L_WPHI = 152064, L_KVT = 157184;
constexpr unsigned L_PHIK = 113664, L_VB = 134144;
constexpr unsigned KB_BYTES = 2240, STEP_BYTES = 9472, KR_OFF = 8960;
constexpr unsigned LDS_BYTES = 163840;
constexpr unsigned C_LAND = L_CNT, C_ASY = L_CNT + 4, C_BPROG = L_CNT + 8, C_BDONE = L_CNT + 12;

#define STAMP(k) do { } while (0)

__device__ __forceinline__ unsigned cvtpk(float lo, float hi) { f32x2 v = {lo, hi}; return __builtin_bit_cast(unsigned, __builtin_convertvector(v, bf16x2)); }
__device__ __forceinline__ int uni(int v) { return __builtin_amdgcn_readfirstlane(v); }

__device__ __forceinline__ void dma_s(unsigned lds, const void* sbase, unsigned voff) {
    asm volatile("s_mov_b32 m0, %0\n\ts_nop 0\n\tglobal_load_lds_dwordx4 %1, %2" :: "s"(lds), "v"(voff), "s"(sbase) : "memory", "m0");
}
__device__ __forceinline__ void dma_s_sc1(unsigned lds, const void* sbase, unsigned voff) {
    asm volatile("s_mov_b32 m0, %0\n\ts_nop 0\n\tglobal_load_lds_dwordx4 %1, %2 sc1" :: "s"(lds), "v"(voff), "s"(sbase) : "memory", "m0");
}
__device__ __forceinline__ void dma_v(unsigned lds, const void* vaddr) {
    asm volatile("s_mov_b32 m0, %0\n\ts_nop 0\n\tglobal_load_lds_dwordx4 %1, off" :: "s"(lds), "v"(vaddr) : "memory", "m0");
}
#define WAITV(n) asm volatile("s_waitcnt vmcnt(" #n ")" ::: "memory")
#define BAR_L() asm volatile("s_waitcnt lgkmcnt(0)\n\ts_barrier" ::: "memory")
template <int N> __device__ __forceinline__ void bar_vl() { asm volatile("s_waitcnt vmcnt(%0) lgkmcnt(0)\n\ts_barrier" :: "n"(N) : "memory"); }

__device__ __forceinline__ u32x2 trd(unsigned a) { return __builtin_bit_cast(u32x2, __builtin_amdgcn_ds_read_tr16_b64_v4i16((AS3 s16x4*)(uintptr_t)a)); }
__device__ __forceinline__ u32x4 trd2(unsigned a0, unsigned a1) { const u32x2 p = trd(a0), q = trd(a1); return (u32x4){p[0], p[1], q[0], q[1]}; }

__device__ __forceinline__ u64 mix64(u64 z) { z += 0x9E3779B97F4A7C15ull; z = (z ^ (z >> 30)) * 0xBF58476D1CE4E5B9ull; z = (z ^ (z >> 27)) * 0x94D049BB133111EBull; return z ^ (z >> 31); }

constexpr unsigned SPIN_MAX = 1u << 20;
__device__ __forceinline__ void poll_flags(const char* ws, unsigned off, int n, u64 tag, int lane, int slp) {
    const gu64* p = (const gu64*)(ws + off) + (lane < n ? lane : 0);
    for (unsigned s = 0;; ++s) {
        const u64 v = __hip_atomic_load(p, __ATOMIC_RELAXED, __HIP_MEMORY_SCOPE_AGENT);
        if (__all(v == tag)) break;
        if (s > SPIN_MAX) { if (lane == 0) __hip_atomic_store((gu32*)(ws + WS_TMO), 0x100u + off, __ATOMIC_RELAXED, __HIP_MEMORY_SCOPE_AGENT); break; }
        if (slp) __builtin_amdgcn_s_sleep(8); else __builtin_amdgcn_s_sleep(1);
    }
    FENCE();
}
__device__ __forceinline__ void put_flag(char* ws, unsigned off, u64 tag) { __hip_atomic_store((gu64*)(ws + off), tag, __ATOMIC_RELAXED, __HIP_MEMORY_SCOPE_AGENT); }
__device__ __forceinline__ void lds_inc(unsigned a, int lane) { if (lane == 0) __hip_atomic_fetch_add((AS3 unsigned*)(uintptr_t)a, 1u, __ATOMIC_RELAXED, __HIP_MEMORY_SCOPE_WORKGROUP); }
template <int SLP> __device__ __forceinline__ void lds_wait(unsigned a, unsigned target, const char* ws) {
    for (unsigned s = 0;; ++s) {
        const unsigned v = uni(__hip_atomic_load((AS3 unsigned*)(uintptr_t)a, __ATOMIC_RELAXED, __HIP_MEMORY_SCOPE_WORKGROUP));
        if (v >= target) break;
        if (s > SPIN_MAX) { __hip_atomic_store((gu32*)(ws + WS_TMO), 0x10u + (a & 15), __ATOMIC_RELAXED, __HIP_MEMORY_SCOPE_AGENT); break; }
        __builtin_amdgcn_s_sleep(SLP);
    }
    FENCE();
}
__device__ __forceinline__ void async_bar(unsigned& gen, int lane, const char* ws) {
    asm volatile("s_waitcnt lgkmcnt(0)" ::: "memory");
    gen += 4; lds_inc(C_ASY, lane); lds_wait<1>(C_ASY, gen, ws);
}

__device__ __forceinline__ void features(const f32x16& a0, float p16, float p17, float p18, float p19, int lane, unsigned (&out)[18]) {
    const u32x4 b0 = {cvtpk(a0[0], a0[1]), cvtpk(a0[2], a0[3]), cvtpk(a0[4], a0[5]), cvtpk(a0[6], a0[7])};
    const u32x4 b1 = {cvtpk(a0[8], a0[9]), cvtpk(a0[10], a0[11]), cvtpk(a0[12], a0[13]), cvtpk(a0[14], a0[15])};
    const u32x2 b2 = {cvtpk(p16, p17), cvtpk(p18, p19)};
    const unsigned wa = L_WPHI + lane * 16, wr = L_WPHI + 2048 + lane * 8;
    f32x16 P0 = {}, P1 = {};
    P0 = MFMA(LA(u32x4, wa), b0, P0); P1 = MFMA(LA(u32x4, wa + 2560), b0, P1);
    P0 = MFMA(LA(u32x4, wa + 1024), b1, P0); P1 = MFMA(LA(u32x4, wa + 2560 + 1024), b1, P1);
    P0 = MFMA8(LA(u32x2, wr), b2, P0); P1 = MFMA8(LA(u32x2, wr + 2560), b2, P1);
    float p[18];
#pragma unroll
    for (int i = 0; i < 16; ++i) p[i] = P0[i];
    p[16] = P1[0]; p[17] = P1[1];
    float mx = p[0], mn = p[0];
#pragma unroll
    for (int i = 1; i < 18; ++i) { mx = __builtin_fmaxf(mx, p[i]); mn = __builtin_fminf(mn, p[i]); }
    const float L = 1.4426950408889634f, mxl = -mx * L, mnl = mn * L;
    float ep[18], en[18], sp = 0.f, sn = 0.f;
#pragma unroll
    for (int i = 0; i < 18; ++i) { ep[i] = EX(__builtin_fmaf(p[i], L, mxl)); en[i] = EX(__builtin_fmaf(p[i], -L, mnl)); sp += ep[i]; sn += en[i]; }
    const float rp = __builtin_amdgcn_rcpf(sp), rn = __builtin_amdgcn_rcpf(sn);
#pragma unroll
    for (int u = 0; u < 9; ++u) { out[u] = cvtpk(ep[2 * u] * rp, ep[2 * u + 1] * rp); out[9 + u] = cvtpk(en[2 * u] * rn, en[2 * u + 1] * rn); }
}

struct Ptrs { const char *x, *wqkv, *wphi, *woA, *wq, *wk, *wv, *woB, *gate; char *out, *ws; };

template <bool ISB>
__device__ __forceinline__ void role_main(const Ptrs& P, const int wid, const int lane) {
    const int b = blockIdx.x, x8 = b & 7, r = b >> 3;
    const int ti = 8 * (x8 & 1) + (r & 7), tj = 4 * (x8 >> 1) + (r >> 3);
    const int tb = wid & 3, n = lane & 31, hi = lane >> 5, n16 = lane & 15, g4 = lane >> 4;
    const int tid = wid * 64 + lane;
    char* const ws = P.ws;
    const __amdgpu_buffer_rsrc_t rws = __builtin_amdgcn_make_buffer_rsrc((void*)ws, (short)0, 0x1000000, 0x00020000);
    STAMP(0);
    u32x2 Ev; u32x4 xf0, xf1, xf2, gf0, gf1, gf2;
    {
        const int kw = 96 * (wid < 6 ? wid : 5) + 8 * g4;
        const unsigned xo = (unsigned)(128 * ti + 8 * tj + (n16 & 7)) * 1152u + kw * 2, go = kw * 2, zo = 0;
        asm volatile("global_load_dwordx2 %0, %7, %10 sc1\n\t"
                     "global_load_dwordx4 %1, %8, %11\n\tglobal_load_dwordx4 %2, %8, %11 offset:64\n\tglobal_load_dwordx4 %3, %8, %11 offset:128\n\t"
                     "global_load_dwordx4 %4, %9, %12\n\tglobal_load_dwordx4 %5, %9, %12 offset:64\n\tglobal_load_dwordx4 %6, %9, %12 offset:128"
                     : "=&v"(Ev), "=&v"(xf0), "=&v"(xf1), "=&v"(xf2), "=&v"(gf0), "=&v"(gf1), "=&v"(gf2)
                     : "v"(zo), "v"(xo), "v"(go), "s"(ws), "s"(P.x), "s"(P.gate) : "memory");
    }
#pragma unroll
    for (int t = 0; t < 5; ++t) {
        const int q = (wid + 8 * t) * 64 + lane;
        const char* src = q < 1296 ? P.wk + q * 16 : P.wv + (q - 1296) * 16;
        dma_v(L_WKV + (wid + 8 * t) * 1024, src);
    }
    if (wid == 0) { if (lane < 32) dma_v(L_WKV + 40 * 1024, P.wv + (40 * 64 + lane - 1296) * 16); }
    if (wid == 6) { dma_s(L_WPR, P.wphi + tj * 1296, lane * 16); if (lane < 17) dma_s(L_WPR + 1024, P.wphi + tj * 1296 + 1024, lane * 16); }
    if (tid == 0) { LW(u32x4, L_CNT) = (u32x4){0u, 0u, 0u, 0u}; }
    const int rot = (r + 3 * x8) % 9;
    constexpr int ND = ISB ? 4 : 5;
    unsigned xo[4]; const char* wb[5]; unsigned wst[5];
    if (ISB) {
#pragma unroll
        for (int i = 0; i < 4; ++i) xo[i] = (unsigned)(128 * ti + 32 * tb + 8 * i + (lane >> 3)) * 1152u + (((lane & 7) ^ (4 * (i & 1) + (lane >> 4))) * 16);
    } else {
        const int dl = 8 * (tj & 1);
#pragma unroll
        for (int i = 0; i < 5; ++i) {
            const int o = (5 * tb + i) * 1024 + lane * 16, row = o / 320, wi = o - row * 320, ty = wi / 80, pc = wi - ty * 80;
            const int col = 72 * tj - dl + pc;
            wb[i] = ty == 3 ? P.wq + row * 1152 + col : P.wqkv + row * 3456 + col + (ty == 0 ? 1152 : (ty == 1 ? 0 : 2304));
            wst[i] = ty == 3 ? 64 * 1152 : 64 * 3456;
        }
    }
    auto issue = [&](int c) {
        int kc = c + rot; if (kc >= 9) kc -= 9;
        const unsigned slot = (c & 3) * SLOT;
        if (ISB) {
            const char* xb = P.x + kc * 128;
#pragma unroll
            for (int i = 0; i < 4; ++i) dma_s(slot + (4 * tb + i) * 1024, xb, xo[i]);
        } else {
#pragma unroll
            for (int i = 0; i < 5; ++i) dma_v(slot + WOFF + (5 * tb + i) * 1024, wb[i] + (u64)kc * wst[i]);
        }
    };
    issue(0); issue(1); issue(2);
    asm volatile("s_waitcnt vmcnt(12) lgkmcnt(0)\n\ts_barrier" : "+v"(Ev), "+v"(xf0), "+v"(xf1), "+v"(xf2), "+v"(gf0), "+v"(gf1), "+v"(gf2) :: "memory");
    STAMP(1);
    const u64 tag = mix64(((u64)(unsigned)uni(Ev[1]) << 32) | (unsigned)uni(Ev[0]));
    u32x4 wf[5]; u32x2 dummy; (void)dummy;
    if (wid < 6) {
        f32x4 d0 = {}, d1 = {}, d2 = {}, d3 = {};
        const unsigned ak = L_WKV + (96 * wid + 8 * g4) * 36 + n16 * 2;
        const unsigned am = L_WKV + ((n16 & 2) ? 20736 : 0) + (96 * wid + 8 * g4) * 36 + 32 + (n16 & 1) * 2;
#pragma unroll
        for (int s = 0; s < 3; ++s) {
            u32x4 a0, a1, a2;
#pragma unroll
            for (int u = 0; u < 4; ++u) {
                const unsigned o = (32 * s + 2 * u) * 36;
                a0[u] = (unsigned)LA(unsigned short, ak + o) | ((unsigned)LA(unsigned short, ak + o + 36) << 16);
                a1[u] = (unsigned)LA(unsigned short, ak + 20736 + o) | ((unsigned)LA(unsigned short, ak + 20736 + o + 36) << 16);
                a2[u] = (unsigned)LA(unsigned short, am + o) | ((unsigned)LA(unsigned short, am + o + 36) << 16);
            }
            const u32x4 xf = s == 0 ? xf0 : (s == 1 ? xf1 : xf2), gf = s == 0 ? gf0 : (s == 1 ? gf1 : gf2);
            d0 = MFMA16(a0, xf, d0); d1 = MFMA16(a1, xf, d1); d2 = MFMA16(a2, xf, d2); d3 = MFMA16(gf, xf, d3);
        }
        const unsigned ra = L_RED + wid * 1280 + (n16 & 7) * 160;
        const bool up = n16 >= 8;
        LW(f32x4, ra + (up ? 64 : 0) + g4 * 16) = up ? d1 : d0;
        if (g4 == 0) LW(f32x4, ra + (up ? 144 : 128)) = up ? d3 : d2;
    } else if (wid == 6) {
        const int hm = (n >> 2) & 1;
#pragma unroll
        for (int mb = 0; mb < 2; ++mb) {
            const int f = 16 * mb + 4 * (n >> 3) + (n & 3);
            const bool vm = hm == hi && f < 18;
            unsigned v[20];
#pragma unroll
            for (int pos = 0; pos < 20; ++pos) {
                const int d = pos - 2 * hi; const bool ok = vm && d >= 0 && d < 18;
                const unsigned e = LA(unsigned short, L_WPR + (ok ? (hi * 324 + d * 18 + f) * 2 : 0));
                v[pos] = ok ? e : 0u;
            }
            wf[2 * mb] = (u32x4){v[0] | (v[1] << 16), v[2] | (v[3] << 16), v[4] | (v[5] << 16), v[6] | (v[7] << 16)};
            wf[2 * mb + 1] = (u32x4){v[8] | (v[9] << 16), v[10] | (v[11] << 16), v[12] | (v[13] << 16), v[14] | (v[15] << 16)};
            if (mb == 0) { wf[4][0] = v[16] | (v[17] << 16); wf[4][1] = v[18] | (v[19] << 16); } else { wf[4][2] = v[16] | (v[17] << 16); wf[4][3] = v[18] | (v[19] << 16); }
        }
    }
    BAR_L();
    STAMP(2);
    if (tid < 320) {
        float s = 0.f;
#pragma unroll
        for (int w = 0; w < 6; ++w) s += LA(float, L_RED + w * 1280 + tid * 4);
        LW(float, L_RES + tid * 4) = s;
    }
    f32x16 acc0 = {}, acc1 = {}, acc2 = {};
    unsigned wa0, wa1, wa2, xa;
    {
        const int i = lane & 15, sl = 2 * (g4 & 1) + ((i & 3) >> 1);
        const unsigned base = WOFF + (8 * (g4 >> 1) + (i >> 2)) * 320 + 8 * (tj & 1) + 32 * (i & 1);
        if (ISB) {
            wa0 = base + 8 * sl;
            wa1 = base + (sl == 0 ? 32 : 240 + 8 * (sl - 1));
            wa2 = base + (sl < 2 ? 240 + 24 + 8 * sl : 160 + 24 + 8 * (sl - 2));
        } else {
            wa0 = base + 80 + 8 * sl;
            wa1 = base + (sl == 0 ? 80 + 32 : 160 + 8 * (sl - 1));
            wa2 = 0;
        }
        xa = (32 * tb + n) * 128 + ((hi ^ ((n >> 1) & 7)) * 16);
    }
#pragma unroll
    for (int c = 0; c < 9; ++c) {
        if (c <= 6) bar_vl<2 * ND>(); else if (c == 7) bar_vl<ND>(); else bar_vl<0>();
        if (c == 0) STAMP(3);
        if (c == 4) STAMP(4);
        if (!ISB && c == 0) {
            if (wid == 7) {
                const int t = lane & 7, bi = tj >> 2, pc = tj & 3;
                const int rho = 16 * (pc >> 1) + 8 * (t >> 2) + 4 * (pc & 1) + (t & 3);
                const int d = lane - 16;
                unsigned base, strd;
                if (lane < 16) { base = t * 40 + 8 * (lane >> 3); strd = 1; }
                else if (lane < 40) { base = d < 16 ? 16 + d : (d < 18 ? 18 + d : 0); strd = 40; }
                else if (lane < 48) { base = t * 40 + 32; strd = 1; }
                else { base = t * 40 + 36; strd = 0; }
                float v[8];
#pragma unroll
                for (int i = 0; i < 8; ++i) v[i] = LA(float, L_RES + (base + i * strd) * 4);
                float ss = v[0] * v[0] + v[1] * v[1];
                float s6 = 0.f;
#pragma unroll
                for (int i = 2; i < 8; ++i) s6 += v[i] * v[i];
                if (lane < 16) ss += s6;
                u32x4 o = {cvtpk(v[0], v[1]), cvtpk(v[2], v[3]), cvtpk(v[4], v[5]), cvtpk(v[6], v[7])};
                if (lane == 34) o = (u32x4){0x3f803f80u, 0x3f803f80u, 0x3f803f80u, 0x3f803f80u};
                const unsigned img = WS_KVIMG + ti * STEP_BYTES;
                const unsigned o16 = lane < 16 ? img + bi * KB_BYTES + ((lane >> 3) * 32 + rho) * 16 : img + bi * KB_BYTES + 1024 + pc * 304 + d * 16;
                if (lane < 35) __builtin_amdgcn_raw_buffer_store_b128(o, rws, o16, 0, 16);
                float tot = __shfl(ss, t) + __shfl(ss, 8 + t) + __shfl(ss, 40 + t);
                tot = __builtin_fmaxf(tot, __shfl_xor(tot, 1)); tot = __builtin_fmaxf(tot, __shfl_xor(tot, 2)); tot = __builtin_fmaxf(tot, __shfl_xor(tot, 4));
                const float gv = __builtin_amdgcn_rcpf(1.f + EX(-1.4426950408889634f * v[0]));
                unsigned o4, d4;
                if (lane < 48) { o4 = img + KR_OFF + (bi >> 1) * 256 + rho * 8 + (bi & 1) * 4; d4 = o[0]; }
                else if (lane < 56) { o4 = WS_G + (128 * ti + 8 * tj + t) * 4; d4 = __float_as_uint(gv); }
                else { o4 = WS_KMAX + (16 * ti + tj) * 4; d4 = __float_as_uint(tot); }
                if (lane >= 40 && lane <= 56) __builtin_amdgcn_raw_buffer_store_b32(d4, rws, o4, 0, 16);
            } else if (wid == 6) {
                LW(u32x4, L_WPHI + lane * 16) = wf[0]; LW(u32x4, L_WPHI + 1024 + lane * 16) = wf[1];
                LW(u32x4, L_WPHI + 2560 + lane * 16) = wf[2]; LW(u32x4, L_WPHI + 2560 + 1024 + lane * 16) = wf[3];
                LW(u32x2, L_WPHI + 2048 + lane * 8) = (u32x2){wf[4][0], wf[4][1]}; LW(u32x2, L_WPHI + 2560 + 2048 + lane * 8) = (u32x2){wf[4][2], wf[4][3]};
            }
        }
        if (!ISB && c == 2) { if (wid == 7 && lane == 0) put_flag(ws, WS_FKV + (16 * ti + tj) * 8, tag); }
        if (c + 3 < 9) issue(c + 3);
        const unsigned slot = (c & 3) * SLOT;
#pragma unroll
        for (int ks = 0; ks < 4; ++ks) {
            const u32x4 xf = LA(u32x4, slot + (xa ^ (ks * 32)));
            const unsigned o = slot + ks * 5120;
            acc0 = MFMA(trd2(wa0 + o, wa0 + o + 1280), xf, acc0);
            acc1 = MFMA(trd2(wa1 + o, wa1 + o + 1280), xf, acc1);
            if (ISB) acc2 = MFMA(trd2(wa2 + o, wa2 + o + 1280), xf, acc2);
        }
    }
    STAMP(5);
    if (!ISB && wid == 7) {
#pragma unroll
        for (int q = 0; q < 4; ++q) poll_flags(ws, WS_FKV + q * 512, 64, tag, lane, 0);
        if (b == 0 && lane == 0) __hip_atomic_store((gu64*)ws, tag, __ATOMIC_RELAXED, __HIP_MEMORY_SCOPE_AGENT);
    }
    BAR_L();
    STAMP(6);
    const float gate = __uint_as_float(__builtin_amdgcn_raw_buffer_load_b32(rws, WS_G + (128 * ti + 32 * tb + n) * 4, 0, 16));
    const unsigned vrow = L_VB + (32 * tb + n) * 112 + hi * 52 + 8;

    if (ISB) {
        const u32x4 km4 = __builtin_bit_cast(u32x4, __builtin_amdgcn_raw_buffer_load_b128(rws, WS_KMAX + lane * 16, 0, 16));
        {
            unsigned ph[18];
            features(acc0, acc1[0], acc1[1], acc1[2], acc1[3], lane, ph);
            const unsigned pa = L_PHIK + (32 * tb + n) * 160 + hi * 80;
#pragma unroll
            for (int u = 0; u < 4; ++u) LW(u32x4, pa + 16 * u) = (u32x4){ph[4 * u], ph[4 * u + 1], ph[4 * u + 2], ph[4 * u + 3]};
            LW(u32x2, pa + 64) = (u32x2){ph[16], ph[17]};
            LW(unsigned, vrow + 24) = cvtpk(acc2[8], acc2[9]); LW(unsigned, vrow + 28) = cvtpk(acc2[10], acc2[11]);
            LW(unsigned, vrow + 32) = cvtpk(acc2[12], acc2[13]); LW(unsigned, vrow + 36) = cvtpk(acc2[14], acc2[15]);
            LW(unsigned, vrow + 36 + 4 * hi) = 0x00003f80u;
        }
        u32x4 QA, QB; u32x2 QRA0, QRA1, QRB0, QRB1; f32x16 MA, MB;
        {
            const float sc = 0.23570226039551584f * 1.4426950408889634f;
            float q[20];
#pragma unroll
            for (int i = 0; i < 12; ++i) q[i] = acc1[4 + i] * sc;
#pragma unroll
            for (int i = 0; i < 8; ++i) q[12 + i] = acc2[i] * sc;
            float n2 = hi ? q[18] * q[18] + q[19] * q[19] : q[0] * q[0] + q[1] * q[1];
#pragma unroll
            for (int i = 2; i < 18; ++i) n2 = __builtin_fmaf(q[i], q[i], n2);
            unsigned cc[10], D[9];
#pragma unroll
            for (int u = 0; u < 10; ++u) cc[u] = cvtpk(q[2 * u], q[2 * u + 1]);
#pragma unroll
            for (int u = 0; u < 9; ++u) D[u] = hi ? cc[u + 1] : cc[u];
#pragma unroll
            for (int u = 0; u < 4; ++u) { const auto sw = __builtin_amdgcn_permlane32_swap(D[u], D[u + 4], false, false); QA[u] = sw[0]; QB[u] = sw[1]; }
            QRA0 = (u32x2){hi ? 0u : D[8], 0u}; QRA1 = (u32x2){0u, hi ? 0u : D[8]};
            QRB0 = (u32x2){hi ? D[8] : 0u, 0u}; QRB1 = (u32x2){0u, hi ? D[8] : 0u};
            SBAR();
            float km = __builtin_fmaxf(__builtin_fmaxf(__uint_as_float(km4[0]), __uint_as_float(km4[1])), __builtin_fmaxf(__uint_as_float(km4[2]), __uint_as_float(km4[3])));
#pragma unroll
            for (int o = 1; o < 64; o <<= 1) km = __builtin_fmaxf(km, __shfl_xor(km, o));
            const float m = -1.01f * __builtin_sqrtf(n2 * km);
            const auto sw = __builtin_amdgcn_permlane32_swap(__float_as_uint(m), __float_as_uint(m), false, false);
            const float ma = __uint_as_float(sw[0]), mb = __uint_as_float(sw[1]);
#pragma unroll
            for (int i = 0; i < 16; ++i) { MA[i] = ma; MB[i] = mb; }
            PIN(MA); PIN(MB); PIN(QA); PIN(QB); PIN(QRA0); PIN(QRA1); PIN(QRB0); PIN(QRB1);
        }
        BAR_L();
        STAMP(7);
        unsigned kp, rp, vp;
        f32x16 OA, OB, S0A, S0B, S1A, S1B;
        u32x4 KMa, KMb, V0a, V0b, V1a, V1b, P0, P1; u32x2 KRX, KRY;
        float e0, e1, e2, e3, f0, f1, f2, f3;
#pragma unroll 1
        for (int pass = 0; pass < NPASS; ++pass) {
        kp = lane * 16; rp = KR_OFF + n * 8; vp = 1024 + hi * 304 + (n < 18 ? n : 18) * 16;
        PIN(kp); PIN(rp); PIN(vp);
        OA = (f32x16){}; OB = (f32x16){}; V1b = (u32x4){}; P0 = (u32x4){}; P1 = (u32x4){}; f0 = f1 = f2 = f3 = 0.f;
        KRX = LA(u32x2, rp); KMa = LA(u32x4, kp);
        S0A = MFMA(KMa, QA, MA); S0B = MFMA(KMa, QB, MB); S0A = MFMA8(KRX, QRA0, S0A); S0B = MFMA8(KRX, QRB0, S0B);
        SBAR();
        KMb = LA(u32x4, kp + KB_BYTES); V0a = LA(u32x4, vp); V1a = LA(u32x4, vp + 608);
        __builtin_amdgcn_s_waitcnt(0xC07F);
        SBAR();
#define LD(dst, T, p) dst = LA(T, p)
#define NOP_ (void)0
#define SLOTB(MF, POST, d0, d1, d2, d3, S, g, Pq, i, a0, a1, a2, a3) do { MF; SBAR(); POST; SBAR(); \
        d0 = EX(S[4 * (g)]); d1 = EX(S[4 * (g) + 1]); PIN(d0); PIN(d1); SBAR(); \
        Pq[i] = cvtpk(a0, a1); Pq[i + 1] = cvtpk(a2, a3); PIN(Pq); SBAR(); \
        d2 = EX(S[4 * (g) + 2]); d3 = EX(S[4 * (g) + 3]); PIN(d2); PIN(d3); SBAR(); } while (0)
#define E_ e0, e1, e2, e3
#define F_ f0, f1, f2, f3
#define SLOT_(...) SLOTB(__VA_ARGS__)
#define WAITL __builtin_amdgcn_s_waitcnt(0xC07F)
#define PERIOD(SA, SB, NA, NB, KMu, KMl, KRu, QRAx, QRBx, V0u, V1u, V0l, V1l, vb, kb, PA3) do { \
        SLOT_(NA = MFMA(KMu, QA, MA),     LD(KMl, u32x4, kp + (kb)),       E_, SA, 0, P1, 2, F_); \
        SLOT_(OB = MFMA(V1l, P1, OB),     LD(V1l, u32x4, vp + (vb) + 608), F_, SA, 1, P0, 0, E_); \
        SLOT_(NA = MFMA8(KRu, QRAx, NA),  LD(V0l, u32x4, vp + (vb)),       E_, SA, 2, P0, 2, F_); \
        SLOT_(OA = MFMA(V0u, P0, OA),     PA3,                             F_, SA, 3, P1, 0, E_); \
        SLOT_(NB = MFMA(KMu, QB, MB),     NOP_,                            E_, SB, 0, P1, 2, F_); \
        SLOT_(OA = MFMA(V1u, P1, OA),     NOP_,                            F_, SB, 1, P0, 0, E_); \
        SLOT_(NB = MFMA8(KRu, QRBx, NB),  WAITL,                           E_, SB, 2, P0, 2, F_); \
        SLOT_(OB = MFMA(V0u, P0, OB),     NOP_,                            F_, SB, 3, P1, 0, E_); \
    } while (0)
#pragma unroll 1
        for (int seg = 0; seg < (pass == NPASS - 1 ? 3 : 1); ++seg) {
            if (seg == 1) { lds_inc(C_BPROG, lane); STAMP(8); }
            if (seg == 2) lds_wait<1>(C_LAND, 4, ws);
            const int ns = seg == 0 ? 9 : (seg == 1 ? 2 : 5);
#pragma unroll 1
            for (int s = 0; s < ns; ++s) {
                PERIOD(S0A, S0B, S1A, S1B, KMb, KMa, KRX, QRA1, QRB1, V0a, V1a, V0b, V1b, KB_BYTES, 2 * KB_BYTES, LD(KRY, u32x2, rp + 256));
                PERIOD(S1A, S1B, S0A, S0B, KMa, KMb, KRY, QRA0, QRB0, V0b, V1b, V0a, V1a, 2 * KB_BYTES, 3 * KB_BYTES, NOP_);
                PERIOD(S0A, S0B, S1A, S1B, KMb, KMa, KRY, QRA1, QRB1, V0a, V1a, V0b, V1b, 3 * KB_BYTES, STEP_BYTES, LD(KRX, u32x2, rp + STEP_BYTES));
                PERIOD(S1A, S1B, S0A, S0B, KMa, KMb, KRX, QRA0, QRB0, V0b, V1b, V0a, V1a, STEP_BYTES, STEP_BYTES + KB_BYTES, NOP_);
                kp += STEP_BYTES; vp += STEP_BYTES; rp += STEP_BYTES; PIN(kp); PIN(vp); PIN(rp); SBAR();
            }
        }
        PIN(OA); PIN(OB);
        }
        P1[2] = cvtpk(f0, f1); P1[3] = cvtpk(f2, f3); PIN(P1); SBAR();
        OB = MFMA(V1b, P1, OB);
        STAMP(10);
        if (__any(!(OA[10] >= 0x1p-80f) || !(OB[10] >= 0x1p-80f))) {
            const unsigned vo = WS_KVIMG + 1024 + hi * 304 + (n < 18 ? n : 18) * 16;
#pragma unroll 1
            for (int which = 0; which < 2; ++which) {
                const u32x4 Q = which ? QB : QA; const u32x2 R0 = which ? QRB0 : QRA0, R1 = which ? QRB1 : QRA1;
                float mx = -__builtin_inff();
#pragma unroll 1
                for (int kb = 0; kb < 64; ++kb) {
                    const unsigned bo = (kb >> 2) * STEP_BYTES;
                    const u32x4 km = __builtin_bit_cast(u32x4, __builtin_amdgcn_raw_buffer_load_b128(rws, WS_KVIMG + bo + (kb & 3) * KB_BYTES + lane * 16, 0, 16));
                    const u32x2 kr = __builtin_bit_cast(u32x2, __builtin_amdgcn_raw_buffer_load_b64(rws, WS_KVIMG + bo + KR_OFF + ((kb >> 1) & 1) * 256 + n * 8, 0, 16));
                    f32x16 S = {}; S = MFMA(km, Q, S); S = MFMA8(kr, (kb & 1) ? R1 : R0, S);
#pragma unroll
                    for (int i = 0; i < 16; ++i) mx = __builtin_fmaxf(mx, S[i]);
                }
                { const auto sw = __builtin_amdgcn_permlane32_swap(__float_as_uint(mx), __float_as_uint(mx), false, false); mx = __builtin_fmaxf(__uint_as_float(sw[0]), __uint_as_float(sw[1])); }
                f32x16 O = {}, C;
#pragma unroll
                for (int i = 0; i < 16; ++i) C[i] = -mx;
#pragma unroll 1
                for (int kb = 0; kb < 64; ++kb) {
                    const unsigned bo = (kb >> 2) * STEP_BYTES, bb = bo + (kb & 3) * KB_BYTES;
                    const u32x4 km = __builtin_bit_cast(u32x4, __builtin_amdgcn_raw_buffer_load_b128(rws, WS_KVIMG + bb + lane * 16, 0, 16));
                    const u32x2 kr = __builtin_bit_cast(u32x2, __builtin_amdgcn_raw_buffer_load_b64(rws, WS_KVIMG + bo + KR_OFF + ((kb >> 1) & 1) * 256 + n * 8, 0, 16));
                    const u32x4 v0 = __builtin_bit_cast(u32x4, __builtin_amdgcn_raw_buffer_load_b128(rws, vo + bb, 0, 16));
                    const u32x4 v1 = __builtin_bit_cast(u32x4, __builtin_amdgcn_raw_buffer_load_b128(rws, vo + bb + 608, 0, 16));
                    f32x16 S = MFMA(km, Q, C); S = MFMA8(kr, (kb & 1) ? R1 : R0, S);
                    float p[16];
#pragma unroll
                    for (int i = 0; i < 16; ++i) p[i] = EX(S[i]);
                    const u32x4 p0 = {cvtpk(p[0], p[1]), cvtpk(p[2], p[3]), cvtpk(p[4], p[5]), cvtpk(p[6], p[7])};
                    const u32x4 p1 = {cvtpk(p[8], p[9]), cvtpk(p[10], p[11]), cvtpk(p[12], p[13]), cvtpk(p[14], p[15])};
                    O = MFMA(v0, p0, O); O = MFMA(v1, p1, O);
                }
                if (which) OB = O; else OA = O;
            }
        }
        {
            const float ga = (1.f - gate) * __builtin_amdgcn_rcpf(OA[10]), gb = (1.f - gate) * __builtin_amdgcn_rcpf(OB[10]);
            const unsigned fo = WS_OBF + (unsigned)(4 * ti + tb) * 36 * 1024;
            const int h0 = 2 * tj;
            const u32x4 fa = {cvtpk(OA[0] * ga, OA[1] * ga), cvtpk(OA[2] * ga, OA[3] * ga), cvtpk(OA[4] * ga, OA[5] * ga), cvtpk(OA[6] * ga, OA[7] * ga)};
            const u32x4 fb = {cvtpk(OB[0] * gb, OB[1] * gb), cvtpk(OB[2] * gb, OB[3] * gb), cvtpk(OB[4] * gb, OB[5] * gb), cvtpk(OB[6] * gb, OB[7] * gb)};
            __builtin_amdgcn_raw_buffer_store_b128(fa, rws, fo + h0 * 1024 + lane * 16, 0, 16);
            __builtin_amdgcn_raw_buffer_store_b128(fb, rws, fo + (h0 + 1) * 1024 + lane * 16, 0, 16);
            const u32x2 rr = {cvtpk(OA[8] * ga, OA[9] * ga), cvtpk(OB[8] * gb, OB[9] * gb)};
            if (!hi) __builtin_amdgcn_raw_buffer_store_b64(rr, rws, fo + (32 + (tj >> 2)) * 1024 + (((tj >> 1) & 1) * 32 + n) * 16 + (tj & 1) * 8, 0, 16);
            WAITV(0);
            unsigned arrived = 0;
            if (lane == 0) arrived = __hip_atomic_fetch_add((AS3 unsigned*)(uintptr_t)C_BDONE, 1u, __ATOMIC_RELAXED, __HIP_MEMORY_SCOPE_WORKGROUP);
            if (lane == 0 && arrived == 3) put_flag(ws, WS_FOB + (16 * ti + tj) * 8, tag);
        }
        STAMP(11);
    } else {
        for (int p = tb; p < 111; p += 4) dma_s_sc1(p * 1024, ws + WS_KVIMG + p * 1024, lane * 16);
#pragma unroll
        for (int u = 0; u < 6; ++u) LW(unsigned, vrow + 4 * u) = cvtpk(acc1[4 + 2 * u], acc1[5 + 2 * u]);
        bar_vl<0>();
        STAMP(7);
        __builtin_amdgcn_s_setprio(2);
        unsigned gen = 0;
        {
            const int hh = tb & 1, i = lane & 15;
            const int tok = 8 * g4 + (i >> 2), gr = i & 3;
            const unsigned va = L_VB + tok * 112 + hh * 56 + 8 + gr * 8;
            const int ntask = tb < 2 ? 2 : 1;
#pragma unroll 1
            for (int t = 0; t < ntask; ++t) {
                const int mbf = tb < 2 ? 2 * t : 1;
                const unsigned pa = L_PHIK + tok * 160 + hh * 80 + mbf * 32 + gr * 8;
                f32x4 d0 = {}, d1 = {};
#pragma unroll
                for (int ks = 0; ks < 4; ++ks) {
                    const u32x4 a = trd2(pa + ks * 32 * 160, pa + ks * 32 * 160 + 4 * 160);
                    const u32x4 b0 = trd2(va + ks * 32 * 112, va + ks * 32 * 112 + 4 * 112);
                    const u32x4 b1 = trd2(va + ks * 32 * 112 + 32, va + ks * 32 * 112 + 4 * 112 + 32);
                    d0 = MFMA16(a, b0, d0); d1 = MFMA16(a, b1, d1);
                }
                const unsigned ko = WS_KVP + (unsigned)(16 * tj + ti) * 5472 + hh * 2736 + (16 * mbf + 4 * g4) * 4;
                if (mbf < 2 || g4 == 0) {
                    __builtin_amdgcn_raw_buffer_store_b128(__builtin_bit_cast(u32x4, d0), rws, ko + n16 * 144, 0, 16);
                    if (n16 < 3) __builtin_amdgcn_raw_buffer_store_b128(__builtin_bit_cast(u32x4, d1), rws, ko + (16 + n16) * 144, 0, 16);
                }
            }
        }
        WAITV(0);
        async_bar(gen, lane, ws);
        if (tb == 0 && lane == 0) put_flag(ws, WS_FKVP + (16 * tj + ti) * 8, tag);
        STAMP(8);
        for (int p = 111 + tb; p < 148; p += 4) dma_s_sc1(p * 1024, ws + WS_KVIMG + p * 1024, lane * 16);
        unsigned pq[18];
        features(acc0, acc1[0], acc1[1], acc1[2], acc1[3], lane, pq);
        WAITV(0);
        lds_inc(C_LAND, lane);
        poll_flags(ws, WS_FKVP + 16 * tj * 8, 16, tag, lane, 1);
        STAMP(9);
        {
            const int at = tb * 64 + lane;
#pragma unroll 1
            for (int rd = 0; rd < 2; ++rd) {
                const int it = rd == 0 ? at : 256 + tb * 22 + lane;
                const bool ok = rd == 0 || (lane < 22 && it < 342);
                const unsigned ko = WS_KVP + (unsigned)(16 * tj) * 5472 + (ok ? it : 0) * 16;
                f32x4 v[16];
#pragma unroll
                for (int i = 0; i < 16; ++i) v[i] = __builtin_bit_cast(f32x4, __builtin_amdgcn_raw_buffer_load_b128(rws, ko + i * 5472, 0, 16));
                f32x4 s = v[0];
#pragma unroll
                for (int i = 1; i < 16; ++i) s += v[i];
                const int hh = it >= 171, rem = it - 171 * hh, d = rem / 9, fq = rem - 9 * d;
                if (ok) LW(u32x2, L_KVT + hh * 1600 + d * 80 + fq * 8) = (u32x2){cvtpk(s[0], s[1]), cvtpk(s[2], s[3])};
            }
            if (tb == 3 && lane < 10) LW(u32x4, L_KVT + (lane >= 5) * 1600 + 19 * 80 + (lane % 5) * 16) = (u32x4){0u, 0u, 0u, 0u};
        }
        async_bar(gen, lane, ws);
        {
            const int hm = (n >> 2) & 1, d0 = 4 * (n >> 3) + (n & 3);
            const unsigned a0 = L_KVT + (hm == hi ? hm * 1600 + d0 * 80 : 19 * 80);
            const unsigned a1 = L_KVT + (hm == hi && d0 < 3 ? hm * 1600 + (16 + d0) * 80 : 19 * 80);
            f32x16 N0 = {}, N1 = {};
#pragma unroll
            for (int s = 0; s < 4; ++s) {
                const u32x4 bq = {pq[4 * s], pq[4 * s + 1], pq[4 * s + 2], pq[4 * s + 3]};
                N0 = MFMA(LA(u32x4, a0 + 16 * s), bq, N0); N1 = MFMA(LA(u32x4, a1 + 16 * s), bq, N1);
            }
            const u32x2 br = {pq[16], pq[17]};
            N0 = MFMA8(LA(u32x2, a0 + 64), br, N0); N1 = MFMA8(LA(u32x2, a1 + 64), br, N1);
            const float sc = gate / (N1[2] + 1e-6f);
            const unsigned fo = WS_OAF + (unsigned)(4 * ti + tb) * 36 * 1024;
            const u32x4 f0 = {cvtpk(N0[0] * sc, N0[1] * sc), cvtpk(N0[2] * sc, N0[3] * sc), cvtpk(N0[4] * sc, N0[5] * sc), cvtpk(N0[6] * sc, N0[7] * sc)};
            const u32x4 f1 = {cvtpk(N0[8] * sc, N0[9] * sc), cvtpk(N0[10] * sc, N0[11] * sc), cvtpk(N0[12] * sc, N0[13] * sc), cvtpk(N0[14] * sc, N0[15] * sc)};
            __builtin_amdgcn_raw_buffer_store_b128(f0, rws, fo + (2 * tj) * 1024 + lane * 16, 0, 16);
            __builtin_amdgcn_raw_buffer_store_b128(f1, rws, fo + (2 * tj + 1) * 1024 + lane * 16, 0, 16);
            __builtin_amdgcn_raw_buffer_store_b32(cvtpk(N1[0] * sc, N1[1] * sc), rws, fo + (32 + (tj >> 2)) * 1024 + (((tj >> 1) & 1) * 32 + n) * 16 + (2 * (tj & 1) + hi) * 4, 0, 16);
        }
        WAITV(0);
        async_bar(gen, lane, ws);
        if (tb == 0 && lane == 0) put_flag(ws, WS_FOA + (16 * ti + tj) * 8, tag);
        STAMP(10);
        const int tt = 4 * x8 + (r & 3), cg = r >> 2;
        unsigned wo[21];
        (void)wo;
        auto stage = [&](const char* w) {
            const char* wsl = w + cg * 144;
#pragma unroll 1
            for (int p = tb; p < 81; p += 4) {
                const int q = p * 64 + lane, row = q / 9, pc = q - row * 9;
                dma_s(p * 1024, wsl, row * 1152 + pc * 16);
            }
        };
        const int i16 = lane & 15, gr = i16 & 3, hk = g4 & 1, fh = g4 >> 1;
        const unsigned fro = (unsigned)(2 * tt + (tb >> 1)) * 36 * 1024 + fh * 1024 + (hk * 32 + n16 + 16 * (tb & 1)) * 16;
        const unsigned remo = (18 * (8 * fh + 4 * hk + (i16 >> 3)) + 16 + ((i16 >> 2) & 1)) * 144 + gr * 8;
        f32x4 T[5] = {};
        auto gemm = [&](unsigned fbase, unsigned maino, unsigned r4s) {
            u32x4 bf[18];
#pragma unroll
            for (int ks = 0; ks < 18; ++ks) bf[ks] = __builtin_bit_cast(u32x4, __builtin_amdgcn_raw_buffer_load_b128(rws, fbase + fro + ks * 2048, 0, 16));
#pragma unroll
            for (int ks = 0; ks < 18; ++ks) {
                const unsigned a = ks < 16 ? maino + ks * 36 * 144 : remo + (ks - 16) * 16 * 18 * 144;
                const unsigned s2 = ks < 16 ? r4s : 2 * 18 * 144;
#pragma unroll
                for (int cb = 0; cb < 5; ++cb) T[cb] = MFMA16(trd2(a + cb * 32, a + cb * 32 + s2), bf[ks], T[cb]);
            }
        };
        lds_wait<16>(C_BPROG, 4, ws);
        stage(P.woA);
        WAITV(0);
        async_bar(gen, lane, ws);
        STAMP(11);
        poll_flags(ws, WS_FOA + 16 * (tt >> 1) * 8, 16, tag, lane, 1);
        gemm(WS_OAF, (18 * hk + 8 * fh + (i16 >> 2)) * 144 + gr * 8, 4 * 144);
        async_bar(gen, lane, ws);
        STAMP(12);
        stage(P.woB);
        WAITV(0);
        async_bar(gen, lane, ws);
        STAMP(13);
        poll_flags(ws, WS_FOB + 16 * (tt >> 1) * 8, 16, tag, lane, 0);
        STAMP(14);
        gemm(WS_OBF, (18 * fh + 4 * hk + (i16 >> 2)) * 144 + gr * 8, 8 * 144);
        char* op = P.out + (size_t)(64 * tt + 16 * tb + n16) * 1152 + cg * 144 + g4 * 8;
#pragma unroll
        for (int cb = 0; cb < 5; ++cb) {
            const u32x2 o = {cvtpk(T[cb][0], T[cb][1]), cvtpk(T[cb][2], T[cb][3])};
            if (cb < 4 || g4 < 2) *(u32x2*)(op + cb * 32) = o;
        }
    }
}

__global__ __launch_bounds__(512, 2) void gated_mix_kernel(const void* x, const void* wqkv, const void* wphi, const void* woA, const void* wq, const void* wk,
                                                           const void* wv, const void* woB, const void* gate, void* out, void* ws) {
    extern __shared__ __attribute__((aligned(16))) char lds[];
    const int lane = threadIdx.x & 63, wid = uni(threadIdx.x >> 6);
    const Ptrs P = {(const char*)x, (const char*)wqkv, (const char*)wphi, (const char*)woA, (const char*)wq, (const char*)wk, (const char*)wv, (const char*)woB, (const char*)gate, (char*)out, (char*)ws};
    if (wid < 4) role_main<true>(P, wid, lane); else role_main<false>(P, wid, lane);
}
}

extern "C" void kernel_launch(void* const* d_in, const int* in_sizes, int n_in, void* d_out, int out_size, void* d_ws, size_t ws_size, hipStream_t stream) {
    (void)in_sizes; (void)n_in; (void)out_size; (void)ws_size;
    (void)hipFuncSetAttribute((const void*)fk::gated_mix_kernel, hipFuncAttributeMaxDynamicSharedMemorySize, fk::LDS_BYTES);
    hipLaunchKernelGGL(fk::gated_mix_kernel, dim3(256), dim3(512), fk::LDS_BYTES, stream,
                       (const void*)d_in[0], (const void*)d_in[1], (const void*)d_in[2], (const void*)d_in[3], (const void*)d_in[5], (const void*)d_in[6],
                       (const void*)d_in[7], (const void*)d_in[8], (const void*)d_in[9], d_out, d_ws);
}
```

```cpp
#define LAT 0
#include <hip/hip_runtime.h>
#include <hip/hip_bf16.h>
#include <stdint.h>

namespace fk {
typedef __bf16 bf16x2 __attribute__((ext_vector_type(2)));
typedef __bf16 bf16x8 __attribute__((ext_vector_type(8)));
typedef short s16x4 __attribute__((ext_vector_type(4)));
typedef float f32x2 __attribute__((ext_vector_type(2)));
typedef float f32x4 __attribute__((ext_vector_type(4)));
typedef float f32x16 __attribute__((ext_vector_type(16)));
typedef unsigned u32x2 __attribute__((ext_vector_type(2)));
typedef unsigned u32x4 __attribute__((ext_vector_type(4)));
typedef unsigned long long u64;
typedef __attribute__((address_space(1))) u64 gu64;
typedef __attribute__((address_space(1))) unsigned gu32;

#define AS3 __attribute__((address_space(3)))
#define SBAR() __builtin_amdgcn_sched_barrier(0)
#define PIN(x) asm volatile("" : "+v"(x))
#define MFMA(a, b, c) __builtin_amdgcn_mfma_f32_32x32x16_bf16(__builtin_bit_cast(bf16x8, a), __builtin_bit_cast(bf16x8, b), c, 0, 0, 0)
#define MFMA8(a, b, c) __builtin_amdgcn_mfma_f32_32x32x8bf16_1k(__builtin_bit_cast(s16x4, a), __builtin_bit_cast(s16x4, b), c, 0, 0, 0)
#define MFMA16(a, b, c) __builtin_amdgcn_mfma_f32_16x16x32_bf16(__builtin_bit_cast(bf16x8, a), __builtin_bit_cast(bf16x8, b), c, 0, 0, 0)
#define LA(T, a) (*(const AS3 T*)(uintptr_t)(a))
#define LW(T, a) (*(AS3 T*)(uintptr_t)(a))
#define FENCE() asm volatile("" ::: "memory")
#define EX(x) __builtin_amdgcn_exp2f(x)

#define WS_TMO 64
constexpr unsigned WS_FKV = 4096, WS_FKVP = 8192, WS_FOA = 12288, WS_FOB = 16384;
#define WS_G 32768
#define WS_KMAX 40960
#define WS_KVIMG 65536
#define WS_KVP 262144
#define WS_OAF (2 << 20)
#define WS_OBF (5 << 20)

constexpr unsigned SLOT = 36864, WOFF = 16384;
constexpr unsigned L_WKV = 110592;
constexpr unsigned L_RED = 152064, L_RES = 159744;
constexpr unsigned L_CNT = 161024, L_WPR = 161152;
constexpr unsigned L_WPHI = 152064, L_KVT = 157184;
constexpr unsigned L_PHIK = 113664, L_VB = 134144;
constexpr unsigned KB_BYTES = 2240, STEP_BYTES = 9472, KR_OFF = 8960;
constexpr unsigned LDS_BYTES = 163840;
constexpr unsigned C_LAND = L_CNT, C_ASY = L_CNT + 4, C_BPROG = L_CNT + 8, C_BDONE = L_CNT + 12;

#define STAMP(k) do { if (tb == 0 && lane == 0) LW(unsigned, 162560 + (ISB ? 64 : 0) + 4 * (k)) = (unsigned)__builtin_amdgcn_s_memrealtime(); } while (0)

__device__ __forceinline__ unsigned cvtpk(float lo, float hi) { f32x2 v = {lo, hi}; return __builtin_bit_cast(unsigned, __builtin_convertvector(v, bf16x2)); }
__device__ __forceinline__ int uni(int v) { return __builtin_amdgcn_readfirstlane(v); }

__device__ __forceinline__ void dma_s(unsigned lds, const void* sbase, unsigned voff) {
    asm volatile("s_mov_b32 m0, %0\n\ts_nop 0\n\tglobal_load_lds_dwordx4 %1, %2" :: "s"(lds), "v"(voff), "s"(sbase) : "memory", "m0");
}
__device__ __forceinline__ void dma_s_sc1(unsigned lds, const void* sbase, unsigned voff) {
    asm volatile("s_mov_b32 m0, %0\n\ts_nop 0\n\tglobal_load_lds_dwordx4 %1, %2 sc1" :: "s"(lds), "v"(voff), "s"(sbase) : "memory", "m0");
}
__device__ __forceinline__ void dma_v(unsigned lds, const void* vaddr) {
    asm volatile("s_mov_b32 m0, %0\n\ts_nop 0\n\tglobal_load_lds_dwordx4 %1, off" :: "s"(lds), "v"(vaddr) : "memory", "m0");
}
#define WAITV(n) asm volatile("s_waitcnt vmcnt(" #n ")" ::: "memory")
#define BAR_L() asm volatile("s_waitcnt lgkmcnt(0)\n\ts_barrier" ::: "memory")
template <int N> __device__ __forceinline__ void bar_vl() { asm volatile("s_waitcnt vmcnt(%0) lgkmcnt(0)\n\ts_barrier" :: "n"(N) : "memory"); }

__device__ __forceinline__ u32x2 trd(unsigned a) { return __builtin_bit_cast(u32x2, __builtin_amdgcn_ds_read_tr16_b64_v4i16((AS3 s16x4*)(uintptr_t)a)); }
__device__ __forceinline__ u32x4 trd2(unsigned a0, unsigned a1) { const u32x2 p = trd(a0), q = trd(a1); return (u32x4){p[0], p[1], q[0], q[1]}; }

__device__ __forceinline__ u64 mix64(u64 z) { z += 0x9E3779B97F4A7C15ull; z = (z ^ (z >> 30)) * 0xBF58476D1CE4E5B9ull; z = (z ^ (z >> 27)) * 0x94D049BB133111EBull; return z ^ (z >> 31); }

constexpr unsigned SPIN_MAX = 1u << 20;
__device__ __forceinline__ void poll_flags(const char* ws, unsigned off, int n, u64 tag, int lane, int slp) {
    const gu64* p = (const gu64*)(ws + off) + (lane < n ? lane : 0);
    for (unsigned s = 0;; ++s) {
        const u64 v = __hip_atomic_load(p, __ATOMIC_RELAXED, __HIP_MEMORY_SCOPE_AGENT);
        if (__all(v == tag)) break;
        if (s > SPIN_MAX) { if (lane == 0) __hip_atomic_store((gu32*)(ws + WS_TMO), 0x100u + off, __ATOMIC_RELAXED, __HIP_MEMORY_SCOPE_AGENT); break; }
        if (slp) __builtin_amdgcn_s_sleep(8); else __builtin_amdgcn_s_sleep(1);
    }
    FENCE();
}
__device__ __forceinline__ void put_flag(char* ws, unsigned off, u64 tag) { __hip_atomic_store((gu64*)(ws + off), tag, __ATOMIC_RELAXED, __HIP_MEMORY_SCOPE_AGENT); }
__device__ __forceinline__ void lds_inc(unsigned a, int lane) { if (lane == 0) __hip_atomic_fetch_add((AS3 unsigned*)(uintptr_t)a, 1u, __ATOMIC_RELAXED, __HIP_MEMORY_SCOPE_WORKGROUP); }
template <int SLP> __device__ __forceinline__ void lds_wait(unsigned a, unsigned target, const char* ws) {
    for (unsigned s = 0;; ++s) {
        const unsigned v = uni(__hip_atomic_load((AS3 unsigned*)(uintptr_t)a, __ATOMIC_RELAXED, __HIP_MEMORY_SCOPE_WORKGROUP));
        if (v >= target) break;
        if (s > SPIN_MAX) { __hip_atomic_store((gu32*)(ws + WS_TMO), 0x10u + (a & 15), __ATOMIC_RELAXED, __HIP_MEMORY_SCOPE_AGENT); break; }
        __builtin_amdgcn_s_sleep(SLP);
    }
    FENCE();
}
__device__ __forceinline__ void async_bar(unsigned& gen, int lane, const char* ws) {
    asm volatile("s_waitcnt lgkmcnt(0)" ::: "memory");
    gen += 4; lds_inc(C_ASY, lane); lds_wait<1>(C_ASY, gen, ws);
}

__device__ __forceinline__ void features(const f32x16& a0, float p16, float p17, float p18, float p19, int lane, unsigned (&out)[18]) {
    const u32x4 b0 = {cvtpk(a0[0], a0[1]), cvtpk(a0[2], a0[3]), cvtpk(a0[4], a0[5]), cvtpk(a0[6], a0[7])};
    const u32x4 b1 = {cvtpk(a0[8], a0[9]), cvtpk(a0[10], a0[11]), cvtpk(a0[12], a0[13]), cvtpk(a0[14], a0[15])};
    const u32x2 b2 = {cvtpk(p16, p17), cvtpk(p18, p19)};
    const unsigned wa = L_WPHI + lane * 16, wr = L_WPHI + 2048 + lane * 8;
    f32x16 P0 = {}, P1 = {};
    P0 = MFMA(LA(u32x4, wa), b0, P0); P1 = MFMA(LA(u32x4, wa + 2560), b0, P1);
    P0 = MFMA(LA(u32x4, wa + 1024), b1, P0); P1 = MFMA(LA(u32x4, wa + 2560 + 1024), b1, P1);
    P0 = MFMA8(LA(u32x2, wr), b2, P0); P1 = MFMA8(LA(u32x2, wr + 2560), b2, P1);
    float p[18];
#pragma unroll
    for (int i = 0; i < 16; ++i) p[i] = P0[i];
    p[16] = P1[0]; p[17] = P1[1];
    float mx = p[0], mn = p[0];
#pragma unroll
    for (int i = 1; i < 18; ++i) { mx = __builtin_fmaxf(mx, p[i]); mn = __builtin_fminf(mn, p[i]); }
    const float L = 1.4426950408889634f, mxl = -mx * L, mnl = mn * L;
    float ep[18], en[18], sp = 0.f, sn = 0.f;
#pragma unroll
    for (int i = 0; i < 18; ++i) { ep[i] = EX(__builtin_fmaf(p[i], L, mxl)); en[i] = EX(__builtin_fmaf(p[i], -L, mnl)); sp += ep[i]; sn += en[i]; }
    const float rp = __builtin_amdgcn_rcpf(sp), rn = __builtin_amdgcn_rcpf(sn);
#pragma unroll
    for (int u = 0; u < 9; ++u) { out[u] = cvtpk(ep[2 * u] * rp, ep[2 * u + 1] * rp); out[9 + u] = cvtpk(en[2 * u] * rn, en[2 * u + 1] * rn); }
}

struct Ptrs { const char *x, *wqkv, *wphi, *woA, *wq, *wk, *wv, *woB, *gate; char *out, *ws; };

template <bool ISB>
__device__ __forceinline__ void role_main(const Ptrs& P, const int wid, const int lane) {
    const int b = blockIdx.x, x8 = b & 7, r = b >> 3;
    const int ti = 8 * (x8 & 1) + (r & 7), tj = 4 * (x8 >> 1) + (r >> 3);
    const int tb = wid & 3, n = lane & 31, hi = lane >> 5, n16 = lane & 15, g4 = lane >> 4;
    const int tid = wid * 64 + lane;
    char* const ws = P.ws;
    const __amdgpu_buffer_rsrc_t rws = __builtin_amdgcn_make_buffer_rsrc((void*)ws, (short)0, 0x1000000, 0x00020000);
    STAMP(0);
    u32x2 Ev; u32x4 xf0, xf1, xf2, gf0, gf1, gf2;
    {
        const int kw = 96 * (wid < 6 ? wid : 5) + 8 * g4;
        const unsigned xo = (unsigned)(128 * ti + 8 * tj + (n16 & 7)) * 1152u + kw * 2, go = kw * 2, zo = 0;
        asm volatile("global_load_dwordx2 %0, %7, %10 sc1\n\t"
                     "global_load_dwordx4 %1, %8, %11\n\tglobal_load_dwordx4 %2, %8, %11 offset:64\n\tglobal_load_dwordx4 %3, %8, %11 offset:128\n\t"
                     "global_load_dwordx4 %4, %9, %12\n\tglobal_load_dwordx4 %5, %9, %12 offset:64\n\tglobal_load_dwordx4 %6, %9, %12 offset:128"
                     : "=&v"(Ev), "=&v"(xf0), "=&v"(xf1), "=&v"(xf2), "=&v"(gf0), "=&v"(gf1), "=&v"(gf2)
                     : "v"(zo), "v"(xo), "v"(go), "s"(ws), "s"(P.x), "s"(P.gate) : "memory");
    }
#pragma unroll
    for (int t = 0; t < 5; ++t) {
        const int q = (wid + 8 * t) * 64 + lane;
        const char* src = q < 1296 ? P.wk + q * 16 : P.wv + (q - 1296) * 16;
        dma_v(L_WKV + (wid + 8 * t) * 1024, src);
    }
    if (wid == 0) { if (lane < 32) dma_v(L_WKV + 40 * 1024, P.wv + (40 * 64 + lane - 1296) * 16); }
    if (wid == 6) { dma_s(L_WPR, P.wphi + tj * 1296, lane * 16); if (lane < 17) dma_s(L_WPR + 1024, P.wphi + tj * 1296 + 1024, lane * 16); }
    if (tid == 0) { LW(u32x4, L_CNT) = (u32x4){0u, 0u, 0u, 0u}; }
    const int rot = (r + 3 * x8) % 9;
    constexpr int ND = ISB ? 4 : 5;
    unsigned xo[4]; const char* wb[5]; unsigned wst[5];
    if (ISB) {
#pragma unroll
        for (int i = 0; i < 4; ++i) xo[i] = (unsigned)(128 * ti + 32 * tb + 8 * i + (lane >> 3)) * 1152u + (((lane & 7) ^ (4 * (i & 1) + (lane >> 4))) * 16);
    } else {
        const int dl = 8 * (tj & 1);
#pragma unroll
        for (int i = 0; i < 5; ++i) {
            const int o = (5 * tb + i) * 1024 + lane * 16, row = o / 320, wi = o - row * 320, ty = wi / 80, pc = wi - ty * 80;
            const int col = 72 * tj - dl + pc;
            wb[i] = ty == 3 ? P.wq + row * 1152 + col : P.wqkv + row * 3456 + col + (ty == 0 ? 1152 : (ty == 1 ? 0 : 2304));
            wst[i] = ty == 3 ? 64 * 1152 : 64 * 3456;
        }
    }
    auto issue = [&](int c) {
        int kc = c + rot; if (kc >= 9) kc -= 9;
        const unsigned slot = (c & 3) * SLOT;
        if (ISB) {
            const char* xb = P.x + kc * 128;
#pragma unroll
            for (int i = 0; i < 4; ++i) dma_s(slot + (4 * tb + i) * 1024, xb, xo[i]);
        } else {
#pragma unroll
            for (int i = 0; i < 5; ++i) dma_v(slot + WOFF + (5 * tb + i) * 1024, wb[i] + (u64)kc * wst[i]);
        }
    };
    issue(0); issue(1); issue(2);
    asm volatile("s_waitcnt vmcnt(12) lgkmcnt(0)\n\ts_barrier" : "+v"(Ev), "+v"(xf0), "+v"(xf1), "+v"(xf2), "+v"(gf0), "+v"(gf1), "+v"(gf2) :: "memory");
    STAMP(1);
    const u64 tag = mix64(((u64)(unsigned)uni(Ev[1]) << 32) | (unsigned)uni(Ev[0]));
    u32x4 wf[5]; u32x2 dummy; (void)dummy;
    if (wid < 6) {
        f32x4 d0 = {}, d1 = {}, d2 = {}, d3 = {};
        const unsigned ak = L_WKV + (96 * wid + 8 * g4) * 36 + n16 * 2;
        const unsigned am = L_WKV + ((n16 & 2) ? 20736 : 0) + (96 * wid + 8 * g4) * 36 + 32 + (n16 & 1) * 2;
#pragma unroll
        for (int s = 0; s < 3; ++s) {
            u32x4 a0, a1, a2;
#pragma unroll
            for (int u = 0; u < 4; ++u) {
                const unsigned o = (32 * s + 2 * u) * 36;
                a0[u] = (unsigned)LA(unsigned short, ak + o) | ((unsigned)LA(unsigned short, ak + o + 36) << 16);
                a1[u] = (unsigned)LA(unsigned short, ak + 20736 + o) | ((unsigned)LA(unsigned short, ak + 20736 + o + 36) << 16);
                a2[u] = (unsigned)LA(unsigned short, am + o) | ((unsigned)LA(unsigned short, am + o + 36) << 16);
            }
            const u32x4 xf = s == 0 ? xf0 : (s == 1 ? xf1 : xf2), gf = s == 0 ? gf0 : (s == 1 ? gf1 : gf2);
            d0 = MFMA16(a0, xf, d0); d1 = MFMA16(a1, xf, d1); d2 = MFMA16(a2, xf, d2); d3 = MFMA16(gf, xf, d3);
        }
        const unsigned ra = L_RED + wid * 1280 + (n16 & 7) * 160;
        const bool up = n16 >= 8;
        LW(f32x4, ra + (up ? 64 : 0) + g4 * 16) = up ? d1 : d0;
        if (g4 == 0) LW(f32x4, ra + (up ? 144 : 128)) = up ? d3 : d2;
    } else if (wid == 6) {
        const int hm = (n >> 2) & 1;
#pragma unroll
        for (int mb = 0; mb < 2; ++mb) {
            const int f = 16 * mb + 4 * (n >> 3) + (n & 3);
            const bool vm = hm == hi && f < 18;
            unsigned v[20];
#pragma unroll
            for (int pos = 0; pos < 20; ++pos) {
                const int d = pos - 2 * hi; const bool ok = vm && d >= 0 && d < 18;
                const unsigned e = LA(unsigned short, L_WPR + (ok ? (hi * 324 + d * 18 + f) * 2 : 0));
                v[pos] = ok ? e : 0u;
            }
            wf[2 * mb] = (u32x4){v[0] | (v[1] << 16), v[2] | (v[3] << 16), v[4] | (v[5] << 16), v[6] | (v[7] << 16)};
            wf[2 * mb + 1] = (u32x4){v[8] | (v[9] << 16), v[10] | (v[11] << 16), v[12] | (v[13] << 16), v[14] | (v[15] << 16)};
            if (mb == 0) { wf[4][0] = v[16] | (v[17] << 16); wf[4][1] = v[18] | (v[19] << 16); } else { wf[4][2] = v[16] | (v[17] << 16); wf[4][3] = v[18] | (v[19] << 16); }
        }
    }
    BAR_L();
    if (tid < 320) {
        float s = 0.f;
#pragma unroll
        for (int w = 0; w < 6; ++w) s += LA(float, L_RED + w * 1280 + tid * 4);
        LW(float, L_RES + tid * 4) = s;
    }
    f32x16 acc0 = {}, acc1 = {}, acc2 = {};
    unsigned wa0, wa1, wa2, xa;
    {
        const int i = lane & 15, sl = 2 * (g4 & 1) + ((i & 3) >> 1);
        const unsigned base = WOFF + (8 * (g4 >> 1) + (i >> 2)) * 320 + 8 * (tj & 1) + 32 * (i & 1);
        if (ISB) {
            wa0 = base + 8 * sl;
            wa1 = base + (sl == 0 ? 32 : 240 + 8 * (sl - 1));
            wa2 = base + (sl < 2 ? 240 + 24 + 8 * sl : 160 + 24 + 8 * (sl - 2));
        } else {
            wa0 = base + 80 + 8 * sl;
            wa1 = base + (sl == 0 ? 80 + 32 : 160 + 8 * (sl - 1));
            wa2 = 0;
        }
        xa = (32 * tb + n) * 128 + ((hi ^ ((n >> 1) & 7)) * 16);
    }
#pragma unroll
    for (int c = 0; c < 9; ++c) {
        if (c <= 6) bar_vl<2 * ND>(); else if (c == 7) bar_vl<ND>(); else bar_vl<0>();
        if (c == 0) STAMP(3);
        if (c == 4) STAMP(4);
        if (!ISB && c == 0) {
            if (wid == 7) {
                const int t = lane & 7, bi = tj >> 2, pc = tj & 3;
                const int rho = 16 * (pc >> 1) + 8 * (t >> 2) + 4 * (pc & 1) + (t & 3);
                const int d = lane - 16;
                unsigned base, strd;
                if (lane < 16) { base = t * 40 + 8 * (lane >> 3); strd = 1; }
                else if (lane < 40) { base = d < 16 ? 16 + d : (d < 18 ? 18 + d : 0); strd = 40; }
                else if (lane < 48) { base = t * 40 + 32; strd = 1; }
                else { base = t * 40 + 36; strd = 0; }
                float v[8];
#pragma unroll
                for (int i = 0; i < 8; ++i) v[i] = LA(float, L_RES + (base + i * strd) * 4);
                float ss = v[0] * v[0] + v[1] * v[1];
                float s6 = 0.f;
#pragma unroll
                for (int i = 2; i < 8; ++i) s6 += v[i] * v[i];
                if (lane < 16) ss += s6;
                u32x4 o = {cvtpk(v[0], v[1]), cvtpk(v[2], v[3]), cvtpk(v[4], v[5]), cvtpk(v[6], v[7])};
                if (lane == 34) o = (u32x4){0x3f803f80u, 0x3f803f80u, 0x3f803f80u, 0x3f803f80u};
                const unsigned img = WS_KVIMG + ti * STEP_BYTES;
                const unsigned o16 = lane < 16 ? img + bi * KB_BYTES + ((lane >> 3) * 32 + rho) * 16 : img + bi * KB_BYTES + 1024 + pc * 304 + d * 16;
                if (lane < 35) __builtin_amdgcn_raw_buffer_store_b128(o, rws, o16, 0, 16);
                float tot = __shfl(ss, t) + __shfl(ss, 8 + t) + __shfl(ss, 40 + t);
                tot = __builtin_fmaxf(tot, __shfl_xor(tot, 1)); tot = __builtin_fmaxf(tot, __shfl_xor(tot, 2)); tot = __builtin_fmaxf(tot, __shfl_xor(tot, 4));
                const float gv = __builtin_amdgcn_rcpf(1.f + EX(-1.4426950408889634f * v[0]));
                unsigned o4, d4;
                if (lane < 48) { o4 = img + KR_OFF + (bi >> 1) * 256 + rho * 8 + (bi & 1) * 4; d4 = o[0]; }
                else if (lane < 56) { o4 = WS_G + (128 * ti + 8 * tj + t) * 4; d4 = __float_as_uint(gv); }
                else { o4 = WS_KMAX + (16 * ti + tj) * 4; d4 = __float_as_uint(tot); }
                if (lane >= 40 && lane <= 56) __builtin_amdgcn_raw_buffer_store_b32(d4, rws, o4, 0, 16);
            } else if (wid == 6) {
                LW(u32x4, L_WPHI + lane * 16) = wf[0]; LW(u32x4, L_WPHI + 1024 + lane * 16) = wf[1];
                LW(u32x4, L_WPHI + 2560 + lane * 16) = wf[2]; LW(u32x4, L_WPHI + 2560 + 1024 + lane * 16) = wf[3];
                LW(u32x2, L_WPHI + 2048 + lane * 8) = (u32x2){wf[4][0], wf[4][1]}; LW(u32x2, L_WPHI + 2560 + 2048 + lane * 8) = (u32x2){wf[4][2], wf[4][3]};
            }
        }
        if (!ISB && c == 2) { if (wid == 7 && lane == 0) put_flag(ws, WS_FKV + (16 * ti + tj) * 8, tag); }
        if (c + 3 < 9) issue(c + 3);
        const unsigned slot = (c & 3) * SLOT;
#pragma unroll
        for (int ks = 0; ks < 4; ++ks) {
            const u32x4 xf = LA(u32x4, slot + (xa ^ (ks * 32)));
            const unsigned o = slot + ks * 5120;
            acc0 = MFMA(trd2(wa0 + o, wa0 + o + 1280), xf, acc0);
            acc1 = MFMA(trd2(wa1 + o, wa1 + o + 1280), xf, acc1);
            if (ISB) acc2 = MFMA(trd2(wa2 + o, wa2 + o + 1280), xf, acc2);
        }
    }
    STAMP(5);
    if (!ISB && wid == 7) {
#pragma unroll
        for (int q = 0; q < 4; ++q) poll_flags(ws, WS_FKV + q * 512, 64, tag, lane, 0);
        if (b == 0 && lane == 0) __hip_atomic_store((gu64*)ws, tag, __ATOMIC_RELAXED, __HIP_MEMORY_SCOPE_AGENT);
    }
    BAR_L();
    STAMP(6);
    const float gate = __uint_as_float(__builtin_amdgcn_raw_buffer_load_b32(rws, WS_G + (128 * ti + 32 * tb + n) * 4, 0, 16));
    const unsigned vrow = L_VB + (32 * tb + n) * 112 + hi * 52 + 8;

    if (ISB) {
        const u32x4 km4 = __builtin_bit_cast(u32x4, __builtin_amdgcn_raw_buffer_load_b128(rws, WS_KMAX + lane * 16, 0, 16));
        {
            unsigned ph[18];
            features(acc0, acc1[0], acc1[1], acc1[2], acc1[3], lane, ph);
            const unsigned pa = L_PHIK + (32 * tb + n) * 160 + hi * 80;
#pragma unroll
            for (int u = 0; u < 4; ++u) LW(u32x4, pa + 16 * u) = (u32x4){ph[4 * u], ph[4 * u + 1], ph[4 * u + 2], ph[4 * u + 3]};
            LW(u32x2, pa + 64) = (u32x2){ph[16], ph[17]};
            LW(unsigned, vrow + 24) = cvtpk(acc2[8], acc2[9]); LW(unsigned, vrow + 28) = cvtpk(acc2[10], acc2[11]);
            LW(unsigned, vrow + 32) = cvtpk(acc2[12], acc2[13]); LW(unsigned, vrow + 36) = cvtpk(acc2[14], acc2[15]);
            LW(unsigned, vrow + 36 + 4 * hi) = 0x00003f80u;
        }
        u32x4 QA, QB; u32x2 QRA0, QRA1, QRB0, QRB1; f32x16 MA, MB;
        {
            const float sc = 0.23570226039551584f * 1.4426950408889634f;
            float q[20];
#pragma unroll
            for (int i = 0; i < 12; ++i) q[i] = acc1[4 + i] * sc;
#pragma unroll
            for (int i = 0; i < 8; ++i) q[12 + i] = acc2[i] * sc;
            float n2 = hi ? q[18] * q[18] + q[19] * q[19] : q[0] * q[0] + q[1] * q[1];
#pragma unroll
            for (int i = 2; i < 18; ++i) n2 = __builtin_fmaf(q[i], q[i], n2);
            unsigned cc[10], D[9];
#pragma unroll
            for (int u = 0; u < 10; ++u) cc[u] = cvtpk(q[2 * u], q[2 * u + 1]);
#pragma unroll
            for (int u = 0; u < 9; ++u) D[u] = hi ? cc[u + 1] : cc[u];
#pragma unroll
            for (int u = 0; u < 4; ++u) { const auto sw = __builtin_amdgcn_permlane32_swap(D[u], D[u + 4], false, false); QA[u] = sw[0]; QB[u] = sw[1]; }
            QRA0 = (u32x2){hi ? 0u : D[8], 0u}; QRA1 = (u32x2){0u, hi ? 0u : D[8]};
            QRB0 = (u32x2){hi ? D[8] : 0u, 0u}; QRB1 = (u32x2){0u, hi ? D[8] : 0u};
            SBAR();
            float km = __builtin_fmaxf(__builtin_fmaxf(__uint_as_float(km4[0]), __uint_as_float(km4[1])), __builtin_fmaxf(__uint_as_float(km4[2]), __uint_as_float(km4[3])));
#pragma unroll
            for (int o = 1; o < 64; o <<= 1) km = __builtin_fmaxf(km, __shfl_xor(km, o));
            const float m = -1.01f * __builtin_sqrtf(n2 * km);
            const auto sw = __builtin_amdgcn_permlane32_swap(__float_as_uint(m), __float_as_uint(m), false, false);
            const float ma = __uint_as_float(sw[0]), mb = __uint_as_float(sw[1]);
#pragma unroll
            for (int i = 0; i < 16; ++i) { MA[i] = ma; MB[i] = mb; }
            PIN(MA); PIN(MB); PIN(QA); PIN(QB); PIN(QRA0); PIN(QRA1); PIN(QRB0); PIN(QRB1);
        }
        BAR_L();
        STAMP(7);
        unsigned kp = lane * 16, rp = KR_OFF + n * 8, vp = 1024 + hi * 304 + (n < 18 ? n : 18) * 16;
        PIN(kp); PIN(rp); PIN(vp);
        f32x16 OA = {}, OB = {}, S0A, S0B, S1A, S1B;
        u32x4 KMa, KMb, V0a, V0b, V1a, V1b = {}, P0 = {}, P1 = {}; u32x2 KRX, KRY;
        float e0, e1, e2, e3, f0 = 0.f, f1 = 0.f, f2 = 0.f, f3 = 0.f;
        KRX = LA(u32x2, rp); KMa = LA(u32x4, kp);
        S0A = MFMA(KMa, QA, MA); S0B = MFMA(KMa, QB, MB); S0A = MFMA8(KRX, QRA0, S0A); S0B = MFMA8(KRX, QRB0, S0B);
        SBAR();
        KMb = LA(u32x4, kp + KB_BYTES); V0a = LA(u32x4, vp); V1a = LA(u32x4, vp + 608);
        __builtin_amdgcn_s_waitcnt(0xC07F);
        SBAR();
#define LD(dst, T, p) dst = LA(T, p)
#define NOP_ (void)0
#define SLOTB(MF, POST, d0, d1, d2, d3, S, g, Pq, i, a0, a1, a2, a3) do { MF; SBAR(); POST; SBAR(); \
        d0 = EX(S[4 * (g)]); d1 = EX(S[4 * (g) + 1]); PIN(d0); PIN(d1); SBAR(); \
        Pq[i] = cvtpk(a0, a1); Pq[i + 1] = cvtpk(a2, a3); PIN(Pq); SBAR(); \
        d2 = EX(S[4 * (g) + 2]); d3 = EX(S[4 * (g) + 3]); PIN(d2); PIN(d3); SBAR(); } while (0)
#define E_ e0, e1, e2, e3
#define F_ f0, f1, f2, f3
#define SLOT_(...) SLOTB(__VA_ARGS__)
#define WAITL __builtin_amdgcn_s_waitcnt(0xC07F)
#define PERIOD(SA, SB, NA, NB, KMu, KMl, KRu, QRAx, QRBx, V0u, V1u, V0l, V1l, vb, kb, PA3) do { \
        SLOT_(NA = MFMA(KMu, QA, MA),     LD(KMl, u32x4, kp + (kb)),       E_, SA, 0, P1, 2, F_); \
        SLOT_(OB = MFMA(V1l, P1, OB),     LD(V1l, u32x4, vp + (vb) + 608), F_, SA, 1, P0, 0, E_); \
        SLOT_(NA = MFMA8(KRu, QRAx, NA),  LD(V0l, u32x4, vp + (vb)),       E_, SA, 2, P0, 2, F_); \
        SLOT_(OA = MFMA(V0u, P0, OA),     PA3,                             F_, SA, 3, P1, 0, E_); \
        SLOT_(NB = MFMA(KMu, QB, MB),     NOP_,                            E_, SB, 0, P1, 2, F_); \
        SLOT_(OA = MFMA(V1u, P1, OA),     NOP_,                            F_, SB, 1, P0, 0, E_); \
        SLOT_(NB = MFMA8(KRu, QRBx, NB),  WAITL,                           E_, SB, 2, P0, 2, F_); \
        SLOT_(OB = MFMA(V0u, P0, OB),     NOP_,                            F_, SB, 3, P1, 0, E_); \
    } while (0)
#pragma unroll 1
        for (int seg = 0; seg < 3; ++seg) {
            if (seg == 1) { lds_inc(C_BPROG, lane); STAMP(8); }
            if (seg == 2) lds_wait<1>(C_LAND, 4, ws);
            const int ns = seg == 0 ? 9 : (seg == 1 ? 2 : 5);
#pragma unroll 1
            for (int s = 0; s < ns; ++s) {
                PERIOD(S0A, S0B, S1A, S1B, KMb, KMa, KRX, QRA1, QRB1, V0a, V1a, V0b, V1b, KB_BYTES, 2 * KB_BYTES, LD(KRY, u32x2, rp + 256));
                PERIOD(S1A, S1B, S0A, S0B, KMa, KMb, KRY, QRA0, QRB0, V0b, V1b, V0a, V1a, 2 * KB_BYTES, 3 * KB_BYTES, NOP_);
                PERIOD(S0A, S0B, S1A, S1B, KMb, KMa, KRY, QRA1, QRB1, V0a, V1a, V0b, V1b, 3 * KB_BYTES, STEP_BYTES, LD(KRX, u32x2, rp + STEP_BYTES));
                PERIOD(S1A, S1B, S0A, S0B, KMa, KMb, KRX, QRA0, QRB0, V0b, V1b, V0a, V1a, STEP_BYTES, STEP_BYTES + KB_BYTES, NOP_);
                kp += STEP_BYTES; vp += STEP_BYTES; rp += STEP_BYTES; PIN(kp); PIN(vp); PIN(rp); SBAR();
            }
        }
        P1[2] = cvtpk(f0, f1); P1[3] = cvtpk(f2, f3); PIN(P1); SBAR();
        OB = MFMA(V1b, P1, OB);
        STAMP(10);
        if (__any(!(OA[10] >= 0x1p-80f) || !(OB[10] >= 0x1p-80f))) {
            const unsigned vo = WS_KVIMG + 1024 + hi * 304 + (n < 18 ? n : 18) * 16;
#pragma unroll 1
            for (int which = 0; which < 2; ++which) {
                const u32x4 Q = which ? QB : QA; const u32x2 R0 = which ? QRB0 : QRA0, R1 = which ? QRB1 : QRA1;
                float mx = -__builtin_inff();
#pragma unroll 1
                for (int kb = 0; kb < 64; ++kb) {
                    const unsigned bo = (kb >> 2) * STEP_BYTES;
                    const u32x4 km = __builtin_bit_cast(u32x4, __builtin_amdgcn_raw_buffer_load_b128(rws, WS_KVIMG + bo + (kb & 3) * KB_BYTES + lane * 16, 0, 16));
                    const u32x2 kr = __builtin_bit_cast(u32x2, __builtin_amdgcn_raw_buffer_load_b64(rws, WS_KVIMG + bo + KR_OFF + ((kb >> 1) & 1) * 256 + n * 8, 0, 16));
                    f32x16 S = {}; S = MFMA(km, Q, S); S = MFMA8(kr, (kb & 1) ? R1 : R0, S);
#pragma unroll
                    for (int i = 0; i < 16; ++i) mx = __builtin_fmaxf(mx, S[i]);
                }
                { const auto sw = __builtin_amdgcn_permlane32_swap(__float_as_uint(mx), __float_as_uint(mx), false, false); mx = __builtin_fmaxf(__uint_as_float(sw[0]), __uint_as_float(sw[1])); }
                f32x16 O = {}, C;
#pragma unroll
                for (int i = 0; i < 16; ++i) C[i] = -mx;
#pragma unroll 1
                for (int kb = 0; kb < 64; ++kb) {
                    const unsigned bo = (kb >> 2) * STEP_BYTES, bb = bo + (kb & 3) * KB_BYTES;
                    const u32x4 km = __builtin_bit_cast(u32x4, __builtin_amdgcn_raw_buffer_load_b128(rws, WS_KVIMG + bb + lane * 16, 0, 16));
                    const u32x2 kr = __builtin_bit_cast(u32x2, __builtin_amdgcn_raw_buffer_load_b64(rws, WS_KVIMG + bo + KR_OFF + ((kb >> 1) & 1) * 256 + n * 8, 0, 16));
                    const u32x4 v0 = __builtin_bit_cast(u32x4, __builtin_amdgcn_raw_buffer_load_b128(rws, vo + bb, 0, 16));
                    const u32x4 v1 = __builtin_bit_cast(u32x4, __builtin_amdgcn_raw_buffer_load_b128(rws, vo + bb + 608, 0, 16));
                    f32x16 S = MFMA(km, Q, C); S = MFMA8(kr, (kb & 1) ? R1 : R0, S);
                    float p[16];
#pragma unroll
                    for (int i = 0; i < 16; ++i) p[i] = EX(S[i]);
                    const u32x4 p0 = {cvtpk(p[0], p[1]), cvtpk(p[2], p[3]), cvtpk(p[4], p[5]), cvtpk(p[6], p[7])};
                    const u32x4 p1 = {cvtpk(p[8], p[9]), cvtpk(p[10], p[11]), cvtpk(p[12], p[13]), cvtpk(p[14], p[15])};
                    O = MFMA(v0, p0, O); O = MFMA(v1, p1, O);
                }
                if (which) OB = O; else OA = O;
            }
        }
        {
            const float ga = (1.f - gate) * __builtin_amdgcn_rcpf(OA[10]), gb = (1.f - gate) * __builtin_amdgcn_rcpf(OB[10]);
            const unsigned fo = WS_OBF + (unsigned)(4 * ti + tb) * 36 * 1024;
            const int h0 = 2 * tj;
            const u32x4 fa = {cvtpk(OA[0] * ga, OA[1] * ga), cvtpk(OA[2] * ga, OA[3] * ga), cvtpk(OA[4] * ga, OA[5] * ga), cvtpk(OA[6] * ga, OA[7] * ga)};
            const u32x4 fb = {cvtpk(OB[0] * gb, OB[1] * gb), cvtpk(OB[2] * gb, OB[3] * gb), cvtpk(OB[4] * gb, OB[5] * gb), cvtpk(OB[6] * gb, OB[7] * gb)};
            __builtin_amdgcn_raw_buffer_store_b128(fa, rws, fo + h0 * 1024 + lane * 16, 0, 16);
            __builtin_amdgcn_raw_buffer_store_b128(fb, rws, fo + (h0 + 1) * 1024 + lane * 16, 0, 16);
            const u32x2 rr = {cvtpk(OA[8] * ga, OA[9] * ga), cvtpk(OB[8] * gb, OB[9] * gb)};
            if (!hi) __builtin_amdgcn_raw_buffer_store_b64(rr, rws, fo + (32 + (tj >> 2)) * 1024 + (((tj >> 1) & 1) * 32 + n) * 16 + (tj & 1) * 8, 0, 16);
            WAITV(0);
            unsigned arrived = 0;
            if (lane == 0) arrived = __hip_atomic_fetch_add((AS3 unsigned*)(uintptr_t)C_BDONE, 1u, __ATOMIC_RELAXED, __HIP_MEMORY_SCOPE_WORKGROUP);
            if (lane == 0 && arrived == 3) put_flag(ws, WS_FOB + (16 * ti + tj) * 8, tag);
        }
        STAMP(11);
    } else {
        for (int p = tb; p < 111; p += 4) dma_s_sc1(p * 1024, ws + WS_KVIMG + p * 1024, lane * 16);
#pragma unroll
        for (int u = 0; u < 6; ++u) LW(unsigned, vrow + 4 * u) = cvtpk(acc1[4 + 2 * u], acc1[5 + 2 * u]);
        bar_vl<0>();
        STAMP(7);
        __builtin_amdgcn_s_setprio(2);
        unsigned gen = 0;
        {
            const int hh = tb & 1, i = lane & 15;
            const int tok = 8 * g4 + (i >> 2), gr = i & 3;
            const unsigned va = L_VB + tok * 112 + hh * 56 + 8 + gr * 8;
            const int ntask = tb < 2 ? 2 : 1;
#pragma unroll 1
            for (int t = 0; t < ntask; ++t) {
                const int mbf = tb < 2 ? 2 * t : 1;
                const unsigned pa = L_PHIK + tok * 160 + hh * 80 + mbf * 32 + gr * 8;
                f32x4 d0 = {}, d1 = {};
#pragma unroll
                for (int ks = 0; ks < 4; ++ks) {
                    const u32x4 a = trd2(pa + ks * 32 * 160, pa + ks * 32 * 160 + 4 * 160);
                    const u32x4 b0 = trd2(va + ks * 32 * 112, va + ks * 32 * 112 + 4 * 112);
                    const u32x4 b1 = trd2(va + ks * 32 * 112 + 32, va + ks * 32 * 112 + 4 * 112 + 32);
                    d0 = MFMA16(a, b0, d0); d1 = MFMA16(a, b1, d1);
                }
                const unsigned ko = WS_KVP + (unsigned)(16 * tj + ti) * 5472 + hh * 2736 + (16 * mbf + 4 * g4) * 4;
                if (mbf < 2 || g4 == 0) {
                    __builtin_amdgcn_raw_buffer_store_b128(__builtin_bit_cast(u32x4, d0), rws, ko + n16 * 144, 0, 16);
                    if (n16 < 3) __builtin_amdgcn_raw_buffer_store_b128(__builtin_bit_cast(u32x4, d1), rws, ko + (16 + n16) * 144, 0, 16);
                }
            }
        }
        WAITV(0);
        async_bar(gen, lane, ws);
        if (tb == 0 && lane == 0) put_flag(ws, WS_FKVP + (16 * tj + ti) * 8, tag);
        STAMP(8);
        for (int p = 111 + tb; p < 148; p += 4) dma_s_sc1(p * 1024, ws + WS_KVIMG + p * 1024, lane * 16);
        unsigned pq[18];
        features(acc0, acc1[0], acc1[1], acc1[2], acc1[3], lane, pq);
        WAITV(0);
        lds_inc(C_LAND, lane);
        poll_flags(ws, WS_FKVP + 16 * tj * 8, 16, tag, lane, 1);
        STAMP(9);
        {
            const int at = tb * 64 + lane;
#pragma unroll 1
            for (int rd = 0; rd < 2; ++rd) {
                const int it = rd == 0 ? at : 256 + tb * 22 + lane;
                const bool ok = rd == 0 || (lane < 22 && it < 342);
                const unsigned ko = WS_KVP + (unsigned)(16 * tj) * 5472 + (ok ? it : 0) * 16;
                f32x4 v[16];
#pragma unroll
                for (int i = 0; i < 16; ++i) v[i] = __builtin_bit_cast(f32x4, __builtin_amdgcn_raw_buffer_load_b128(rws, ko + i * 5472, 0, 16));
                f32x4 s = v[0];
#pragma unroll
                for (int i = 1; i < 16; ++i) s += v[i];
                const int hh = it >= 171, rem = it - 171 * hh, d = rem / 9, fq = rem - 9 * d;
                if (ok) LW(u32x2, L_KVT + hh * 1600 + d * 80 + fq * 8) = (u32x2){cvtpk(s[0], s[1]), cvtpk(s[2], s[3])};
            }
            if (tb == 3 && lane < 10) LW(u32x4, L_KVT + (lane >= 5) * 1600 + 19 * 80 + (lane % 5) * 16) = (u32x4){0u, 0u, 0u, 0u};
        }
        async_bar(gen, lane, ws);
        {
            const int hm = (n >> 2) & 1, d0 = 4 * (n >> 3) + (n & 3);
            const unsigned a0 = L_KVT + (hm == hi ? hm * 1600 + d0 * 80 : 19 * 80);
            const unsigned a1 = L_KVT + (hm == hi && d0 < 3 ? hm * 1600 + (16 + d0) * 80 : 19 * 80);
            f32x16 N0 = {}, N1 = {};
#pragma unroll
            for (int s = 0; s < 4; ++s) {
                const u32x4 bq = {pq[4 * s], pq[4 * s + 1], pq[4 * s + 2], pq[4 * s + 3]};
                N0 = MFMA(LA(u32x4, a0 + 16 * s), bq, N0); N1 = MFMA(LA(u32x4, a1 + 16 * s), bq, N1);
            }
            const u32x2 br = {pq[16], pq[17]};
            N0 = MFMA8(LA(u32x2, a0 + 64), br, N0); N1 = MFMA8(LA(u32x2, a1 + 64), br, N1);
            const float sc = gate / (N1[2] + 1e-6f);
            const unsigned fo = WS_OAF + (unsigned)(4 * ti + tb) * 36 * 1024;
            const u32x4 f0 = {cvtpk(N0[0] * sc, N0[1] * sc), cvtpk(N0[2] * sc, N0[3] * sc), cvtpk(N0[4] * sc, N0[5] * sc), cvtpk(N0[6] * sc, N0[7] * sc)};
            const u32x4 f1 = {cvtpk(N0[8] * sc, N0[9] * sc), cvtpk(N0[10] * sc, N0[11] * sc), cvtpk(N0[12] * sc, N0[13] * sc), cvtpk(N0[14] * sc, N0[15] * sc)};
            __builtin_amdgcn_raw_buffer_store_b128(f0, rws, fo + (2 * tj) * 1024 + lane * 16, 0, 16);
            __builtin_amdgcn_raw_buffer_store_b128(f1, rws, fo + (2 * tj + 1) * 1024 + lane * 16, 0, 16);
            __builtin_amdgcn_raw_buffer_store_b32(cvtpk(N1[0] * sc, N1[1] * sc), rws, fo + (32 + (tj >> 2)) * 1024 + (((tj >> 1) & 1) * 32 + n) * 16 + (2 * (tj & 1) + hi) * 4, 0, 16);
        }
        WAITV(0);
        async_bar(gen, lane, ws);
        if (tb == 0 && lane == 0) put_flag(ws, WS_FOA + (16 * ti + tj) * 8, tag);
        STAMP(10);
        const int tt = 4 * x8 + (r & 3), cg = r >> 2;
        unsigned wo[21];
        (void)wo;
        auto stage = [&](const char* w) {
            const char* wsl = w + cg * 144;
#pragma unroll 1
            for (int p = tb; p < 81; p += 4) {
                const int q = p * 64 + lane, row = q / 9, pc = q - row * 9;
                dma_s(p * 1024, wsl, row * 1152 + pc * 16);
            }
        };
        const int i16 = lane & 15, gr = i16 & 3, hk = g4 & 1, fh = g4 >> 1;
        const unsigned fro = (unsigned)(2 * tt + (tb >> 1)) * 36 * 1024 + fh * 1024 + (hk * 32 + n16 + 16 * (tb & 1)) * 16;
        const unsigned remo = (18 * (8 * fh + 4 * hk + (i16 >> 3)) + 16 + ((i16 >> 2) & 1)) * 144 + gr * 8;
        f32x4 T[5] = {};
        auto gemm = [&](unsigned fbase, unsigned maino, unsigned r4s) {
            u32x4 bf[18];
#pragma unroll
            for (int ks = 0; ks < 18; ++ks) bf[ks] = __builtin_bit_cast(u32x4, __builtin_amdgcn_raw_buffer_load_b128(rws, fbase + fro + ks * 2048, 0, 16));
#pragma unroll
            for (int ks = 0; ks < 18; ++ks) {
                const unsigned a = ks < 16 ? maino + ks * 36 * 144 : remo + (ks - 16) * 16 * 18 * 144;
                const unsigned s2 = ks < 16 ? r4s : 2 * 18 * 144;
#pragma unroll
                for (int cb = 0; cb < 5; ++cb) T[cb] = MFMA16(trd2(a + cb * 32, a + cb * 32 + s2), bf[ks], T[cb]);
            }
        };
        lds_wait<16>(C_BPROG, 4, ws);
        stage(P.woA);
        WAITV(0);
        async_bar(gen, lane, ws);
        STAMP(11);
        poll_flags(ws, WS_FOA + 16 * (tt >> 1) * 8, 16, tag, lane, 1);
        gemm(WS_OAF, (18 * hk + 8 * fh + (i16 >> 2)) * 144 + gr * 8, 4 * 144);
        async_bar(gen, lane, ws);
        STAMP(12);
        stage(P.woB);
        WAITV(0);
        async_bar(gen, lane, ws);
        STAMP(13);
        poll_flags(ws, WS_FOB + 16 * (tt >> 1) * 8, 16, tag, lane, 0);
        STAMP(14);
        gemm(WS_OBF, (18 * fh + 4 * hk + (i16 >> 2)) * 144 + gr * 8, 8 * 144);
        char* op = P.out + (size_t)(64 * tt + 16 * tb + n16) * 1152 + cg * 144 + g4 * 8;
#pragma unroll
        for (int cb = 0; cb < 5; ++cb) {
            const u32x2 o = {cvtpk(T[cb][0], T[cb][1]), cvtpk(T[cb][2], T[cb][3])};
            if (cb < 4 || g4 < 2) *(u32x2*)(op + cb * 32) = o;
        }
    }
    if (!ISB && tb == 0) {
        STAMP(15);
        asm volatile("s_waitcnt lgkmcnt(0)" ::: "memory");
        const unsigned t0 = uni(LA(unsigned, 162560 + 8)), tk = uni(LA(unsigned, 162560 + 4 * (LAT)));
        const unsigned d = (tk - t0) * 4, now = (unsigned)__builtin_amdgcn_s_memrealtime();
        if (d < 100000) while ((unsigned)__builtin_amdgcn_s_memrealtime() - now < d) __builtin_amdgcn_s_sleep(2);
    }
}

__global__ __launch_bounds__(512, 2) void gated_mix_kernel(const void* x, const void* wqkv, const void* wphi, const void* woA, const void* wq, const void* wk,
                                                           const void* wv, const void* woB, const void* gate, void* out, void* ws) {
    extern __shared__ __attribute__((aligned(16))) char lds[];
    unsigned long long tf; asm volatile("s_memrealtime %0\n\ts_waitcnt lgkmcnt(0)" : "=s"(tf) :: "memory");
    if (threadIdx.x == 256) LW(unsigned, 162560 + 4 * 2) = (unsigned)tf;
    const int lane = threadIdx.x & 63, wid = uni(threadIdx.x >> 6);
    const Ptrs P = {(const char*)x, (const char*)wqkv, (const char*)wphi, (const char*)woA, (const char*)wq, (const char*)wk, (const char*)wv, (const char*)woB, (const char*)gate, (char*)out, (char*)ws};
    if (wid < 4) role_main<true>(P, wid, lane); else role_main<false>(P, wid, lane);
}
}

extern "C" void kernel_launch(void* const* d_in, const int* in_sizes, int n_in, void* d_out, int out_size, void* d_ws, size_t ws_size, hipStream_t stream) {
    (void)in_sizes; (void)n_in; (void)out_size; (void)ws_size;
    (void)hipFuncSetAttribute((const void*)fk::gated_mix_kernel, hipFuncAttributeMaxDynamicSharedMemorySize, fk::LDS_BYTES);
    hipLaunchKernelGGL(fk::gated_mix_kernel, dim3(256), dim3(512), fk::LDS_BYTES, stream,
                       (const void*)d_in[0], (const void*)d_in[1], (const void*)d_in[2], (const void*)d_in[3], (const void*)d_in[5], (const void*)d_in[6],
                       (const void*)d_in[7], (const void*)d_in[8], (const void*)d_in[9], d_out, d_ws);
}
```

```cpp
#define MODE 0
#include <hip/hip_runtime.h>
#include <hip/hip_bf16.h>
#include <stdint.h>

namespace fk {
typedef __bf16 bf16x2 __attribute__((ext_vector_type(2)));
typedef __bf16 bf16x8 __attribute__((ext_vector_type(8)));
typedef short s16x4 __attribute__((ext_vector_type(4)));
typedef float f32x2 __attribute__((ext_vector_type(2)));
typedef float f32x4 __attribute__((ext_vector_type(4)));
typedef float f32x16 __attribute__((ext_vector_type(16)));
typedef unsigned u32x2 __attribute__((ext_vector_type(2)));
typedef unsigned u32x4 __attribute__((ext_vector_type(4)));
typedef unsigned long long u64;
typedef __attribute__((address_space(1))) u64 gu64;
typedef __attribute__((address_space(1))) unsigned gu32;

#define AS3 __attribute__((address_space(3)))
#define SBAR() __builtin_amdgcn_sched_barrier(0)
#define PIN(x) asm volatile("" : "+v"(x))
#define MFMA(a, b, c) __builtin_amdgcn_mfma_f32_32x32x16_bf16(__builtin_bit_cast(bf16x8, a), __builtin_bit_cast(bf16x8, b), c, 0, 0, 0)
#define MFMA8(a, b, c) __builtin_amdgcn_mfma_f32_32x32x8bf16_1k(__builtin_bit_cast(s16x4, a), __builtin_bit_cast(s16x4, b), c, 0, 0, 0)
#define MFMA16(a, b, c) __builtin_amdgcn_mfma_f32_16x16x32_bf16(__builtin_bit_cast(bf16x8, a), __builtin_bit_cast(bf16x8, b), c, 0, 0, 0)
#define LA(T, a) (*(const AS3 T*)(uintptr_t)(a))
#define LW(T, a) (*(AS3 T*)(uintptr_t)(a))
#define FENCE() asm volatile("" ::: "memory")
#define EX(x) __builtin_amdgcn_exp2f(x)

#define WS_TMO 64
constexpr unsigned WS_FKV = 4096, WS_FKVP = 8192, WS_FOA = 12288, WS_FOB = 16384;
#define WS_G 32768
#define WS_KMAX 40960
#define WS_KVIMG 65536
#define WS_KVP 262144
#define WS_OAF (2 << 20)
#define WS_OBF (5 << 20)

constexpr unsigned SLOT = 36864, WOFF = 16384;
constexpr unsigned L_WKV = 110592;
constexpr unsigned L_RED = 152064, L_RES = 159744;
constexpr unsigned L_CNT = 161024, L_WPR = 161152;
constexpr unsigned L_WPHI = 152064, L_KVT = 157184;
constexpr unsigned L_PHIK = 113664, L_VB = 134144;
constexpr unsigned KB_BYTES = 2240, STEP_BYTES = 9472, KR_OFF = 8960;
constexpr unsigned LDS_BYTES = 163840;
constexpr unsigned C_LAND = L_CNT, C_ASY = L_CNT + 4, C_BPROG = L_CNT + 8, C_BDONE = L_CNT + 12;

#define STAMP(k) do { } while (0)

__device__ __forceinline__ unsigned cvtpk(float lo, float hi) { f32x2 v = {lo, hi}; return __builtin_bit_cast(unsigned, __builtin_convertvector(v, bf16x2)); }
__device__ __forceinline__ int uni(int v) { return __builtin_amdgcn_readfirstlane(v); }

__device__ __forceinline__ void dma_s(unsigned lds, const void* sbase, unsigned voff) {
    asm volatile("s_mov_b32 m0, %0\n\ts_nop 0\n\tglobal_load_lds_dwordx4 %1, %2" :: "s"(lds), "v"(voff), "s"(sbase) : "memory", "m0");
}
__device__ __forceinline__ void dma_s_sc1(unsigned lds, const void* sbase, unsigned voff) {
    asm volatile("s_mov_b32 m0, %0\n\ts_nop 0\n\tglobal_load_lds_dwordx4 %1, %2 sc1" :: "s"(lds), "v"(voff), "s"(sbase) : "memory", "m0");
}
__device__ __forceinline__ void dma_v(unsigned lds, const void* vaddr) {
    asm volatile("s_mov_b32 m0, %0\n\ts_nop 0\n\tglobal_load_lds_dwordx4 %1, off" :: "s"(lds), "v"(vaddr) : "memory", "m0");
}
#define WAITV(n) asm volatile("s_waitcnt vmcnt(" #n ")" ::: "memory")
#define BAR_L() asm volatile("s_waitcnt lgkmcnt(0)\n\ts_barrier" ::: "memory")
template <int N> __device__ __forceinline__ void bar_vl() { asm volatile("s_waitcnt vmcnt(%0) lgkmcnt(0)\n\ts_barrier" :: "n"(N) : "memory"); }

__device__ __forceinline__ u32x2 trd(unsigned a) { return __builtin_bit_cast(u32x2, __builtin_amdgcn_ds_read_tr16_b64_v4i16((AS3 s16x4*)(uintptr_t)a)); }
__device__ __forceinline__ u32x4 trd2(unsigned a0, unsigned a1) { const u32x2 p = trd(a0), q = trd(a1); return (u32x4){p[0], p[1], q[0], q[1]}; }

__device__ __forceinline__ u64 mix64(u64 z) { z += 0x9E3779B97F4A7C15ull; z = (z ^ (z >> 30)) * 0xBF58476D1CE4E5B9ull; z = (z ^ (z >> 27)) * 0x94D049BB133111EBull; return z ^ (z >> 31); }

constexpr unsigned SPIN_MAX = 1u << 20;
__device__ __forceinline__ void poll_flags(const char* ws, unsigned off, int n, u64 tag, int lane, int slp) {
    const gu64* p = (const gu64*)(ws + off) + (lane < n ? lane : 0);
    for (unsigned s = 0;; ++s) {
        const u64 v = __hip_atomic_load(p, __ATOMIC_RELAXED, __HIP_MEMORY_SCOPE_AGENT);
        if (__all(v == tag)) break;
        if (s > SPIN_MAX) { if (lane == 0) __hip_atomic_store((gu32*)(ws + WS_TMO), 0x100u + off, __ATOMIC_RELAXED, __HIP_MEMORY_SCOPE_AGENT); break; }
        if (slp) __builtin_amdgcn_s_sleep(8); else __builtin_amdgcn_s_sleep(1);
    }
    FENCE();
}
__device__ __forceinline__ void put_flag(char* ws, unsigned off, u64 tag) { __hip_atomic_store((gu64*)(ws + off), tag, __ATOMIC_RELAXED, __HIP_MEMORY_SCOPE_AGENT); }
__device__ __forceinline__ void lds_inc(unsigned a, int lane) { if (lane == 0) __hip_atomic_fetch_add((AS3 unsigned*)(uintptr_t)a, 1u, __ATOMIC_RELAXED, __HIP_MEMORY_SCOPE_WORKGROUP); }
template <int SLP> __device__ __forceinline__ void lds_wait(unsigned a, unsigned target, const char* ws) {
    for (unsigned s = 0;; ++s) {
        const unsigned v = uni(__hip_atomic_load((AS3 unsigned*)(uintptr_t)a, __ATOMIC_RELAXED, __HIP_MEMORY_SCOPE_WORKGROUP));
        if (v >= target) break;
        if (s > SPIN_MAX) { __hip_atomic_store((gu32*)(ws + WS_TMO), 0x10u + (a & 15), __ATOMIC_RELAXED, __HIP_MEMORY_SCOPE_AGENT); break; }
        __builtin_amdgcn_s_sleep(SLP);
    }
    FENCE();
}
__device__ __forceinline__ void async_bar(unsigned& gen, int lane, const char* ws) {
    asm volatile("s_waitcnt lgkmcnt(0)" ::: "memory");
    gen += 4; lds_inc(C_ASY, lane); lds_wait<1>(C_ASY, gen, ws);
}

__device__ __forceinline__ void features(const f32x16& a0, float p16, float p17, float p18, float p19, int lane, unsigned (&out)[18]) {
    const u32x4 b0 = {cvtpk(a0[0], a0[1]), cvtpk(a0[2], a0[3]), cvtpk(a0[4], a0[5]), cvtpk(a0[6], a0[7])};
    const u32x4 b1 = {cvtpk(a0[8], a0[9]), cvtpk(a0[10], a0[11]), cvtpk(a0[12], a0[13]), cvtpk(a0[14], a0[15])};
    const u32x2 b2 = {cvtpk(p16, p17), cvtpk(p18, p19)};
    const unsigned wa = L_WPHI + lane * 16, wr = L_WPHI + 2048 + lane * 8;
    f32x16 P0 = {}, P1 = {};
    P0 = MFMA(LA(u32x4, wa), b0, P0); P1 = MFMA(LA(u32x4, wa + 2560), b0, P1);
    P0 = MFMA(LA(u32x4, wa + 1024), b1, P0); P1 = MFMA(LA(u32x4, wa + 2560 + 1024), b1, P1);
    P0 = MFMA8(LA(u32x2, wr), b2, P0); P1 = MFMA8(LA(u32x2, wr + 2560), b2, P1);
    float p[18];
#pragma unroll
    for (int i = 0; i < 16; ++i) p[i] = P0[i];
    p[16] = P1[0]; p[17] = P1[1];
    float mx = p[0], mn = p[0];
#pragma unroll
    for (int i = 1; i < 18; ++i) { mx = __builtin_fmaxf(mx, p[i]); mn = __builtin_fminf(mn, p[i]); }
    const float L = 1.4426950408889634f, mxl = -mx * L, mnl = mn * L;
    float ep[18], en[18], sp = 0.f, sn = 0.f;
#pragma unroll
    for (int i = 0; i < 18; ++i) { ep[i] = EX(__builtin_fmaf(p[i], L, mxl)); en[i] = EX(__builtin_fmaf(p[i], -L, mnl)); sp += ep[i]; sn += en[i]; }
    const float rp = __builtin_amdgcn_rcpf(sp), rn = __builtin_amdgcn_rcpf(sn);
#pragma unroll
    for (int u = 0; u < 9; ++u) { out[u] = cvtpk(ep[2 * u] * rp, ep[2 * u + 1] * rp); out[9 + u] = cvtpk(en[2 * u] * rn, en[2 * u + 1] * rn); }
}

struct Ptrs { const char *x, *wqkv, *wphi, *woA, *wq, *wk, *wv, *woB, *gate; char *out, *ws; };

template <bool ISB>
__device__ __forceinline__ void role_main(const Ptrs& P, const int wid, const int lane) {
    const int b = blockIdx.x, x8 = b & 7, r = b >> 3;
    const int ti = 8 * (x8 & 1) + (r & 7), tj = 4 * (x8 >> 1) + (r >> 3);
    const int tb = wid & 3, n = lane & 31, hi = lane >> 5, n16 = lane & 15, g4 = lane >> 4;
    const int tid = wid * 64 + lane;
    char* const ws = P.ws;
    const __amdgpu_buffer_rsrc_t rws = __builtin_amdgcn_make_buffer_rsrc((void*)ws, (short)0, 0x1000000, 0x00020000);
    STAMP(0);
    u32x2 Ev; u32x4 xf0, xf1, xf2, gf0, gf1, gf2;
    {
        const int kw = 96 * (wid < 6 ? wid : 5) + 8 * g4;
        const unsigned xo = (unsigned)(128 * ti + 8 * tj + (n16 & 7)) * 1152u + kw * 2, go = kw * 2, zo = 0;
        asm volatile("global_load_dwordx2 %0, %7, %10 sc1\n\t"
                     "global_load_dwordx4 %1, %8, %11\n\tglobal_load_dwordx4 %2, %8, %11 offset:64\n\tglobal_load_dwordx4 %3, %8, %11 offset:128\n\t"
                     "global_load_dwordx4 %4, %9, %12\n\tglobal_load_dwordx4 %5, %9, %12 offset:64\n\tglobal_load_dwordx4 %6, %9, %12 offset:128"
                     : "=&v"(Ev), "=&v"(xf0), "=&v"(xf1), "=&v"(xf2), "=&v"(gf0), "=&v"(gf1), "=&v"(gf2)
                     : "v"(zo), "v"(xo), "v"(go), "s"(ws), "s"(P.x), "s"(P.gate) : "memory");
    }
#pragma unroll
    for (int t = 0; t < 5; ++t) {
        const int q = (wid + 8 * t) * 64 + lane;
        const char* src = q < 1296 ? P.wk + q * 16 : P.wv + (q - 1296) * 16;
        dma_v(L_WKV + (wid + 8 * t) * 1024, src);
    }
    if (wid == 0) { if (lane < 32) dma_v(L_WKV + 40 * 1024, P.wv + (40 * 64 + lane - 1296) * 16); }
    if (wid == 6) { dma_s(L_WPR, P.wphi + tj * 1296, lane * 16); if (lane < 17) dma_s(L_WPR + 1024, P.wphi + tj * 1296 + 1024, lane * 16); }
    if (tid == 0) { LW(u32x4, L_CNT) = (u32x4){0u, 0u, 0u, 0u}; }
    const int rot = (r + 3 * x8) % 9;
    constexpr int ND = ISB ? 4 : 5;
    unsigned xo[4]; const char* wb[5]; unsigned wst[5];
    if (ISB) {
#pragma unroll
        for (int i = 0; i < 4; ++i) xo[i] = (unsigned)(128 * ti + 32 * tb + 8 * i + (lane >> 3)) * 1152u + (((lane & 7) ^ (4 * (i & 1) + (lane >> 4))) * 16);
    } else {
        const int dl = 8 * (tj & 1);
#pragma unroll
        for (int i = 0; i < 5; ++i) {
            const int o = (5 * tb + i) * 1024 + lane * 16, row = o / 320, wi = o - row * 320, ty = wi / 80, pc = wi - ty * 80;
            const int col = 72 * tj - dl + pc;
            wb[i] = ty == 3 ? P.wq + row * 1152 + col : P.wqkv + row * 3456 + col + (ty == 0 ? 1152 : (ty == 1 ? 0 : 2304));
            wst[i] = ty == 3 ? 64 * 1152 : 64 * 3456;
        }
    }
    auto issue = [&](int c) {
        int kc = c + rot; if (kc >= 9) kc -= 9;
        const unsigned slot = (c & 3) * SLOT;
        if (ISB) {
            const char* xb = P.x + kc * 128;
#pragma unroll
            for (int i = 0; i < 4; ++i) dma_s(slot + (4 * tb + i) * 1024, xb, xo[i]);
        } else {
#pragma unroll
            for (int i = 0; i < 5; ++i) dma_v(slot + WOFF + (5 * tb + i) * 1024, wb[i] + (u64)kc * wst[i]);
        }
    };
    issue(0); issue(1); issue(2);
    asm volatile("s_waitcnt vmcnt(12) lgkmcnt(0)\n\ts_barrier" : "+v"(Ev), "+v"(xf0), "+v"(xf1), "+v"(xf2), "+v"(gf0), "+v"(gf1), "+v"(gf2) :: "memory");
    STAMP(1);
    const u64 tag = mix64(((u64)(unsigned)uni(Ev[1]) << 32) | (unsigned)uni(Ev[0]));
    u32x4 wf[5]; u32x2 dummy; (void)dummy;
    if (wid < 6) {
        f32x4 d0 = {}, d1 = {}, d2 = {}, d3 = {};
        const unsigned ak = L_WKV + (96 * wid + 8 * g4) * 36 + n16 * 2;
        const unsigned am = L_WKV + ((n16 & 2) ? 20736 : 0) + (96 * wid + 8 * g4) * 36 + 32 + (n16 & 1) * 2;
#pragma unroll
        for (int s = 0; s < 3; ++s) {
            u32x4 a0, a1, a2;
#pragma unroll
            for (int u = 0; u < 4; ++u) {
                const unsigned o = (32 * s + 2 * u) * 36;
                a0[u] = (unsigned)LA(unsigned short, ak + o) | ((unsigned)LA(unsigned short, ak + o + 36) << 16);
                a1[u] = (unsigned)LA(unsigned short, ak + 20736 + o) | ((unsigned)LA(unsigned short, ak + 20736 + o + 36) << 16);
                a2[u] = (unsigned)LA(unsigned short, am + o) | ((unsigned)LA(unsigned short, am + o + 36) << 16);
            }
            const u32x4 xf = s == 0 ? xf0 : (s == 1 ? xf1 : xf2), gf = s == 0 ? gf0 : (s == 1 ? gf1 : gf2);
            d0 = MFMA16(a0, xf, d0); d1 = MFMA16(a1, xf, d1); d2 = MFMA16(a2, xf, d2); d3 = MFMA16(gf, xf, d3);
        }
        const unsigned ra = L_RED + wid * 1280 + (n16 & 7) * 160;
        const bool up = n16 >= 8;
        LW(f32x4, ra + (up ? 64 : 0) + g4 * 16) = up ? d1 : d0;
        if (g4 == 0) LW(f32x4, ra + (up ? 144 : 128)) = up ? d3 : d2;
    } else if (wid == 6) {
        const int hm = (n >> 2) & 1;
#pragma unroll
        for (int mb = 0; mb < 2; ++mb) {
            const int f = 16 * mb + 4 * (n >> 3) + (n & 3);
            const bool vm = hm == hi && f < 18;
            unsigned v[20];
#pragma unroll
            for (int pos = 0; pos < 20; ++pos) {
                const int d = pos - 2 * hi; const bool ok = vm && d >= 0 && d < 18;
                const unsigned e = LA(unsigned short, L_WPR + (ok ? (hi * 324 + d * 18 + f) * 2 : 0));
                v[pos] = ok ? e : 0u;
            }
            wf[2 * mb] = (u32x4){v[0] | (v[1] << 16), v[2] | (v[3] << 16), v[4] | (v[5] << 16), v[6] | (v[7] << 16)};
            wf[2 * mb + 1] = (u32x4){v[8] | (v[9] << 16), v[10] | (v[11] << 16), v[12] | (v[13] << 16), v[14] | (v[15] << 16)};
            if (mb == 0) { wf[4][0] = v[16] | (v[17] << 16); wf[4][1] = v[18] | (v[19] << 16); } else { wf[4][2] = v[16] | (v[17] << 16); wf[4][3] = v[18] | (v[19] << 16); }
        }
    }
    BAR_L();
    STAMP(2);
    if (tid < 320) {
        float s = 0.f;
#pragma unroll
        for (int w = 0; w < 6; ++w) s += LA(float, L_RED + w * 1280 + tid * 4);
        LW(float, L_RES + tid * 4) = s;
    }
    f32x16 acc0 = {}, acc1 = {}, acc2 = {};
    unsigned wa0, wa1, wa2, xa;
    {
        const int i = lane & 15, sl = 2 * (g4 & 1) + ((i & 3) >> 1);
        const unsigned base = WOFF + (8 * (g4 >> 1) + (i >> 2)) * 320 + 8 * (tj & 1) + 32 * (i & 1);
        if (ISB) {
            wa0 = base + 8 * sl;
            wa1 = base + (sl == 0 ? 32 : 240 + 8 * (sl - 1));
            wa2 = base + (sl < 2 ? 240 + 24 + 8 * sl : 160 + 24 + 8 * (sl - 2));
        } else {
            wa0 = base + 80 + 8 * sl;
            wa1 = base + (sl == 0 ? 80 + 32 : 160 + 8 * (sl - 1));
            wa2 = 0;
        }
        xa = (32 * tb + n) * 128 + ((hi ^ ((n >> 1) & 7)) * 16);
    }
#pragma unroll
    for (int c = 0; c < 9; ++c) {
        if (c <= 6) bar_vl<2 * ND>(); else if (c == 7) bar_vl<ND>(); else bar_vl<0>();
        if (c == 0) STAMP(3);
        if (c == 4) STAMP(4);
        if (!ISB && c == 0) {
            if (wid == 7) {
                const int t = lane & 7, bi = tj >> 2, pc = tj & 3;
                const int rho = 16 * (pc >> 1) + 8 * (t >> 2) + 4 * (pc & 1) + (t & 3);
                const int d = lane - 16;
                unsigned base, strd;
                if (lane < 16) { base = t * 40 + 8 * (lane >> 3); strd = 1; }
                else if (lane < 40) { base = d < 16 ? 16 + d : (d < 18 ? 18 + d : 0); strd = 40; }
                else if (lane < 48) { base = t * 40 + 32; strd = 1; }
                else { base = t * 40 + 36; strd = 0; }
                float v[8];
#pragma unroll
                for (int i = 0; i < 8; ++i) v[i] = LA(float, L_RES + (base + i * strd) * 4);
                float ss = v[0] * v[0] + v[1] * v[1];
                float s6 = 0.f;
#pragma unroll
                for (int i = 2; i < 8; ++i) s6 += v[i] * v[i];
                if (lane < 16) ss += s6;
                u32x4 o = {cvtpk(v[0], v[1]), cvtpk(v[2], v[3]), cvtpk(v[4], v[5]), cvtpk(v[6], v[7])};
                if (lane == 34) o = (u32x4){0x3f803f80u, 0x3f803f80u, 0x3f803f80u, 0x3f803f80u};
                const unsigned img = WS_KVIMG + ti * STEP_BYTES;
                const unsigned o16 = lane < 16 ? img + bi * KB_BYTES + ((lane >> 3) * 32 + rho) * 16 : img + bi * KB_BYTES + 1024 + pc * 304 + d * 16;
                if (lane < 35) __builtin_amdgcn_raw_buffer_store_b128(o, rws, o16, 0, 16);
                float tot = __shfl(ss, t) + __shfl(ss, 8 + t) + __shfl(ss, 40 + t);
                tot = __builtin_fmaxf(tot, __shfl_xor(tot, 1)); tot = __builtin_fmaxf(tot, __shfl_xor(tot, 2)); tot = __builtin_fmaxf(tot, __shfl_xor(tot, 4));
                const float gv = __builtin_amdgcn_rcpf(1.f + EX(-1.4426950408889634f * v[0]));
                unsigned o4, d4;
                if (lane < 48) { o4 = img + KR_OFF + (bi >> 1) * 256 + rho * 8 + (bi & 1) * 4; d4 = o[0]; }
                else if (lane < 56) { o4 = WS_G + (128 * ti + 8 * tj + t) * 4; d4 = __float_as_uint(gv); }
                else { o4 = WS_KMAX + (16 * ti + tj) * 4; d4 = __float_as_uint(tot); }
                if (lane >= 40 && lane <= 56) __builtin_amdgcn_raw_buffer_store_b32(d4, rws, o4, 0, 16);
            } else if (wid == 6) {
                LW(u32x4, L_WPHI + lane * 16) = wf[0]; LW(u32x4, L_WPHI + 1024 + lane * 16) = wf[1];
                LW(u32x4, L_WPHI + 2560 + lane * 16) = wf[2]; LW(u32x4, L_WPHI + 2560 + 1024 + lane * 16) = wf[3];
                LW(u32x2, L_WPHI + 2048 + lane * 8) = (u32x2){wf[4][0], wf[4][1]}; LW(u32x2, L_WPHI + 2560 + 2048 + lane * 8) = (u32x2){wf[4][2], wf[4][3]};
            }
        }
        if (!ISB && c == 2) { if (wid == 7 && lane == 0) put_flag(ws, WS_FKV + (16 * ti + tj) * 8, tag); }
        if (c + 3 < 9) issue(c + 3);
        const unsigned slot = (c & 3) * SLOT;
#pragma unroll
        for (int ks = 0; ks < 4; ++ks) {
            const u32x4 xf = LA(u32x4, slot + (xa ^ (ks * 32)));
            const unsigned o = slot + ks * 5120;
            acc0 = MFMA(trd2(wa0 + o, wa0 + o + 1280), xf, acc0);
            acc1 = MFMA(trd2(wa1 + o, wa1 + o + 1280), xf, acc1);
            if (ISB) acc2 = MFMA(trd2(wa2 + o, wa2 + o + 1280), xf, acc2);
        }
    }
    STAMP(5);
    if (!ISB && wid == 7) {
#pragma unroll
        for (int q = 0; q < 4; ++q) poll_flags(ws, WS_FKV + q * 512, 64, tag, lane, 0);
        if (b == 0 && lane == 0) __hip_atomic_store((gu64*)ws, tag, __ATOMIC_RELAXED, __HIP_MEMORY_SCOPE_AGENT);
    }
    BAR_L();
    STAMP(6);
    const float gate = __uint_as_float(__builtin_amdgcn_raw_buffer_load_b32(rws, WS_G + (128 * ti + 32 * tb + n) * 4, 0, 16));
    const unsigned vrow = L_VB + (32 * tb + n) * 112 + hi * 52 + 8;

    if (ISB) {
        const u32x4 km4 = __builtin_bit_cast(u32x4, __builtin_amdgcn_raw_buffer_load_b128(rws, WS_KMAX + lane * 16, 0, 16));
        {
            unsigned ph[18];
            features(acc0, acc1[0], acc1[1], acc1[2], acc1[3], lane, ph);
            const unsigned pa = L_PHIK + (32 * tb + n) * 160 + hi * 80;
#pragma unroll
            for (int u = 0; u < 4; ++u) LW(u32x4, pa + 16 * u) = (u32x4){ph[4 * u], ph[4 * u + 1], ph[4 * u + 2], ph[4 * u + 3]};
            LW(u32x2, pa + 64) = (u32x2){ph[16], ph[17]};
            LW(unsigned, vrow + 24) = cvtpk(acc2[8], acc2[9]); LW(unsigned, vrow + 28) = cvtpk(acc2[10], acc2[11]);
            LW(unsigned, vrow + 32) = cvtpk(acc2[12], acc2[13]); LW(unsigned, vrow + 36) = cvtpk(acc2[14], acc2[15]);
            LW(unsigned, vrow + 36 + 4 * hi) = 0x00003f80u;
        }
        u32x4 QA, QB; u32x2 QRA0, QRA1, QRB0, QRB1; f32x16 MA, MB;
        {
            const float sc = 0.23570226039551584f * 1.4426950408889634f;
            float q[20];
#pragma unroll
            for (int i = 0; i < 12; ++i) q[i] = acc1[4 + i] * sc;
#pragma unroll
            for (int i = 0; i < 8; ++i) q[12 + i] = acc2[i] * sc;
            float n2 = hi ? q[18] * q[18] + q[19] * q[19] : q[0] * q[0] + q[1] * q[1];
#pragma unroll
            for (int i = 2; i < 18; ++i) n2 = __builtin_fmaf(q[i], q[i], n2);
            unsigned cc[10], D[9];
#pragma unroll
            for (int u = 0; u < 10; ++u) cc[u] = cvtpk(q[2 * u], q[2 * u + 1]);
#pragma unroll
            for (int u = 0; u < 9; ++u) D[u] = hi ? cc[u + 1] : cc[u];
#pragma unroll
            for (int u = 0; u < 4; ++u) { const auto sw = __builtin_amdgcn_permlane32_swap(D[u], D[u + 4], false, false); QA[u] = sw[0]; QB[u] = sw[1]; }
            QRA0 = (u32x2){hi ? 0u : D[8], 0u}; QRA1 = (u32x2){0u, hi ? 0u : D[8]};
            QRB0 = (u32x2){hi ? D[8] : 0u, 0u}; QRB1 = (u32x2){0u, hi ? D[8] : 0u};
            SBAR();
            float km = __builtin_fmaxf(__builtin_fmaxf(__uint_as_float(km4[0]), __uint_as_float(km4[1])), __builtin_fmaxf(__uint_as_float(km4[2]), __uint_as_float(km4[3])));
#pragma unroll
            for (int o = 1; o < 64; o <<= 1) km = __builtin_fmaxf(km, __shfl_xor(km, o));
            const float m = -1.01f * __builtin_sqrtf(n2 * km);
            const auto sw = __builtin_amdgcn_permlane32_swap(__float_as_uint(m), __float_as_uint(m), false, false);
            const float ma = __uint_as_float(sw[0]), mb = __uint_as_float(sw[1]);
#pragma unroll
            for (int i = 0; i < 16; ++i) { MA[i] = ma; MB[i] = mb; }
            PIN(MA); PIN(MB); PIN(QA); PIN(QB); PIN(QRA0); PIN(QRA1); PIN(QRB0); PIN(QRB1);
        }
        BAR_L();
        STAMP(7);
        unsigned kp = lane * 16, rp = KR_OFF + n * 8, vp = 1024 + hi * 304 + (n < 18 ? n : 18) * 16;
        PIN(kp); PIN(rp); PIN(vp);
        f32x16 OA = {}, OB = {}, S0A, S0B, S1A, S1B;
        u32x4 KMa, KMb, V0a, V0b, V1a, V1b = {}, P0 = {}, P1 = {}; u32x2 KRX, KRY;
        float e0, e1, e2, e3, f0 = 0.f, f1 = 0.f, f2 = 0.f, f3 = 0.f;
        KRX = LA(u32x2, rp); KMa = LA(u32x4, kp);
        S0A = MFMA(KMa, QA, MA); S0B = MFMA(KMa, QB, MB); S0A = MFMA8(KRX, QRA0, S0A); S0B = MFMA8(KRX, QRB0, S0B);
        SBAR();
        KMb = LA(u32x4, kp + KB_BYTES); V0a = LA(u32x4, vp); V1a = LA(u32x4, vp + 608);
        __builtin_amdgcn_s_waitcnt(0xC07F);
        SBAR();
#define LD(dst, T, p) dst = LA(T, p)
#define NOP_ (void)0
#define SLOTB(MF, POST, d0, d1, d2, d3, S, g, Pq, i, a0, a1, a2, a3) do { MF; SBAR(); POST; SBAR(); \
        d0 = EX(S[4 * (g)]); d1 = EX(S[4 * (g) + 1]); PIN(d0); PIN(d1); SBAR(); \
        Pq[i] = cvtpk(a0, a1); Pq[i + 1] = cvtpk(a2, a3); PIN(Pq); SBAR(); \
        d2 = EX(S[4 * (g) + 2]); d3 = EX(S[4 * (g) + 3]); PIN(d2); PIN(d3); SBAR(); } while (0)
#define E_ e0, e1, e2, e3
#define F_ f0, f1, f2, f3
#define SLOT_(...) SLOTB(__VA_ARGS__)
#define WAITL __builtin_amdgcn_s_waitcnt(0xC07F)
#define PERIOD(SA, SB, NA, NB, KMu, KMl, KRu, QRAx, QRBx, V0u, V1u, V0l, V1l, vb, kb, PA3) do { \
        SLOT_(NA = MFMA(KMu, QA, MA),     LD(KMl, u32x4, kp + (kb)),       E_, SA, 0, P1, 2, F_); \
        SLOT_(OB = MFMA(V1l, P1, OB),     LD(V1l, u32x4, vp + (vb) + 608), F_, SA, 1, P0, 0, E_); \
        SLOT_(NA = MFMA8(KRu, QRAx, NA),  LD(V0l, u32x4, vp + (vb)),       E_, SA, 2, P0, 2, F_); \
        SLOT_(OA = MFMA(V0u, P0, OA),     PA3,                             F_, SA, 3, P1, 0, E_); \
        SLOT_(NB = MFMA(KMu, QB, MB),     NOP_,                            E_, SB, 0, P1, 2, F_); \
        SLOT_(OA = MFMA(V1u, P1, OA),     NOP_,                            F_, SB, 1, P0, 0, E_); \
        SLOT_(NB = MFMA8(KRu, QRBx, NB),  WAITL,                           E_, SB, 2, P0, 2, F_); \
        SLOT_(OB = MFMA(V0u, P0, OB),     NOP_,                            F_, SB, 3, P1, 0, E_); \
    } while (0)
#pragma unroll 1
        for (int seg = 0; seg < 3; ++seg) {
            if (seg == 1) { lds_inc(C_BPROG, lane); STAMP(8); }
            if (seg == 2) lds_wait<1>(C_LAND, 4, ws);
            const int ns = seg == 0 ? 9 : (seg == 1 ? 2 : 5);
#pragma unroll 1
            for (int s = 0; s < ns; ++s) {
                PERIOD(S0A, S0B, S1A, S1B, KMb, KMa, KRX, QRA1, QRB1, V0a, V1a, V0b, V1b, KB_BYTES, 2 * KB_BYTES, LD(KRY, u32x2, rp + 256));
                PERIOD(S1A, S1B, S0A, S0B, KMa, KMb, KRY, QRA0, QRB0, V0b, V1b, V0a, V1a, 2 * KB_BYTES, 3 * KB_BYTES, NOP_);
                PERIOD(S0A, S0B, S1A, S1B, KMb, KMa, KRY, QRA1, QRB1, V0a, V1a, V0b, V1b, 3 * KB_BYTES, STEP_BYTES, LD(KRX, u32x2, rp + STEP_BYTES));
                PERIOD(S1A, S1B, S0A, S0B, KMa, KMb, KRX, QRA0, QRB0, V0b, V1b, V0a, V1a, STEP_BYTES, STEP_BYTES + KB_BYTES, NOP_);
                kp += STEP_BYTES; vp += STEP_BYTES; rp += STEP_BYTES; PIN(kp); PIN(vp); PIN(rp); SBAR();
            }
        }
        P1[2] = cvtpk(f0, f1); P1[3] = cvtpk(f2, f3); PIN(P1); SBAR();
        OB = MFMA(V1b, P1, OB);
        STAMP(10);
        if (__any(!(OA[10] >= 0x1p-80f) || !(OB[10] >= 0x1p-80f))) {
            const unsigned vo = WS_KVIMG + 1024 + hi * 304 + (n < 18 ? n : 18) * 16;
#pragma unroll 1
            for (int which = 0; which < 2; ++which) {
                const u32x4 Q = which ? QB : QA; const u32x2 R0 = which ? QRB0 : QRA0, R1 = which ? QRB1 : QRA1;
                float mx = -__builtin_inff();
#pragma unroll 1
                for (int kb = 0; kb < 64; ++kb) {
                    const unsigned bo = (kb >> 2) * STEP_BYTES;
                    const u32x4 km = __builtin_bit_cast(u32x4, __builtin_amdgcn_raw_buffer_load_b128(rws, WS_KVIMG + bo + (kb & 3) * KB_BYTES + lane * 16, 0, 16));
                    const u32x2 kr = __builtin_bit_cast(u32x2, __builtin_amdgcn_raw_buffer_load_b64(rws, WS_KVIMG + bo + KR_OFF + ((kb >> 1) & 1) * 256 + n * 8, 0, 16));
                    f32x16 S = {}; S = MFMA(km, Q, S); S = MFMA8(kr, (kb & 1) ? R1 : R0, S);
#pragma unroll
                    for (int i = 0; i < 16; ++i) mx = __builtin_fmaxf(mx, S[i]);
                }
                { const auto sw = __builtin_amdgcn_permlane32_swap(__float_as_uint(mx), __float_as_uint(mx), false, false); mx = __builtin_fmaxf(__uint_as_float(sw[0]), __uint_as_float(sw[1])); }
                f32x16 O = {}, C;
#pragma unroll
                for (int i = 0; i < 16; ++i) C[i] = -mx;
#pragma unroll 1
                for (int kb = 0; kb < 64; ++kb) {
                    const unsigned bo = (kb >> 2) * STEP_BYTES, bb = bo + (kb & 3) * KB_BYTES;
                    const u32x4 km = __builtin_bit_cast(u32x4, __builtin_amdgcn_raw_buffer_load_b128(rws, WS_KVIMG + bb + lane * 16, 0, 16));
                    const u32x2 kr = __builtin_bit_cast(u32x2, __builtin_amdgcn_raw_buffer_load_b64(rws, WS_KVIMG + bo + KR_OFF + ((kb >> 1) & 1) * 256 + n * 8, 0, 16));
                    const u32x4 v0 = __builtin_bit_cast(u32x4, __builtin_amdgcn_raw_buffer_load_b128(rws, vo + bb, 0, 16));
                    const u32x4 v1 = __builtin_bit_cast(u32x4, __builtin_amdgcn_raw_buffer_load_b128(rws, vo + bb + 608, 0, 16));
                    f32x16 S = MFMA(km, Q, C); S = MFMA8(kr, (kb & 1) ? R1 : R0, S);
                    float p[16];
#pragma unroll
                    for (int i = 0; i < 16; ++i) p[i] = EX(S[i]);
                    const u32x4 p0 = {cvtpk(p[0], p[1]), cvtpk(p[2], p[3]), cvtpk(p[4], p[5]), cvtpk(p[6], p[7])};
                    const u32x4 p1 = {cvtpk(p[8], p[9]), cvtpk(p[10], p[11]), cvtpk(p[12], p[13]), cvtpk(p[14], p[15])};
                    O = MFMA(v0, p0, O); O = MFMA(v1, p1, O);
                }
                if (which) OB = O; else OA = O;
            }
        }
        {
            const float ga = (1.f - gate) * __builtin_amdgcn_rcpf(OA[10]), gb = (1.f - gate) * __builtin_amdgcn_rcpf(OB[10]);
            const unsigned fo = WS_OBF + (unsigned)(4 * ti + tb) * 36 * 1024;
            const int h0 = 2 * tj;
            const u32x4 fa = {cvtpk(OA[0] * ga, OA[1] * ga), cvtpk(OA[2] * ga, OA[3] * ga), cvtpk(OA[4] * ga, OA[5] * ga), cvtpk(OA[6] * ga, OA[7] * ga)};
            const u32x4 fb = {cvtpk(OB[0] * gb, OB[1] * gb), cvtpk(OB[2] * gb, OB[3] * gb), cvtpk(OB[4] * gb, OB[5] * gb), cvtpk(OB[6] * gb, OB[7] * gb)};
            __builtin_amdgcn_raw_buffer_store_b128(fa, rws, fo + h0 * 1024 + lane * 16, 0, 16);
            __builtin_amdgcn_raw_buffer_store_b128(fb, rws, fo + (h0 + 1) * 1024 + lane * 16, 0, 16);
            const u32x2 rr = {cvtpk(OA[8] * ga, OA[9] * ga), cvtpk(OB[8] * gb, OB[9] * gb)};
            if (!hi) __builtin_amdgcn_raw_buffer_store_b64(rr, rws, fo + (32 + (tj >> 2)) * 1024 + (((tj >> 1) & 1) * 32 + n) * 16 + (tj & 1) * 8, 0, 16);
            WAITV(0);
            unsigned arrived = 0;
            if (lane == 0) arrived = __hip_atomic_fetch_add((AS3 unsigned*)(uintptr_t)C_BDONE, 1u, __ATOMIC_RELAXED, __HIP_MEMORY_SCOPE_WORKGROUP);
            if (lane == 0 && arrived == 3) put_flag(ws, WS_FOB + (16 * ti + tj) * 8, tag);
        }
        STAMP(11);
    } else {
        for (int p = tb; p < 111; p += 4) dma_s_sc1(p * 1024, ws + WS_KVIMG + p * 1024, lane * 16);
#pragma unroll
        for (int u = 0; u < 6; ++u) LW(unsigned, vrow + 4 * u) = cvtpk(acc1[4 + 2 * u], acc1[5 + 2 * u]);
        bar_vl<0>();
        STAMP(7);
        __builtin_amdgcn_s_setprio(2);
        unsigned gen = 0;
        {
            const int hh = tb & 1, i = lane & 15;
            const int tok = 8 * g4 + (i >> 2), gr = i & 3;
            const unsigned va = L_VB + tok * 112 + hh * 56 + 8 + gr * 8;
            const int ntask = tb < 2 ? 2 : 1;
#pragma unroll 1
            for (int t = 0; t < ntask; ++t) {
                const int mbf = tb < 2 ? 2 * t : 1;
                const unsigned pa = L_PHIK + tok * 160 + hh * 80 + mbf * 32 + gr * 8;
                f32x4 d0 = {}, d1 = {};
#pragma unroll
                for (int ks = 0; ks < 4; ++ks) {
                    const u32x4 a = trd2(pa + ks * 32 * 160, pa + ks * 32 * 160 + 4 * 160);
                    const u32x4 b0 = trd2(va + ks * 32 * 112, va + ks * 32 * 112 + 4 * 112);
                    const u32x4 b1 = trd2(va + ks * 32 * 112 + 32, va + ks * 32 * 112 + 4 * 112 + 32);
                    d0 = MFMA16(a, b0, d0); d1 = MFMA16(a, b1, d1);
                }
                const unsigned ko = WS_KVP + (unsigned)(16 * tj + ti) * 5472 + hh * 2736 + (16 * mbf + 4 * g4) * 4;
                if (mbf < 2 || g4 == 0) {
                    __builtin_amdgcn_raw_buffer_store_b128(__builtin_bit_cast(u32x4, d0), rws, ko + n16 * 144, 0, 16);
                    if (n16 < 3) __builtin_amdgcn_raw_buffer_store_b128(__builtin_bit_cast(u32x4, d1), rws, ko + (16 + n16) * 144, 0, 16);
                }
            }
        }
        WAITV(0);
        async_bar(gen, lane, ws);
        if (tb == 0 && lane == 0) put_flag(ws, WS_FKVP + (16 * tj + ti) * 8, tag);
        STAMP(8);
        for (int p = 111 + tb; p < 148; p += 4) dma_s_sc1(p * 1024, ws + WS_KVIMG + p * 1024, lane * 16);
        unsigned pq[18];
        features(acc0, acc1[0], acc1[1], acc1[2], acc1[3], lane, pq);
        WAITV(0);
        lds_inc(C_LAND, lane);
        poll_flags(ws, WS_FKVP + 16 * tj * 8, 16, tag, lane, 1);
        STAMP(9);
        {
            const int at = tb * 64 + lane;
#pragma unroll 1
            for (int rd = 0; rd < 2; ++rd) {
                const int it = rd == 0 ? at : 256 + tb * 22 + lane;
                const bool ok = rd == 0 || (lane < 22 && it < 342);
                const unsigned ko = WS_KVP + (unsigned)(16 * tj) * 5472 + (ok ? it : 0) * 16;
                f32x4 v[16];
#pragma unroll
                for (int i = 0; i < 16; ++i) v[i] = __builtin_bit_cast(f32x4, __builtin_amdgcn_raw_buffer_load_b128(rws, ko + i * 5472, 0, 16));
                f32x4 s = v[0];
#pragma unroll
                for (int i = 1; i < 16; ++i) s += v[i];
                const int hh = it >= 171, rem = it - 171 * hh, d = rem / 9, fq = rem - 9 * d;
                if (ok) LW(u32x2, L_KVT + hh * 1600 + d * 80 + fq * 8) = (u32x2){cvtpk(s[0], s[1]), cvtpk(s[2], s[3])};
            }
            if (tb == 3 && lane < 10) LW(u32x4, L_KVT + (lane >= 5) * 1600 + 19 * 80 + (lane % 5) * 16) = (u32x4){0u, 0u, 0u, 0u};
        }
        async_bar(gen, lane, ws);
        {
            const int hm = (n >> 2) & 1, d0 = 4 * (n >> 3) + (n & 3);
            const unsigned a0 = L_KVT + (hm == hi ? hm * 1600 + d0 * 80 : 19 * 80);
            const unsigned a1 = L_KVT + (hm == hi && d0 < 3 ? hm * 1600 + (16 + d0) * 80 : 19 * 80);
            f32x16 N0 = {}, N1 = {};
#pragma unroll
            for (int s = 0; s < 4; ++s) {
                const u32x4 bq = {pq[4 * s], pq[4 * s + 1], pq[4 * s + 2], pq[4 * s + 3]};
                N0 = MFMA(LA(u32x4, a0 + 16 * s), bq, N0); N1 = MFMA(LA(u32x4, a1 + 16 * s), bq, N1);
            }
            const u32x2 br = {pq[16], pq[17]};
            N0 = MFMA8(LA(u32x2, a0 + 64), br, N0); N1 = MFMA8(LA(u32x2, a1 + 64), br, N1);
            const float sc = gate / (N1[2] + 1e-6f);
            const unsigned fo = WS_OAF + (unsigned)(4 * ti + tb) * 36 * 1024;
            const u32x4 f0 = {cvtpk(N0[0] * sc, N0[1] * sc), cvtpk(N0[2] * sc, N0[3] * sc), cvtpk(N0[4] * sc, N0[5] * sc), cvtpk(N0[6] * sc, N0[7] * sc)};
            const u32x4 f1 = {cvtpk(N0[8] * sc, N0[9] * sc), cvtpk(N0[10] * sc, N0[11] * sc), cvtpk(N0[12] * sc, N0[13] * sc), cvtpk(N0[14] * sc, N0[15] * sc)};
            __builtin_amdgcn_raw_buffer_store_b128(f0, rws, fo + (2 * tj) * 1024 + lane * 16, 0, 16);
            __builtin_amdgcn_raw_buffer_store_b128(f1, rws, fo + (2 * tj + 1) * 1024 + lane * 16, 0, 16);
            __builtin_amdgcn_raw_buffer_store_b32(cvtpk(N1[0] * sc, N1[1] * sc), rws, fo + (32 + (tj >> 2)) * 1024 + (((tj >> 1) & 1) * 32 + n) * 16 + (2 * (tj & 1) + hi) * 4, 0, 16);
        }
        WAITV(0);
        async_bar(gen, lane, ws);
        if (tb == 0 && lane == 0) put_flag(ws, WS_FOA + (16 * ti + tj) * 8, tag);
        STAMP(10);
        const int tt = 4 * x8 + (r & 3), cg = r >> 2;
        unsigned wo[21];
        (void)wo;
        auto stage = [&](const char* w) {
            const char* wsl = w + cg * 144;
#pragma unroll 1
            for (int p = tb; p < 81; p += 4) {
                const int q = p * 64 + lane, row = q / 9, pc = q - row * 9;
                dma_s(p * 1024, wsl, row * 1152 + pc * 16);
            }
        };
        const int i16 = lane & 15, gr = i16 & 3, hk = g4 & 1, fh = g4 >> 1;
        const unsigned fro = (unsigned)(2 * tt + (tb >> 1)) * 36 * 1024 + fh * 1024 + (hk * 32 + n16 + 16 * (tb & 1)) * 16;
        const unsigned remo = (18 * (8 * fh + 4 * hk + (i16 >> 3)) + 16 + ((i16 >> 2) & 1)) * 144 + gr * 8;
        f32x4 T[5] = {};
        auto gemm = [&](unsigned fbase, unsigned maino, unsigned r4s) {
            u32x4 bf[18];
#pragma unroll
            for (int ks = 0; ks < 18; ++ks) bf[ks] = __builtin_bit_cast(u32x4, __builtin_amdgcn_raw_buffer_load_b128(rws, fbase + fro + ks * 2048, 0, 16));
#pragma unroll
            for (int ks = 0; ks < 18; ++ks) {
                const unsigned a = ks < 16 ? maino + ks * 36 * 144 : remo + (ks - 16) * 16 * 18 * 144;
                const unsigned s2 = ks < 16 ? r4s : 2 * 18 * 144;
#pragma unroll
                for (int cb = 0; cb < 5; ++cb) T[cb] = MFMA16(trd2(a + cb * 32, a + cb * 32 + s2), bf[ks], T[cb]);
            }
        };
        lds_wait<16>(C_BPROG, 4, ws);
        stage(P.woA);
        WAITV(0);
        async_bar(gen, lane, ws);
        STAMP(11);
        poll_flags(ws, WS_FOA + 16 * (tt >> 1) * 8, 16, tag, lane, 1);
        gemm(WS_OAF, (18 * hk + 8 * fh + (i16 >> 2)) * 144 + gr * 8, 4 * 144);
        async_bar(gen, lane, ws);
        STAMP(12);
        stage(P.woB);
        WAITV(0);
        async_bar(gen, lane, ws);
        STAMP(13);
        poll_flags(ws, WS_FOB + 16 * (tt >> 1) * 8, 16, tag, lane, 0);
        STAMP(14);
        gemm(WS_OBF, (18 * fh + 4 * hk + (i16 >> 2)) * 144 + gr * 8, 8 * 144);
        char* op = P.out + (size_t)(64 * tt + 16 * tb + n16) * 1152 + cg * 144 + g4 * 8;
#pragma unroll
        for (int cb = 0; cb < 5; ++cb) {
            const u32x2 o = {cvtpk(T[cb][0], T[cb][1]), cvtpk(T[cb][2], T[cb][3])};
            if (cb < 4 || g4 < 2) *(u32x2*)(op + cb * 32) = o;
        }
    }
}

__global__ __launch_bounds__(512, 2) void gated_mix_kernel(const void* x, const void* wqkv, const void* wphi, const void* woA, const void* wq, const void* wk,
                                                           const void* wv, const void* woB, const void* gate, void* out, void* ws) {
    extern __shared__ __attribute__((aligned(16))) char lds[];
    const unsigned tf = (unsigned)__builtin_amdgcn_s_memrealtime();
    const int lane = threadIdx.x & 63, wid = uni(threadIdx.x >> 6);
    const Ptrs P = {(const char*)x, (const char*)wqkv, (const char*)wphi, (const char*)woA, (const char*)wq, (const char*)wk, (const char*)wv, (const char*)woB, (const char*)gate, (char*)out, (char*)ws};
    if (wid < 4) role_main<true>(P, wid, lane); else role_main<false>(P, wid, lane);
    if (lane == 0) { u32x2 v = {tf, (unsigned)__builtin_amdgcn_s_memrealtime()}; *(u32x2*)((char*)ws + (9 << 20) + (blockIdx.x * 8 + wid) * 8) = v; }
}
__global__ void marker_kernel(void* ws) {
    const unsigned now = (unsigned)__builtin_amdgcn_s_memrealtime();
    unsigned ref = *(unsigned*)((char*)ws + (9 << 20));
    int smin = 0x7fffffff, smax = -0x7fffffff, emax = -0x7fffffff;
    for (int i = threadIdx.x; i < 2048; i += 64) { u32x2 v = *(u32x2*)((char*)ws + (9 << 20) + i * 8); smin = min(smin, (int)(v[0] - ref)); smax = max(smax, (int)(v[0] - ref)); emax = max(emax, (int)(v[1] - ref)); }
    for (int o = 1; o < 64; o <<= 1) { smin = min(smin, __shfl_xor(smin, o)); smax = max(smax, __shfl_xor(smax, o)); emax = max(emax, __shfl_xor(emax, o)); }
    unsigned d = MODE == 0 ? 0 : (MODE == 1 ? (int)(now - ref) - emax : (MODE == 2 ? emax - smin : smax - smin));
    d *= 4;
    const unsigned n2 = (unsigned)__builtin_amdgcn_s_memrealtime();
    if (d < 100000) while ((unsigned)__builtin_amdgcn_s_memrealtime() - n2 < d) __builtin_amdgcn_s_sleep(2);
}
}

extern "C" void kernel_launch(void* const* d_in, const int* in_sizes, int n_in, void* d_out, int out_size, void* d_ws, size_t ws_size, hipStream_t stream) {
    (void)in_sizes; (void)n_in; (void)out_size; (void)ws_size;
    (void)hipFuncSetAttribute((const void*)fk::gated_mix_kernel, hipFuncAttributeMaxDynamicSharedMemorySize, fk::LDS_BYTES);
    hipLaunchKernelGGL(fk::gated_mix_kernel, dim3(256), dim3(512), fk::LDS_BYTES, stream,
                       (const void*)d_in[0], (const void*)d_in[1], (const void*)d_in[2], (const void*)d_in[3], (const void*)d_in[5], (const void*)d_in[6],
                       (const void*)d_in[7], (const void*)d_in[8], (const void*)d_in[9], d_out, d_ws);
    hipLaunchKernelGGL(fk::marker_kernel, dim3(1), dim3(64), 0, stream, d_ws);
}
```

```cpp
#include <hip/hip_runtime.h>
#include <hip/hip_bf16.h>
#include <stdint.h>

namespace fk {
typedef __bf16 bf16x2 __attribute__((ext_vector_type(2)));
typedef __bf16 bf16x8 __attribute__((ext_vector_type(8)));
typedef short s16x4 __attribute__((ext_vector_type(4)));
typedef float f32x2 __attribute__((ext_vector_type(2)));
typedef float f32x4 __attribute__((ext_vector_type(4)));
typedef float f32x16 __attribute__((ext_vector_type(16)));
typedef unsigned u32x2 __attribute__((ext_vector_type(2)));
typedef unsigned u32x4 __attribute__((ext_vector_type(4)));
typedef unsigned long long u64;
typedef __attribute__((address_space(1))) u64 gu64;
typedef __attribute__((address_space(1))) unsigned gu32;

#define AS3 __attribute__((address_space(3)))
#define SBAR() __builtin_amdgcn_sched_barrier(0)
#define PIN(x) asm volatile("" : "+v"(x))
#define MFMA(a, b, c) __builtin_amdgcn_mfma_f32_32x32x16_bf16(__builtin_bit_cast(bf16x8, a), __builtin_bit_cast(bf16x8, b), c, 0, 0, 0)
#define MFMA8(a, b, c) __builtin_amdgcn_mfma_f32_32x32x8bf16_1k(__builtin_bit_cast(s16x4, a), __builtin_bit_cast(s16x4, b), c, 0, 0, 0)
#define MFMA1616(a, b, c) __builtin_amdgcn_mfma_f32_16x16x16bf16_1k(__builtin_bit_cast(s16x4, a), __builtin_bit_cast(s16x4, b), c, 0, 0, 0)
#define MFMA16(a, b, c) __builtin_amdgcn_mfma_f32_16x16x32_bf16(__builtin_bit_cast(bf16x8, a), __builtin_bit_cast(bf16x8, b), c, 0, 0, 0)
#define LA(T, a) (*(const AS3 T*)(uintptr_t)(a))
#define LW(T, a) (*(AS3 T*)(uintptr_t)(a))
#define FENCE() asm volatile("" ::: "memory")
#define EX(x) __builtin_amdgcn_exp2f(x)

#define WS_TMO 64
constexpr unsigned WS_FKV = 4096, WS_FKVP = 8192, WS_FOA = 12288, WS_FOB = 16384;
#define WS_G 32768
#define WS_KMAX 40960
#define WS_KVIMG 65536
#define WS_KVP 262144
#define WS_OAF (2 << 20)
#define WS_OBF (5 << 20)

constexpr unsigned SLOT = 36864, WOFF = 16384;
constexpr unsigned L_WKV = 110592;
constexpr unsigned L_RED = 152064, L_RES = 159744;
constexpr unsigned L_CNT = 161024, L_WPR = 161152;
constexpr unsigned L_WPHI = 152064, L_KVT = 157184;
constexpr unsigned L_PHIK = 0, L_VB = 20480;
constexpr unsigned L_KMAX = 161152, L_WOS = 37888;
constexpr unsigned KB_BYTES = 2240, STEP_BYTES = 9472, KR_OFF = 8960;
constexpr unsigned LDS_BYTES = 163840;
constexpr unsigned C_LAND0 = L_CNT, C_LAND1 = L_CNT + 4, C_LAND2 = L_CNT + 8, C_PHI = L_CNT + 12, C_ASY = L_CNT + 16, C_BPROG = L_CNT + 20, C_BDONE = L_CNT + 24, C_RAW = L_CNT + 28, C_WKVF = L_CNT + 32;
constexpr unsigned POISON = 0xffffffffu;

#define STAMP(k) do { } while (0)

__device__ __forceinline__ unsigned cvtpk(float lo, float hi) { f32x2 v = {lo, hi}; return __builtin_bit_cast(unsigned, __builtin_convertvector(v, bf16x2)); }
__device__ __forceinline__ int uni(int v) { return __builtin_amdgcn_readfirstlane(v); }

__device__ __forceinline__ void dma_s(unsigned lds, const void* sbase, unsigned voff) {
    asm volatile("s_mov_b32 m0, %0\n\ts_nop 0\n\tglobal_load_lds_dwordx4 %1, %2" :: "s"(lds), "v"(voff), "s"(sbase) : "memory", "m0");
}
__device__ __forceinline__ void dma_s_sc1(unsigned lds, const void* sbase, unsigned voff) {
    asm volatile("s_mov_b32 m0, %0\n\ts_nop 0\n\tglobal_load_lds_dwordx4 %1, %2 sc1" :: "s"(lds), "v"(voff), "s"(sbase) : "memory", "m0");
}
__device__ __forceinline__ void dma_v(unsigned lds, const void* vaddr) {
    asm volatile("s_mov_b32 m0, %0\n\ts_nop 0\n\tglobal_load_lds_dwordx4 %1, off" :: "s"(lds), "v"(vaddr) : "memory", "m0");
}
#define WAITV(n) asm volatile("s_waitcnt vmcnt(" #n ")" ::: "memory")
#define BAR_L() asm volatile("s_waitcnt lgkmcnt(0)\n\ts_barrier" ::: "memory")
template <int N> __device__ __forceinline__ void waitv() { asm volatile("s_waitcnt vmcnt(%0)" :: "n"(N) : "memory"); }
template <int N> __device__ __forceinline__ void bar_vl() { asm volatile("s_waitcnt vmcnt(%0) lgkmcnt(0)\n\ts_barrier" :: "n"(N) : "memory"); }

__device__ __forceinline__ u32x2 trd(unsigned a) { return __builtin_bit_cast(u32x2, __builtin_amdgcn_ds_read_tr16_b64_v4i16((AS3 s16x4*)(uintptr_t)a)); }
__device__ __forceinline__ u32x4 trd2(unsigned a0, unsigned a1) { const u32x2 p = trd(a0), q = trd(a1); return (u32x4){p[0], p[1], q[0], q[1]}; }

template <int CTRL> __device__ __forceinline__ float dppf(float v) { return __builtin_bit_cast(float, __builtin_amdgcn_update_dpp(0, __builtin_bit_cast(int, v), CTRL, 0xf, 0xf, true)); }
__device__ __forceinline__ float wave_max(float v) {
    v = __builtin_fmaxf(v, dppf<0x121>(v)); v = __builtin_fmaxf(v, dppf<0x122>(v)); v = __builtin_fmaxf(v, dppf<0x124>(v)); v = __builtin_fmaxf(v, dppf<0x128>(v));
    { const auto t = __builtin_amdgcn_permlane16_swap(__float_as_uint(v), __float_as_uint(v), false, false); v = __builtin_fmaxf(__uint_as_float(t[0]), __uint_as_float(t[1])); }
    { const auto t = __builtin_amdgcn_permlane32_swap(__float_as_uint(v), __float_as_uint(v), false, false); v = __builtin_fmaxf(__uint_as_float(t[0]), __uint_as_float(t[1])); }
    return v;
}
__device__ __forceinline__ u64 mix64(u64 z) { z += 0x9E3779B97F4A7C15ull; z = (z ^ (z >> 30)) * 0xBF58476D1CE4E5B9ull; z = (z ^ (z >> 27)) * 0x94D049BB133111EBull; return z ^ (z >> 31); }

constexpr unsigned SPIN_MAX = 1u << 20;
__device__ __forceinline__ void poll_flags(const char* ws, unsigned off, int n, u64 tag, int lane, int slp) {
    const gu64* p = (const gu64*)(ws + off) + (lane < n ? lane : 0);
    for (unsigned s = 0;; ++s) {
        const u64 v = __hip_atomic_load(p, __ATOMIC_RELAXED, __HIP_MEMORY_SCOPE_AGENT);
        if (__all(v == tag)) break;
        if (s > SPIN_MAX) { if (lane == 0) __hip_atomic_store((gu32*)(ws + WS_TMO), 0x100u + off, __ATOMIC_RELAXED, __HIP_MEMORY_SCOPE_AGENT); break; }
        if (slp) __builtin_amdgcn_s_sleep(8); else __builtin_amdgcn_s_sleep(1);
    }
    FENCE();
}
__device__ __forceinline__ void poll4(const char* ws, u64 tag, int lane) {
    const gu64* p = (const gu64*)(ws + WS_FKV) + lane;
    for (unsigned s = 0;; ++s) {
        const u64 v0 = __hip_atomic_load(p, __ATOMIC_RELAXED, __HIP_MEMORY_SCOPE_AGENT), v1 = __hip_atomic_load(p + 64, __ATOMIC_RELAXED, __HIP_MEMORY_SCOPE_AGENT);
        const u64 v2 = __hip_atomic_load(p + 128, __ATOMIC_RELAXED, __HIP_MEMORY_SCOPE_AGENT), v3 = __hip_atomic_load(p + 192, __ATOMIC_RELAXED, __HIP_MEMORY_SCOPE_AGENT);
        if (__all(v0 == tag && v1 == tag && v2 == tag && v3 == tag)) break;
        if (s > SPIN_MAX) { if (lane == 0) __hip_atomic_store((gu32*)(ws + WS_TMO), 0x100u, __ATOMIC_RELAXED, __HIP_MEMORY_SCOPE_AGENT); break; }
        __builtin_amdgcn_s_sleep(1);
    }
    FENCE();
}
__device__ __forceinline__ void put_flag(char* ws, unsigned off, u64 tag) { __hip_atomic_store((gu64*)(ws + off), tag, __ATOMIC_RELAXED, __HIP_MEMORY_SCOPE_AGENT); }
__device__ __forceinline__ void lds_inc(unsigned a, int lane) { if (lane == 0) __hip_atomic_fetch_add((AS3 unsigned*)(uintptr_t)a, 1u, __ATOMIC_RELAXED, __HIP_MEMORY_SCOPE_WORKGROUP); }
template <int SLP> __device__ __forceinline__ void lds_wait(unsigned a, unsigned target, const char* ws) {
    for (unsigned s = 0;; ++s) {
        const unsigned v = uni(__hip_atomic_load((AS3 unsigned*)(uintptr_t)a, __ATOMIC_RELAXED, __HIP_MEMORY_SCOPE_WORKGROUP));
        if (v >= target) break;
        if (s > SPIN_MAX) { __hip_atomic_store((gu32*)(ws + WS_TMO), 0x10u + (a & 15), __ATOMIC_RELAXED, __HIP_MEMORY_SCOPE_AGENT); break; }
        __builtin_amdgcn_s_sleep(SLP);
    }
    FENCE();
}
__device__ __forceinline__ void async_bar(unsigned& gen, int lane, const char* ws) {
    asm volatile("s_waitcnt lgkmcnt(0)" ::: "memory");
    gen += 4; lds_inc(C_ASY, lane); lds_wait<2>(C_ASY, gen, ws);
}

__device__ __forceinline__ void features(const f32x16& a0, float p16, float p17, float p18, float p19, int lane, unsigned (&out)[18]) {
    const u32x4 b0 = {cvtpk(a0[0], a0[1]), cvtpk(a0[2], a0[3]), cvtpk(a0[4], a0[5]), cvtpk(a0[6], a0[7])};
    const u32x4 b1 = {cvtpk(a0[8], a0[9]), cvtpk(a0[10], a0[11]), cvtpk(a0[12], a0[13]), cvtpk(a0[14], a0[15])};
    const u32x2 b2 = {cvtpk(p16, p17), cvtpk(p18, p19)};
    const unsigned wa = L_WPHI + lane * 16, wr = L_WPHI + 2048 + lane * 8;
    f32x16 P0 = {}, P1 = {};
    P0 = MFMA(LA(u32x4, wa), b0, P0); P1 = MFMA(LA(u32x4, wa + 2560), b0, P1);
    P0 = MFMA(LA(u32x4, wa + 1024), b1, P0); P1 = MFMA(LA(u32x4, wa + 2560 + 1024), b1, P1);
    P0 = MFMA8(LA(u32x2, wr), b2, P0); P1 = MFMA8(LA(u32x2, wr + 2560), b2, P1);
    float p[18];
#pragma unroll
    for (int i = 0; i < 16; ++i) p[i] = P0[i];
    p[16] = P1[0]; p[17] = P1[1];
    float mx = p[0], mn = p[0];
#pragma unroll
    for (int i = 1; i < 18; ++i) { mx = __builtin_fmaxf(mx, p[i]); mn = __builtin_fminf(mn, p[i]); }
    const float L = 1.4426950408889634f, mxl = -mx * L, mnl = mn * L;
    float ep[18], en[18], sp = 0.f, sn = 0.f;
#pragma unroll
    for (int i = 0; i < 18; ++i) { ep[i] = EX(__builtin_fmaf(p[i], L, mxl)); en[i] = EX(__builtin_fmaf(p[i], -L, mnl)); sp += ep[i]; sn += en[i]; }
    const float rp = __builtin_amdgcn_rcpf(sp), rn = __builtin_amdgcn_rcpf(sn);
#pragma unroll
    for (int u = 0; u < 9; ++u) { out[u] = cvtpk(ep[2 * u] * rp, ep[2 * u + 1] * rp); out[9 + u] = cvtpk(en[2 * u] * rn, en[2 * u + 1] * rn); }
}

struct Ptrs { const char *x, *wqkv, *wphi, *woA, *wq, *wk, *wv, *woB, *gate; char *out, *ws; };

template <bool ISB>
__device__ __forceinline__ void role_main(const Ptrs& P, const int wid, const int lane) {
    const int b = blockIdx.x, x8 = b & 7, r = b >> 3;
    const int ti = 8 * (x8 & 1) + (r & 7), tj = 4 * (x8 >> 1) + (r >> 3);
    const int tb = wid & 3, n = lane & 31, hi = lane >> 5, n16 = lane & 15, g4 = lane >> 4;
    const int tid = wid * 64 + lane;
    char* const ws = P.ws;
    const __amdgpu_buffer_rsrc_t rws = __builtin_amdgcn_make_buffer_rsrc((void*)ws, (short)0, 0x1000000, 0x00020000);
    STAMP(0);
    if (tid == 0) { LW(u32x4, L_CNT) = (u32x4){0u, 0u, 0u, 0u}; LW(u32x4, L_CNT + 16) = (u32x4){0u, 0u, 0u, 0u}; LW(u32x4, L_CNT + 32) = (u32x4){0u, 0u, 0u, 0u}; }
    BAR_L();
    if (ISB) STAMP(28);
    u32x2 Ev;
    const unsigned obf = WS_OBF + (unsigned)(4 * ti + tb) * 36 * 1024, obm = obf + 2 * tj * 1024 + lane * 16;
    const unsigned obr = obf + (32 + (tj >> 2)) * 1024 + (((tj >> 1) & 1) * 32 + n) * 16 + (tj & 1) * 8;
    unsigned xo[4]; u64 wb[5]; unsigned wst[5];
    auto dma1 = [&](int c, int i) {
        const unsigned slot = (c & 3) * SLOT;
        if (i < 4) dma_s(slot + (4 * tb + i) * 1024, P.x + c * 128, xo[i]);
        else { dma_v(slot + WOFF + (5 * tb + i - 4) * 1024, (const char*)wb[i - 4]); wb[i - 4] += wst[i - 4]; }
    };
    auto issue = [&](int c) {
#pragma unroll
        for (int i = 0; i < 9; ++i) dma1(c, i);
    };
#pragma unroll
    for (int i = 0; i < 4; ++i) xo[i] = (unsigned)(128 * ti + 32 * tb + 8 * i + (lane >> 3)) * 1152u + (((lane & 7) ^ (4 * (i & 1) + (lane >> 4))) * 16);
    if (!ISB) {
        u32x4 xf[5], gf[5];
        {
            const unsigned go = (32 * tb + 8 * g4) * 2, g4o = tb < 2 ? go + 1024 : go, zo = 0;
            const unsigned xr = (unsigned)(128 * ti + 8 * tj + (n16 & 7)) * 1152u, xa = xr + go, x4 = xr + g4o;
            asm volatile("global_load_dwordx2 %0, %11, %16 sc1\n\t"
                         "global_load_dwordx4 %1, %12, %17\n\tglobal_load_dwordx4 %2, %12, %17 offset:256\n\tglobal_load_dwordx4 %3, %12, %17 offset:512\n\t"
                         "global_load_dwordx4 %4, %12, %17 offset:768\n\tglobal_load_dwordx4 %5, %13, %17\n\t"
                         "global_load_dwordx4 %6, %14, %18\n\tglobal_load_dwordx4 %7, %14, %18 offset:256\n\tglobal_load_dwordx4 %8, %14, %18 offset:512\n\t"
                         "global_load_dwordx4 %9, %14, %18 offset:768\n\tglobal_load_dwordx4 %10, %15, %18"
                         : "=&v"(Ev), "=&v"(xf[0]), "=&v"(xf[1]), "=&v"(xf[2]), "=&v"(xf[3]), "=&v"(xf[4]), "=&v"(gf[0]), "=&v"(gf[1]), "=&v"(gf[2]), "=&v"(gf[3]), "=&v"(gf[4])
                         : "v"(zo), "v"(xa), "v"(x4), "v"(go), "v"(g4o), "s"(ws), "s"(P.x), "s"(P.gate) : "memory");
        }
        STAMP(28);
#pragma unroll
        for (int t = 0; t < 10; ++t) {
            const int p = tb + 4 * t;
            if (t != 5 || tb != 0) dma_s(L_WKV + p * 1024, p < 20 ? P.wk + p * 1024 : P.wv + (p * 1024 - 20736), lane * 16);
            else { if (lane < 16) dma_s(L_WKV + 20 * 1024, P.wk + 20 * 1024, lane * 16); else dma_s(L_WKV + 20 * 1024, P.wv - 256, lane * 16); }
        }
        if (tb == 1) { if (lane < 32) dma_s(L_WKV + 40 * 1024, P.wv + (40 * 1024 - 20736), lane * 16); }
        if (tb == 2) { dma_s(L_WPR, P.wphi + tj * 1296, lane * 16); if (lane < 17) dma_s(L_WPR + 1024, P.wphi + tj * 1296 + 1024, lane * 16); }
        STAMP(29);
        {
            const u32x4 pz = {POISON, POISON, POISON, POISON};
            __builtin_amdgcn_raw_buffer_store_b128(pz, rws, obm, 0, 16);
            __builtin_amdgcn_raw_buffer_store_b128(pz, rws, obm + 1024, 0, 16);
            __builtin_amdgcn_raw_buffer_store_b64((u32x2){POISON, POISON}, rws, obr, 0, 16);
        }
        STAMP(30);
        asm volatile("s_barrier" ::: "memory");
        STAMP(16);
        asm volatile("s_waitcnt vmcnt(3)" : "+v"(Ev), "+v"(xf[0]), "+v"(xf[1]), "+v"(xf[2]), "+v"(xf[3]), "+v"(xf[4]), "+v"(gf[0]), "+v"(gf[1]), "+v"(gf[2]), "+v"(gf[3]), "+v"(gf[4]) :: "memory");
        lds_inc(C_RAW, lane); lds_wait<1>(C_RAW, 4, ws);
        STAMP(1);
        const int i = lane & 15;
        const unsigned rowa = L_WKV + (32 * tb + 8 * g4 + 2 * (i >> 2)) * 36;
        const unsigned am = rowa + 8 * (i & 3), ar = rowa + (i & 1) * 20736 + 32;
        u32x2 we0[5], we1[5], wr[5], wo0[5], wo1[5];
#pragma unroll
        for (int s = 0; s < 5; ++s) {
            const unsigned o = (s < 4 || tb < 2 ? s : 0) * 128 * 36;
            we0[s] = trd(am + o); we1[s] = trd(am + o + 20736); wr[s] = trd(ar + o); wo0[s] = trd(am + o + 40); wo1[s] = trd(am + o + 40 + 20736);
        }
        SBAR();
#pragma unroll
        for (int s = 0; s < 5; ++s) { PIN(we0[s]); PIN(we1[s]); PIN(wr[s]); PIN(wo0[s]); PIN(wo1[s]); }
        lds_inc(C_WKVF, lane);
        f32x4 E0 = {}, E1 = {}, E2 = {}, O0 = {}, O1 = {}, O2 = {}, G = {};
#pragma unroll
        for (int s = 0; s < 5; ++s) {
            if (s < 4 || tb < 2) {
                const u32x2 be = {__builtin_amdgcn_perm(xf[s][1], xf[s][0], 0x05040100u), __builtin_amdgcn_perm(xf[s][3], xf[s][2], 0x05040100u)};
                const u32x2 bo = {__builtin_amdgcn_perm(xf[s][1], xf[s][0], 0x07060302u), __builtin_amdgcn_perm(xf[s][3], xf[s][2], 0x07060302u)};
                E0 = MFMA1616(we0[s], be, E0); E1 = MFMA1616(we1[s], be, E1); E2 = MFMA1616(wr[s], be, E2);
                O0 = MFMA1616(wo0[s], bo, O0); O1 = MFMA1616(wo1[s], bo, O1); O2 = MFMA1616(wr[s], bo, O2);
                G = MFMA16(gf[s], xf[s], G);
            }
        }
        const int ra_ = ((lane - 16) & 63) * 4;
        auto rot16 = [&](float v) { return __int_as_float(__builtin_amdgcn_ds_bpermute(ra_, __float_as_int(v))); };
        const float k0 = rot16(O0[2]), k1 = rot16(O0[3]), v0 = rot16(O1[2]), v1 = rot16(O1[3]), w0 = rot16(O2[2]), w1 = rot16(O2[3]);
        const bool g0 = g4 == 0, up = n16 >= 8;
        const f32x4 K = {E0[0] + (g0 ? O2[2] : k0), E0[1] + (g0 ? O2[3] : k1), E0[2] + O0[0], E0[3] + O0[1]};
        const f32x4 V = {E1[0] + (g0 ? w0 : v0), E1[1] + (g0 ? w1 : v1), E1[2] + O1[0], E1[3] + O1[1]};
        const unsigned ra = L_RED + tb * 1280 + (n16 & 7) * 160;
        LW(f32x4, ra + (up ? 64 : 0) + g4 * 16) = up ? V : K;
        const f32x2 rm = g0 ? (f32x2){E2[0] + k0, E2[1] + k1} : (g4 == 3 ? (f32x2){E2[0] + O1[2], E2[1] + O1[3]} : (f32x2){G[0], G[0]});
        if (!up && g4 != 2) LW(f32x2, ra + (g0 ? 128 : (g4 == 3 ? 136 : 144))) = rm;
        STAMP(2);
    } else {
        { const unsigned zo = 0; asm volatile("global_load_dwordx2 %0, %1, %2 sc1" : "=&v"(Ev) : "v"(zo), "s"(ws) : "memory"); }
        const int c0 = 72 * tj - 8 * (tj & 1);
#pragma unroll
        for (int i = 0; i < 5; ++i) {
            const unsigned q = (5 * tb + i) * 64 + lane, row = q / 20, wi = q - row * 20, ty = wi / 5, pc = wi - ty * 5;
            const bool isq = ty == 3;
            const unsigned rs = isq ? 1152u : 3456u, to = ty == 0 ? 1152u : (ty == 2 ? 2304u : 0u);
            wb[i] = (isq ? (u64)P.wq : (u64)P.wqkv) + (row * rs + to + c0 + pc * 16);
            wst[i] = rs * 64;
        }
        STAMP(17);
        asm volatile("s_barrier" ::: "memory");
        STAMP(29);
        issue(0);
        STAMP(30);
        issue(1);
        STAMP(31);
        issue(2);
        STAMP(18);
    }
    f32x16 acc0 = {}, acc1 = {}, acc2 = {};
    unsigned wa[2][3], xk[2][4];
    {
        const int i = lane & 15, sl = 2 * (g4 & 1) + ((i & 3) >> 1);
        const unsigned base = WOFF + (8 * (g4 >> 1) + (i >> 2)) * 320 + 8 * (tj & 1) + 32 * (i & 1);
        if (ISB) {
            wa[0][0] = base + 240 + 8 * sl;
            wa[0][1] = base + (sl == 0 ? 240 + 32 : 160 + 8 * (sl - 1));
            wa[0][2] = 0;
        } else {
            wa[0][0] = base + 8 * sl;
            wa[0][1] = base + 80 + 8 * sl;
            wa[0][2] = base + (sl == 0 ? 32 : (sl == 1 ? 80 + 32 : 160 + 24 + 8 * (sl - 2)));
        }
        const unsigned xa = (32 * tb + n) * 128 + ((hi ^ ((n >> 1) & 7)) * 16);
#pragma unroll
        for (int k = 0; k < 4; ++k) { xk[0][k] = xa ^ (k * 32); xk[1][k] = xk[0][k] + 2 * SLOT; PIN(xk[0][k]); PIN(xk[1][k]); }
#pragma unroll
        for (int m = 0; m < (ISB ? 2 : 3); ++m) { wa[1][m] = wa[0][m] + 2 * SLOT; PIN(wa[0][m]); PIN(wa[1][m]); }
    }
#define WPHI_AT 2
    constexpr bool NODMA = false;
#define HM(acc, a, b) acc = MFMA(a, b, acc)
    constexpr bool HRD = true;
    u32x4 FX[4], FW0[4], FW1[4], FW2[4];
    auto rdx = [&](int c, int ks) { return LA(u32x4, xk[(c >> 1) & 1][ks] + (c & 1) * SLOT); };
    auto rdw = [&](int c, int ks, int m) { const unsigned a = wa[(c >> 1) & 1][m] + (c & 1) * SLOT + ks * 5120; return trd2(a, a + 1280); };
    if (!ISB) BAR_L();
    else asm volatile("s_waitcnt vmcnt(18)\n\ts_barrier" : "+v"(Ev) :: "memory");
    const u64 tag = mix64(((u64)(unsigned)uni(Ev[1]) << 32) | (unsigned)uni(Ev[0]));
    STAMP(3);
    if (!ISB) {
        if (tb == 3) {
            const int t = lane & 7, bi = tj >> 2, pc = tj & 3;
            const int rho = 16 * (pc >> 1) + 8 * (t >> 2) + 4 * (pc & 1) + (t & 3);
            const int d = lane - 16;
            unsigned base, strd;
            if (lane < 16) { base = t * 40 + 8 * (lane >> 3); strd = 1; }
            else if (lane < 40) { base = d < 16 ? 16 + d : (d < 18 ? 18 + d : 0); strd = 40; }
            else if (lane < 48) { base = t * 40 + 32; strd = 1; }
            else { base = t * 40 + 36; strd = 0; }
            float v[8];
#pragma unroll
            for (int i = 0; i < 8; ++i) {
                const unsigned ad = L_RED + (base + i * strd) * 4;
                v[i] = (LA(float, ad) + LA(float, ad + 1280)) + (LA(float, ad + 2560) + LA(float, ad + 3840));
            }
            float ss = v[0] * v[0] + v[1] * v[1];
            float s6 = 0.f;
#pragma unroll
            for (int i = 2; i < 8; ++i) s6 += v[i] * v[i];
            if (lane < 16) ss += s6;
            u32x4 o = {cvtpk(v[0], v[1]), cvtpk(v[2], v[3]), cvtpk(v[4], v[5]), cvtpk(v[6], v[7])};
            if (lane == 34) o = (u32x4){0x3f803f80u, 0x3f803f80u, 0x3f803f80u, 0x3f803f80u};
            const unsigned img = WS_KVIMG + ti * STEP_BYTES;
            const unsigned o16 = lane < 16 ? img + bi * KB_BYTES + ((lane >> 3) * 32 + rho) * 16 : img + bi * KB_BYTES + 1024 + pc * 304 + d * 16;
            if (lane < 35) __builtin_amdgcn_raw_buffer_store_b128(o, rws, o16, 0, 16);
            float tot = __shfl(ss, t) + __shfl(ss, 8 + t) + __shfl(ss, 40 + t);
            tot = __builtin_fmaxf(tot, dppf<0xb1>(tot)); tot = __builtin_fmaxf(tot, dppf<0x4e>(tot)); tot = __builtin_fmaxf(tot, dppf<0x141>(tot));
            const float gv = __builtin_amdgcn_rcpf(1.f + EX(-1.4426950408889634f * v[0]));
            unsigned o4, d4;
            if (lane < 48) { o4 = img + KR_OFF + (bi >> 1) * 256 + rho * 8 + (bi & 1) * 4; d4 = o[0]; }
            else if (lane < 56) { o4 = WS_G + (128 * ti + 8 * tj + t) * 4; d4 = __float_as_uint(gv); }
            else { o4 = WS_KMAX + (16 * ti + tj) * 4; d4 = __float_as_uint(tot); }
            if (lane >= 40 && lane <= 56) __builtin_amdgcn_raw_buffer_store_b32(d4, rws, o4, 0, 16);
        }
    }
    auto build_wphi = [&]() {
            const int hm = (n >> 2) & 1;
            auto wval = [&](int mb, int pos) {
                const int f = 16 * mb + 4 * (n >> 3) + (n & 3), d = pos - 2 * hi;
                const bool ok = hm == hi && f < 18 && d >= 0 && d < 18;
                const unsigned e = LA(unsigned short, L_WPR + (ok ? (hi * 324 + d * 18 + f) * 2 : 0));
                return ok ? e : 0u;
            };
            if (tb < 2) {
                const int mb = tb;
                unsigned v[16];
#pragma unroll
                for (int pos = 0; pos < 16; ++pos) v[pos] = wval(mb, pos);
                LW(u32x4, L_WPHI + mb * 2560 + lane * 16) = (u32x4){v[0] | (v[1] << 16), v[2] | (v[3] << 16), v[4] | (v[5] << 16), v[6] | (v[7] << 16)};
                LW(u32x4, L_WPHI + mb * 2560 + 1024 + lane * 16) = (u32x4){v[8] | (v[9] << 16), v[10] | (v[11] << 16), v[12] | (v[13] << 16), v[14] | (v[15] << 16)};
            } else {
#pragma unroll
                for (int mb = 0; mb < 2; ++mb)
                    LW(u32x2, L_WPHI + mb * 2560 + 2048 + lane * 8) = (u32x2){wval(mb, 16) | (wval(mb, 17) << 16), wval(mb, 18) | (wval(mb, 19) << 16)};
            }
    };
    SBAR();
#pragma unroll
    for (int ks = 0; ks < 2; ++ks) { FX[ks] = rdx(0, ks); FW0[ks] = rdw(0, ks, 0); FW1[ks] = rdw(0, ks, 1); if (!ISB) FW2[ks] = rdw(0, ks, 2); }
    u32x2 fl0, fl1, fl2, fl3;
    SBAR();
#pragma unroll
    for (int c = 0; c < 9; ++c) {
#pragma unroll
        for (int ks = 0; ks < 4; ++ks) {
            if (ks == 0 && c == 0 && ISB) lds_wait<1>(C_WKVF, 4, ws);
            if (ks == 0 && c == 6 && !ISB) {
                if (wid == 7) {
                    const unsigned fo = WS_FKV + lane * 8;
                    asm volatile("global_load_dwordx2 %0, %4, %5 sc1\n\tglobal_load_dwordx2 %1, %4, %5 offset:512 sc1\n\t"
                                 "global_load_dwordx2 %2, %4, %5 offset:1024 sc1\n\tglobal_load_dwordx2 %3, %4, %5 offset:1536 sc1"
                                 : "=&v"(fl0), "=&v"(fl1), "=&v"(fl2), "=&v"(fl3) : "v"(fo), "s"(ws) : "memory");
                }
            }
            if (ks == 2) {
                if (ISB) {
                    if (c == 0) bar_vl<18>(); else if (c == 1) bar_vl<14>(); else if (c <= 5) bar_vl<10>(); else if (c == 6) bar_vl<5>(); else if (c == 7) bar_vl<0>(); else BAR_L();
                } else if (c == 1) {
                    bar_vl<4>();
                    if (tb == 3 && lane == 0) put_flag(ws, WS_FKV + (16 * ti + tj) * 8, tag);
                } else if (c >= 3 && c <= 5) bar_vl<8>();
                else if (c == 6) { if (tb == 3) WAITV(8); else WAITV(4); BAR_L(); }
                else if (c == 7) {
                    if (tb == 3) {
                        asm volatile("s_waitcnt vmcnt(0)" : "+v"(fl0), "+v"(fl1), "+v"(fl2), "+v"(fl3) :: "memory");
                        const unsigned t0 = (unsigned)tag, t1 = (unsigned)(tag >> 32);
                        const bool seen = fl0[0] == t0 && fl0[1] == t1 && fl1[0] == t0 && fl1[1] == t1 && fl2[0] == t0 && fl2[1] == t1 && fl3[0] == t0 && fl3[1] == t1;
                        if (!__all(seen)) poll4(ws, tag, lane);
                        if (b == 0 && lane == 0) __hip_atomic_store((gu64*)ws, tag, __ATOMIC_RELAXED, __HIP_MEMORY_SCOPE_AGENT);
                    }
                    bar_vl<0>();
                } else BAR_L();
                if (!ISB && c == WPHI_AT) { if (tb < 3) build_wphi(); }
                if (c == 0) STAMP(20);
                if (c == 1) STAMP(21);
                if (c == 3) STAMP(4);
                if (c == 5) STAMP(22);
                if (c == 7) STAMP(23);
                if (c == 8) STAMP(6);
            }
            const int lc = ks < 2 ? c : c + 1, lk = (ks + 2) & 3;
            const bool ld = lc < 9;
            const int dc = ks >= 2 ? c + 4 : c + 3;
            const int p0 = dc == 3 ? 5 * ks : (ISB ? (ks == 2 ? 4 : (ks == 3 ? 5 : (ks == 0 ? 6 : 8))) : (ks + 2) & 3);
            const int p1 = NODMA || dc >= 9 ? p0 : (dc == 3 ? (!ISB ? p0 : (ks == 0 ? 5 : 9)) : (ISB ? (ks == 2 ? 5 : (ks == 3 ? 6 : (ks == 0 ? 8 : 9))) : p0 + 1));
            const int pm = p0 + (p1 - p0 + 1) / 2;
            const int ei = 4 * (c - 7) + ks - 2;
            auto early = [&](int e) {
                if (e < 9) { const unsigned p = 37 + tb + 4 * e; dma_s_sc1(p * 1024, ws + WS_KVIMG + p * 1024, lane * 16); }
                else dma_s_sc1(tb == 1 ? L_KMAX : 73 * 1024, ws + (tb == 1 ? WS_KMAX : WS_KVIMG + 73 * 1024), lane * 16);
            };
            HM(acc0, FW0[ks], FX[ks]); SBAR();
            if (ld && HRD) { FX[lk] = rdx(lc, lk); FW0[lk] = rdw(lc, lk, 0); } SBAR();
#pragma unroll
            for (int p = p0; p < pm; ++p) dma1(dc, p);
            if (ISB && ei >= 0 && ei < 4) { early(ei < 2 ? 3 * ei : 2 * ei + 2); if (ei < 2) early(3 * ei + 1); }
            SBAR();
            HM(acc1, FW1[ks], FX[ks]); SBAR();
            if (ld && HRD) FW1[lk] = rdw(lc, lk, 1); SBAR();
#pragma unroll
            for (int p = pm; p < p1; ++p) dma1(dc, p);
            if (ISB && ei >= 0 && ei < 4) early(ei < 2 ? 3 * ei + 2 : 2 * ei + 3);
            SBAR();
            if (!ISB) { HM(acc2, FW2[ks], FX[ks]); SBAR(); if (ld && HRD) FW2[lk] = rdw(lc, lk, 2); SBAR(); }
        }
    }
    STAMP(5);
    const unsigned vrow = L_VB + (32 * tb + n) * 112 + hi * 52 + 8;

    if (ISB) {
#pragma unroll
        for (int u = 0; u < 6; ++u) LW(unsigned, vrow + 4 * u) = cvtpk(acc1[4 + 2 * u], acc1[5 + 2 * u]);
        asm volatile("s_waitcnt vmcnt(0) lgkmcnt(0)" ::: "memory");
        lds_inc(C_PHI, lane); lds_inc(C_LAND0, lane);
        const float gate = __uint_as_float(__builtin_amdgcn_raw_buffer_load_b32(rws, WS_G + (128 * ti + 32 * tb + n) * 4, 0, 16));
        u32x4 QA, QB; u32x2 QRA0, QRA1, QRB0, QRB1; f32x16 MA, MB;
        {
            const float sc = 0.23570226039551584f * 1.4426950408889634f;
            float q[20];
#pragma unroll
            for (int i = 0; i < 16; ++i) q[i] = acc0[i] * sc;
#pragma unroll
            for (int i = 0; i < 4; ++i) q[16 + i] = acc1[i] * sc;
            float n2 = hi ? q[18] * q[18] + q[19] * q[19] : q[0] * q[0] + q[1] * q[1];
#pragma unroll
            for (int i = 2; i < 18; ++i) n2 = __builtin_fmaf(q[i], q[i], n2);
            unsigned cc[10], D[9];
#pragma unroll
            for (int u = 0; u < 10; ++u) cc[u] = cvtpk(q[2 * u], q[2 * u + 1]);
#pragma unroll
            for (int u = 0; u < 9; ++u) D[u] = hi ? cc[u + 1] : cc[u];
#pragma unroll
            for (int u = 0; u < 4; ++u) { const auto sw = __builtin_amdgcn_permlane32_swap(D[u], D[u + 4], false, false); QA[u] = sw[0]; QB[u] = sw[1]; }
            QRA0 = (u32x2){hi ? 0u : D[8], 0u}; QRA1 = (u32x2){0u, hi ? 0u : D[8]};
            QRB0 = (u32x2){hi ? D[8] : 0u, 0u}; QRB1 = (u32x2){0u, hi ? D[8] : 0u};
            SBAR();
            lds_wait<1>(C_LAND0, 4, ws);
            const f32x4 km4 = LA(f32x4, L_KMAX + lane * 16);
            const float km = wave_max(__builtin_fmaxf(__builtin_fmaxf(km4[0], km4[1]), __builtin_fmaxf(km4[2], km4[3])));
            const float m = -1.01f * __builtin_amdgcn_sqrtf(n2 * km);
            const auto sw = __builtin_amdgcn_permlane32_swap(__float_as_uint(m), __float_as_uint(m), false, false);
            const float ma = __uint_as_float(sw[0]), mb = __uint_as_float(sw[1]);
#pragma unroll
            for (int i = 0; i < 16; ++i) { MA[i] = ma; MB[i] = mb; }
            PIN(MA); PIN(MB); PIN(QA); PIN(QB); PIN(QRA0); PIN(QRA1); PIN(QRB0); PIN(QRB1);
        }
        STAMP(7);
        unsigned kp = 4 * STEP_BYTES + lane * 16, rp = 4 * STEP_BYTES + KR_OFF + n * 8, vp = 4 * STEP_BYTES + 1024 + hi * 304 + (n < 18 ? n : 18) * 16;
        if (n > 18) vp = 0x80000;
        PIN(kp); PIN(rp); PIN(vp);
        f32x16 OA = {}, OB = {}, S0A, S0B, S1A, S1B;
        u32x4 KMa, KMb, V0a, V0b, V1a, V1b = {}, P0 = {}, P1 = {}; u32x2 KRX, KRY;
        float e0, e1, e2, e3, f0 = 0.f, f1 = 0.f, f2 = 0.f, f3 = 0.f;
        KRX = LA(u32x2, rp); KMa = LA(u32x4, kp);
        S0A = MFMA(KMa, QA, MA); S0B = MFMA(KMa, QB, MB); S0A = MFMA8(KRX, QRA0, S0A); S0B = MFMA8(KRX, QRB0, S0B);
        SBAR();
        KMb = LA(u32x4, kp + KB_BYTES); V0a = LA(u32x4, vp); V1a = LA(u32x4, vp + 608);
        __builtin_amdgcn_s_waitcnt(0xC07F);
        SBAR();
#define LD(dst, T, p) dst = LA(T, p)
#define NOP_ (void)0
#define SLOTB(MF, POST, d0, d1, d2, d3, S, g, Pq, i, a0, a1, a2, a3) do { MF; SBAR(); POST; SBAR(); \
        d0 = EX(S[4 * (g)]); d1 = EX(S[4 * (g) + 1]); PIN(d0); PIN(d1); SBAR(); \
        Pq[i] = cvtpk(a0, a1); Pq[i + 1] = cvtpk(a2, a3); PIN(Pq); SBAR(); \
        d2 = EX(S[4 * (g) + 2]); d3 = EX(S[4 * (g) + 3]); PIN(d2); PIN(d3); SBAR(); } while (0)
#define E_ e0, e1, e2, e3
#define F_ f0, f1, f2, f3
#define SLOT_(...) SLOTB(__VA_ARGS__)
#define WAITL __builtin_amdgcn_s_waitcnt(0xC07F)
#define PERIOD(SA, SB, NA, NB, KMu, KMl, KRu, QRAx, QRBx, V0u, V1u, V0l, V1l, va, ka, PA3) do { \
        SLOT_(NA = MFMA(KMu, QA, MA),     LD(KMl, u32x4, ka),              E_, SA, 0, P1, 2, F_); \
        SLOT_(OB = MFMA(V1l, P1, OB),     LD(V1l, u32x4, (va) + 608),      F_, SA, 1, P0, 0, E_); \
        SLOT_(NA = MFMA8(KRu, QRAx, NA),  LD(V0l, u32x4, va),              E_, SA, 2, P0, 2, F_); \
        SLOT_(OA = MFMA(V0u, P0, OA),     PA3,                             F_, SA, 3, P1, 0, E_); \
        SLOT_(NB = MFMA(KMu, QB, MB),     NOP_,                            E_, SB, 0, P1, 2, F_); \
        SLOT_(OA = MFMA(V1u, P1, OA),     NOP_,                            F_, SB, 1, P0, 0, E_); \
        SLOT_(NB = MFMA8(KRu, QRBx, NB),  WAITL,                           E_, SB, 2, P0, 2, F_); \
        SLOT_(OB = MFMA(V0u, P0, OB),     NOP_,                            F_, SB, 3, P1, 0, E_); \
    } while (0)
#pragma unroll 1
        for (int seg = 0; seg < 4; ++seg) {
            if (seg == 1) lds_wait<1>(C_LAND1, 4, ws);
            if (seg == 2) { lds_inc(C_BPROG, lane); STAMP(8); }
            if (seg == 3) lds_wait<1>(C_LAND2, 4, ws);
            const int ns = seg == 0 ? 3 : (seg == 1 ? 6 : (seg == 2 ? 2 : 5));
            unsigned dl = seg == 3 ? 0u - 15 * STEP_BYTES : STEP_BYTES;
#pragma unroll 1
            for (int s = 0; s < ns; ++s) {
                unsigned kn = kp + dl, vn = vp + dl; PIN(kn); PIN(vn); SBAR();
                PERIOD(S0A, S0B, S1A, S1B, KMb, KMa, KRX, QRA1, QRB1, V0a, V1a, V0b, V1b, vp + KB_BYTES, kp + 2 * KB_BYTES, LD(KRY, u32x2, rp + 256));
                PERIOD(S1A, S1B, S0A, S0B, KMa, KMb, KRY, QRA0, QRB0, V0b, V1b, V0a, V1a, vp + 2 * KB_BYTES, kp + 3 * KB_BYTES, NOP_);
                rp += dl; PIN(rp); SBAR();
                PERIOD(S0A, S0B, S1A, S1B, KMb, KMa, KRY, QRA1, QRB1, V0a, V1a, V0b, V1b, vp + 3 * KB_BYTES, kn, LD(KRX, u32x2, rp));
                PERIOD(S1A, S1B, S0A, S0B, KMa, KMb, KRX, QRA0, QRB0, V0b, V1b, V0a, V1a, vn, kn + KB_BYTES, NOP_);
                kp = kn; vp = vn; dl = STEP_BYTES;
            }
        }
        P1[2] = cvtpk(f0, f1); P1[3] = cvtpk(f2, f3); PIN(P1); SBAR();
        OB = MFMA(V1b, P1, OB);
        STAMP(10);
        auto rowsum = [&](const f32x16& O) { const auto sw = __builtin_amdgcn_permlane32_swap(__float_as_uint(O[10]), __float_as_uint(O[10]), false, false); return __uint_as_float(sw[0]); };
        float lA = rowsum(OA), lB = rowsum(OB);
        if (__any(!(lA >= 0x1p-80f) || !(lB >= 0x1p-80f))) {
            const unsigned vo = WS_KVIMG + 1024 + hi * 304 + (n < 18 ? n : 18) * 16;
#pragma unroll 1
            for (int which = 0; which < 2; ++which) {
                const u32x4 Q = which ? QB : QA; const u32x2 R0 = which ? QRB0 : QRA0, R1 = which ? QRB1 : QRA1;
                float mx = -__builtin_inff();
#pragma unroll 1
                for (int kb = 0; kb < 64; ++kb) {
                    const unsigned bo = (kb >> 2) * STEP_BYTES;
                    const u32x4 km = __builtin_bit_cast(u32x4, __builtin_amdgcn_raw_buffer_load_b128(rws, WS_KVIMG + bo + (kb & 3) * KB_BYTES + lane * 16, 0, 16));
                    const u32x2 kr = __builtin_bit_cast(u32x2, __builtin_amdgcn_raw_buffer_load_b64(rws, WS_KVIMG + bo + KR_OFF + ((kb >> 1) & 1) * 256 + n * 8, 0, 16));
                    f32x16 S = {}; S = MFMA(km, Q, S); S = MFMA8(kr, (kb & 1) ? R1 : R0, S);
#pragma unroll
                    for (int i = 0; i < 16; ++i) mx = __builtin_fmaxf(mx, S[i]);
                }
                { const auto sw = __builtin_amdgcn_permlane32_swap(__float_as_uint(mx), __float_as_uint(mx), false, false); mx = __builtin_fmaxf(__uint_as_float(sw[0]), __uint_as_float(sw[1])); }
                f32x16 O = {}, C;
#pragma unroll
                for (int i = 0; i < 16; ++i) C[i] = -mx;
#pragma unroll 1
                for (int kb = 0; kb < 64; ++kb) {
                    const unsigned bo = (kb >> 2) * STEP_BYTES, bb = bo + (kb & 3) * KB_BYTES;
                    const u32x4 km = __builtin_bit_cast(u32x4, __builtin_amdgcn_raw_buffer_load_b128(rws, WS_KVIMG + bb + lane * 16, 0, 16));
                    const u32x2 kr = __builtin_bit_cast(u32x2, __builtin_amdgcn_raw_buffer_load_b64(rws, WS_KVIMG + bo + KR_OFF + ((kb >> 1) & 1) * 256 + n * 8, 0, 16));
                    const u32x4 v0 = __builtin_bit_cast(u32x4, __builtin_amdgcn_raw_buffer_load_b128(rws, vo + bb, 0, 16));
                    const u32x4 v1 = __builtin_bit_cast(u32x4, __builtin_amdgcn_raw_buffer_load_b128(rws, vo + bb + 608, 0, 16));
                    f32x16 S = MFMA(km, Q, C); S = MFMA8(kr, (kb & 1) ? R1 : R0, S);
                    float p[16];
#pragma unroll
                    for (int i = 0; i < 16; ++i) p[i] = EX(S[i]);
                    const u32x4 p0 = {cvtpk(p[0], p[1]), cvtpk(p[2], p[3]), cvtpk(p[4], p[5]), cvtpk(p[6], p[7])};
                    const u32x4 p1 = {cvtpk(p[8], p[9]), cvtpk(p[10], p[11]), cvtpk(p[12], p[13]), cvtpk(p[14], p[15])};
                    O = MFMA(v0, p0, O); O = MFMA(v1, p1, O);
                }
                const float lo = rowsum(O);
                if (which) { OB = O; lB = lo; } else { OA = O; lA = lo; }
            }
        }
        {
            const float ga = (1.f - gate) * __builtin_amdgcn_rcpf(lA), gb = (1.f - gate) * __builtin_amdgcn_rcpf(lB);
            auto word = [&](float lo, float hi_) { const unsigned w = cvtpk(lo, hi_); return w < POISON ? w : POISON - 1; };
            const u32x4 fa = {word(OA[0] * ga, OA[1] * ga), word(OA[2] * ga, OA[3] * ga), word(OA[4] * ga, OA[5] * ga), word(OA[6] * ga, OA[7] * ga)};
            const u32x4 fb = {word(OB[0] * gb, OB[1] * gb), word(OB[2] * gb, OB[3] * gb), word(OB[4] * gb, OB[5] * gb), word(OB[6] * gb, OB[7] * gb)};
            const u32x2 rr = {word(OA[8] * ga, OA[9] * ga), word(OB[8] * gb, OB[9] * gb)};
            __builtin_amdgcn_raw_buffer_store_b128(fa, rws, obm, 0, 16);
            __builtin_amdgcn_raw_buffer_store_b128(fb, rws, obm + 1024, 0, 16);
            if (!hi) __builtin_amdgcn_raw_buffer_store_b64(rr, rws, obr, 0, 16);
            lds_inc(C_BDONE, lane);
        }
        STAMP(11);
    } else {
        __builtin_amdgcn_s_setprio(1);
        {
            unsigned ph[18];
            features(acc0, acc2[0], acc2[1], acc2[2], acc2[3], lane, ph);
            const unsigned pa = L_PHIK + (32 * tb + n) * 160 + hi * 80;
#pragma unroll
            for (int u = 0; u < 4; ++u) LW(u32x4, pa + 16 * u) = (u32x4){ph[4 * u], ph[4 * u + 1], ph[4 * u + 2], ph[4 * u + 3]};
            LW(u32x2, pa + 64) = (u32x2){ph[16], ph[17]};
            LW(unsigned, vrow + 24) = cvtpk(acc2[8], acc2[9]); LW(unsigned, vrow + 28) = cvtpk(acc2[10], acc2[11]);
            LW(unsigned, vrow + 32) = cvtpk(acc2[12], acc2[13]); LW(unsigned, vrow + 36) = cvtpk(acc2[14], acc2[15]);
            LW(unsigned, vrow + 36 + 4 * hi) = 0x00003f80u;
            asm volatile("s_waitcnt lgkmcnt(0)" ::: "memory");
            lds_inc(C_PHI, lane);
        }
#pragma unroll 1
        for (int i = 0; i < 19; ++i) { const int p = 74 + tb + 4 * i < 147 ? 74 + tb + 4 * i : 147; dma_s_sc1(p * 1024, ws + WS_KVIMG + p * 1024, lane * 16); }
        const float gate = __uint_as_float(__builtin_amdgcn_raw_buffer_load_b32(rws, WS_G + (128 * ti + 32 * tb + n) * 4, 0, 16));
        STAMP(7);
        lds_wait<2>(C_PHI, 8, ws);
        __builtin_amdgcn_s_setprio(2);
        unsigned gen = 0;
        {
            const int hh = tb & 1, i = lane & 15;
            const int tok = 8 * g4 + (i >> 2), gr = i & 3;
            const unsigned va = L_VB + tok * 112 + hh * 56 + 8 + gr * 8;
            const int ntask = tb < 2 ? 2 : 1;
#pragma unroll 1
            for (int t = 0; t < ntask; ++t) {
                const int mbf = tb < 2 ? 2 * t : 1;
                const unsigned pa = L_PHIK + tok * 160 + hh * 80 + mbf * 32 + gr * 8;
                f32x4 d0 = {}, d1 = {};
#pragma unroll
                for (int ks = 0; ks < 4; ++ks) {
                    const u32x4 a = trd2(pa + ks * 32 * 160, pa + ks * 32 * 160 + 4 * 160);
                    const u32x4 b0 = trd2(va + ks * 32 * 112, va + ks * 32 * 112 + 4 * 112);
                    const u32x4 b1 = trd2(va + ks * 32 * 112 + 32, va + ks * 32 * 112 + 4 * 112 + 32);
                    d0 = MFMA16(a, b0, d0); d1 = MFMA16(a, b1, d1);
                }
                const unsigned ko = WS_KVP + (unsigned)(16 * tj + ti) * 5472 + hh * 2736 + (16 * mbf + 4 * g4) * 4;
                if (mbf < 2 || g4 == 0) {
                    __builtin_amdgcn_raw_buffer_store_b128(__builtin_bit_cast(u32x4, d0), rws, ko + n16 * 144, 0, 16);
                    if (n16 < 3) __builtin_amdgcn_raw_buffer_store_b128(__builtin_bit_cast(u32x4, d1), rws, ko + (16 + n16) * 144, 0, 16);
                }
            }
        }
        WAITV(0);
        lds_inc(C_LAND1, lane);
        async_bar(gen, lane, ws);
        if (tb == 0 && lane == 0) put_flag(ws, WS_FKVP + (16 * tj + ti) * 8, tag);
        STAMP(8);
        for (int p = tb; p < 37; p += 4) dma_s_sc1(p * 1024, ws + WS_KVIMG + p * 1024, lane * 16);
        unsigned pq[18];
        features(acc1, acc2[4], acc2[5], acc2[6], acc2[7], lane, pq);
        WAITV(0);
        lds_inc(C_LAND2, lane);
        poll_flags(ws, WS_FKVP + 16 * tj * 8, 16, tag, lane, 1);
        STAMP(9);
        {
            const int at = tb * 64 + lane;
#pragma unroll 1
            for (int rd = 0; rd < 2; ++rd) {
                const int it = rd == 0 ? at : 256 + tb * 22 + lane;
                const bool ok = rd == 0 || (lane < 22 && it < 342);
                const unsigned ko = WS_KVP + (unsigned)(16 * tj) * 5472 + (ok ? it : 0) * 16;
                f32x4 v[16];
#pragma unroll
                for (int i = 0; i < 16; ++i) v[i] = __builtin_bit_cast(f32x4, __builtin_amdgcn_raw_buffer_load_b128(rws, ko + i * 5472, 0, 16));
                f32x4 s = v[0];
#pragma unroll
                for (int i = 1; i < 16; ++i) s += v[i];
                const int hh = it >= 171, rem = it - 171 * hh, d = rem / 9, fq = rem - 9 * d;
                if (ok) LW(u32x2, L_KVT + hh * 1600 + d * 80 + fq * 8) = (u32x2){cvtpk(s[0], s[1]), cvtpk(s[2], s[3])};
            }
            if (tb == 3 && lane < 10) LW(u32x4, L_KVT + (lane >= 5) * 1600 + 19 * 80 + (lane % 5) * 16) = (u32x4){0u, 0u, 0u, 0u};
        }
        async_bar(gen, lane, ws);
        {
            const int hm = (n >> 2) & 1, d0 = 4 * (n >> 3) + (n & 3);
            const unsigned a0 = L_KVT + (hm == hi ? hm * 1600 + d0 * 80 : 19 * 80);
            const unsigned a1 = L_KVT + (hm == hi && d0 < 3 ? hm * 1600 + (16 + d0) * 80 : 19 * 80);
            f32x16 N0 = {}, N1 = {};
#pragma unroll
            for (int s = 0; s < 4; ++s) {
                const u32x4 bq = {pq[4 * s], pq[4 * s + 1], pq[4 * s + 2], pq[4 * s + 3]};
                N0 = MFMA(LA(u32x4, a0 + 16 * s), bq, N0); N1 = MFMA(LA(u32x4, a1 + 16 * s), bq, N1);
            }
            const u32x2 br = {pq[16], pq[17]};
            N0 = MFMA8(LA(u32x2, a0 + 64), br, N0); N1 = MFMA8(LA(u32x2, a1 + 64), br, N1);
            const float sc = gate / (N1[2] + 1e-6f);
            const unsigned fo = WS_OAF + (unsigned)(4 * ti + tb) * 36 * 1024;
            const u32x4 f0 = {cvtpk(N0[0] * sc, N0[1] * sc), cvtpk(N0[2] * sc, N0[3] * sc), cvtpk(N0[4] * sc, N0[5] * sc), cvtpk(N0[6] * sc, N0[7] * sc)};
            const u32x4 f1 = {cvtpk(N0[8] * sc, N0[9] * sc), cvtpk(N0[10] * sc, N0[11] * sc), cvtpk(N0[12] * sc, N0[13] * sc), cvtpk(N0[14] * sc, N0[15] * sc)};
            __builtin_amdgcn_raw_buffer_store_b128(f0, rws, fo + (2 * tj) * 1024 + lane * 16, 0, 16);
            __builtin_amdgcn_raw_buffer_store_b128(f1, rws, fo + (2 * tj + 1) * 1024 + lane * 16, 0, 16);
            __builtin_amdgcn_raw_buffer_store_b32(cvtpk(N1[0] * sc, N1[1] * sc), rws, fo + (32 + (tj >> 2)) * 1024 + (((tj >> 1) & 1) * 32 + n) * 16 + (2 * (tj & 1) + hi) * 4, 0, 16);
        }
        WAITV(0);
        async_bar(gen, lane, ws);
        if (tb == 0 && lane == 0) put_flag(ws, WS_FOA + (16 * ti + tj) * 8, tag);
        STAMP(10);
        const int tt = 4 * x8 + (r & 3), cg = r >> 2;
        unsigned wo[9];
#pragma unroll
        for (int i = 0; i < 9; ++i) { const unsigned q = (tb + 4 * i) * 64 + lane, row = q / 9; wo[i] = row * 1152 + (q - row * 9) * 16; }
        auto stage = [&](const char* w) {
            const char* wsl = w + cg * 144;
#pragma unroll
            for (int i = 0; i < 21; ++i) {
                const int p = tb + 4 * i;
                if (i < 20 || tb == 0) dma_s(L_WOS + p * 1024, wsl + (i / 9) * 256 * 1152, wo[i % 9]);
            }
        };
        const int i16 = lane & 15, gr = i16 & 3, hk = g4 & 1, fh = g4 >> 1;
        const unsigned fro = (unsigned)(2 * tt + (tb >> 1)) * 36 * 1024 + fh * 1024 + (hk * 32 + n16 + 16 * (tb & 1)) * 16;
        const unsigned remo = L_WOS + (18 * (8 * fh + 4 * hk + (i16 >> 3)) + 16 + ((i16 >> 2) & 1)) * 144 + gr * 8;
        f32x4 T[5] = {};
        auto wfrag = [&](int ks, int cb, unsigned maino, unsigned r4s) {
            const unsigned a = (ks < 16 ? maino + ks * 36 * 144 : remo + (ks - 16) * 16 * 18 * 144) + cb * 32;
            return trd2(a, a + (ks < 16 ? r4s : 2 * 18 * 144));
        };
        auto gemm = [&](const u32x4 (&bf)[18], unsigned maino, unsigned r4s, auto ord) {
            u32x4 wf[2][5];
#pragma unroll
            for (int cb = 0; cb < 5; ++cb) wf[0][cb] = wfrag(ord(0), cb, maino, r4s);
            SBAR();
#pragma unroll
            for (int q = 0; q < 18; ++q) {
#pragma unroll
                for (int cb = 0; cb < 5; ++cb) {
                    T[cb] = MFMA16(wf[q & 1][cb], bf[q], T[cb]); SBAR();
                    if (q < 17) wf[(q + 1) & 1][cb] = wfrag(ord(q + 1), cb, maino, r4s);
                    SBAR();
                }
            }
        };
        u32x4 bf[18];
        poll_flags(ws, WS_FOA + 16 * (tt >> 1) * 8, 16, tag, lane, 1);
#pragma unroll
        for (int ks = 0; ks < 18; ++ks) bf[ks] = __builtin_bit_cast(u32x4, __builtin_amdgcn_raw_buffer_load_b128(rws, WS_OAF + fro + ks * 2048, 0, 16));
        SBAR();
        lds_wait<16>(C_BPROG, 4, ws);
        stage(P.woA);
        WAITV(0);
        async_bar(gen, lane, ws);
        STAMP(11);
        gemm(bf, L_WOS + (18 * hk + 8 * fh + (i16 >> 2)) * 144 + gr * 8, 4 * 144, [](int q) { return q; });
        async_bar(gen, lane, ws);
        STAMP(12);
        stage(P.woB);
        WAITV(0);
        async_bar(gen, lane, ws);
        STAMP(13);
        {
            const unsigned maino = L_WOS + (18 * fh + 4 * hk + (i16 >> 2)) * 144 + gr * 8;
            const unsigned remg = L_WOS + (36 * g4 + 16 + 18 * (i16 >> 3) + ((i16 >> 2) & 1)) * 144 + gr * 8;
            const unsigned rro = WS_OBF + (unsigned)(2 * tt + (tb >> 1)) * 36 * 1024 + 32 * 1024 + ((g4 >> 1) * 32 + 16 * (tb & 1) + n16) * 16 + (g4 & 1) * 8;
            f32x4 G[4][5];
#pragma unroll
            for (int cb = 0; cb < 5; ++cb) { G[0][cb] = T[cb]; G[1][cb] = (f32x4){}; G[2][cb] = (f32x4){}; G[3][cb] = (f32x4){}; }
            u32x2 br[4];
            unsigned have = 0, done = 0;
            bool bdone = false;
#pragma unroll 1
            for (unsigned s = 0;; ++s) {
#pragma unroll
                for (int g = 0; g < 4; ++g) {
                    if (!((have >> g) & 1)) {
#pragma unroll
                        for (int u = 0; u < 4; ++u) bf[4 * g + u] = __builtin_bit_cast(u32x4, __builtin_amdgcn_raw_buffer_load_b128(rws, WS_OBF + fro + (4 * g + u) * 2048, 0, 16));
                        br[g] = __builtin_bit_cast(u32x2, __builtin_amdgcn_raw_buffer_load_b64(rws, rro + g * 1024, 0, 16));
                    }
                }
#pragma unroll
                for (int g = 0; g < 4; ++g) {
                    if (!((have >> g) & 1)) {
                        unsigned m = br[g][0] > br[g][1] ? br[g][0] : br[g][1];
#pragma unroll
                        for (int u = 0; u < 4; ++u) {
                            const u32x4 w = bf[4 * g + u];
                            const unsigned m0 = w[0] > w[1] ? w[0] : w[1], m1 = w[2] > w[3] ? w[2] : w[3];
                            m = m > m0 ? m : m0; m = m > m1 ? m : m1;
                        }
                        if (__all(m != POISON)) have |= 1u << g;
                    }
                }
                if (!bdone) { bdone = uni(__hip_atomic_load((AS3 unsigned*)(uintptr_t)C_BDONE, __ATOMIC_RELAXED, __HIP_MEMORY_SCOPE_WORKGROUP)) >= 4; if (bdone) STAMP(14); }
                if (bdone) {
#pragma unroll
                    for (int g = 0; g < 4; ++g) {
                        if (((have & ~done) >> g) & 1) {
                            u32x4 wf[2][5]; u32x2 wr[5];
#pragma unroll
                            for (int cb = 0; cb < 5; ++cb) wf[0][cb] = wfrag(4 * g, cb, maino, 8 * 144);
                            SBAR();
#pragma unroll
                            for (int u = 0; u < 4; ++u) {
#pragma unroll
                                for (int cb = 0; cb < 5; ++cb) {
                                    G[g][cb] = MFMA16(wf[u & 1][cb], bf[4 * g + u], G[g][cb]); SBAR();
                                    if (u < 3) wf[(u + 1) & 1][cb] = wfrag(4 * g + u + 1, cb, maino, 8 * 144);
                                    else wr[cb] = trd(remg + g * 144 * 144 + cb * 32);
                                    SBAR();
                                }
                            }
#pragma unroll
                            for (int cb = 0; cb < 5; ++cb) G[g][cb] = MFMA1616(wr[cb], br[g], G[g][cb]);
                            done |= 1u << g;
                        }
                    }
                    if (done == 15) {
                        break;
                    }
                }
                if (s > SPIN_MAX) { if (lane == 0) __hip_atomic_store((gu32*)(ws + WS_TMO), 0x300u, __ATOMIC_RELAXED, __HIP_MEMORY_SCOPE_AGENT); break; }
                if (bdone) __builtin_amdgcn_s_sleep(1); else __builtin_amdgcn_s_sleep(16);
            }
#pragma unroll
            for (int cb = 0; cb < 5; ++cb) T[cb] = (G[0][cb] + G[1][cb]) + (G[2][cb] + G[3][cb]);
        }
        STAMP(19);
        char* op = P.out + (size_t)(64 * tt + 16 * tb + n16) * 1152 + cg * 144 + g4 * 8;
#pragma unroll
        for (int cb = 0; cb < 5; ++cb) {
            const u32x2 o = {cvtpk(T[cb][0], T[cb][1]), cvtpk(T[cb][2], T[cb][3])};
            if (cb < 4 || g4 < 2) *(u32x2*)(op + cb * 32) = o;
        }
    }
}

__global__ __launch_bounds__(512, 2) void gated_mix_kernel(const void* x, const void* wqkv, const void* wphi, const void* woA, const void* wq, const void* wk,
                                                           const void* wv, const void* woB, const void* gate, void* out, void* ws) {
    extern __shared__ __attribute__((aligned(16))) char lds[];
    const int lane = threadIdx.x & 63, wid = uni(threadIdx.x >> 6);
    const Ptrs P = {(const char*)x, (const char*)wqkv, (const char*)wphi, (const char*)woA, (const char*)wq, (const char*)wk, (const char*)wv, (const char*)woB, (const char*)gate, (char*)out, (char*)ws};
    if (wid < 4) role_main<true>(P, wid, lane); else role_main<false>(P, wid, lane);
}
}

extern "C" void kernel_launch(void* const* d_in, const int* in_sizes, int n_in, void* d_out, int out_size, void* d_ws, size_t ws_size, hipStream_t stream) {
    (void)in_sizes; (void)n_in; (void)out_size; (void)ws_size;
    (void)hipFuncSetAttribute((const void*)fk::gated_mix_kernel, hipFuncAttributeMaxDynamicSharedMemorySize, fk::LDS_BYTES);
    hipLaunchKernelGGL(fk::gated_mix_kernel, dim3(256), dim3(512), fk::LDS_BYTES, stream,
                       (const void*)d_in[0], (const void*)d_in[1], (const void*)d_in[2], (const void*)d_in[3], (const void*)d_in[5], (const void*)d_in[6],
                       (const void*)d_in[7], (const void*)d_in[8], (const void*)d_in[9], d_out, d_ws);
}
```

```cpp
#include <hip/hip_runtime.h>
#include <hip/hip_bf16.h>
#include <stdint.h>

namespace fk {
typedef __bf16 bf16x2 __attribute__((ext_vector_type(2)));
typedef __bf16 bf16x8 __attribute__((ext_vector_type(8)));
typedef short s16x4 __attribute__((ext_vector_type(4)));
typedef float f32x2 __attribute__((ext_vector_type(2)));
typedef float f32x4 __attribute__((ext_vector_type(4)));
typedef float f32x16 __attribute__((ext_vector_type(16)));
typedef unsigned u32x2 __attribute__((ext_vector_type(2)));
typedef unsigned u32x4 __attribute__((ext_vector_type(4)));
typedef unsigned long long u64;
typedef __attribute__((address_space(1))) u64 gu64;
typedef __attribute__((address_space(1))) unsigned gu32;

#define AS3 __attribute__((address_space(3)))
#define SBAR() __builtin_amdgcn_sched_barrier(0)
#define PIN(x) asm volatile("" : "+v"(x))
#define MFMA(a, b, c) __builtin_amdgcn_mfma_f32_32x32x16_bf16(__builtin_bit_cast(bf16x8, a), __builtin_bit_cast(bf16x8, b), c, 0, 0, 0)
#define MFMA8(a, b, c) __builtin_amdgcn_mfma_f32_32x32x8bf16_1k(__builtin_bit_cast(s16x4, a), __builtin_bit_cast(s16x4, b), c, 0, 0, 0)
#define MFMA1616(a, b, c) __builtin_amdgcn_mfma_f32_16x16x16bf16_1k(__builtin_bit_cast(s16x4, a), __builtin_bit_cast(s16x4, b), c, 0, 0, 0)
#define MFMA16(a, b, c) __builtin_amdgcn_mfma_f32_16x16x32_bf16(__builtin_bit_cast(bf16x8, a), __builtin_bit_cast(bf16x8, b), c, 0, 0, 0)
#define LA(T, a) (*(const AS3 T*)(uintptr_t)(a))
#define LW(T, a) (*(AS3 T*)(uintptr_t)(a))
#define APRIO 3
#define FENCE() asm volatile("" ::: "memory")
#define EX(x) __builtin_amdgcn_exp2f(x)

#define WS_TMO 64
constexpr unsigned WS_FKV = 4096, WS_FKVP = 8192, WS_FOA = 12288, WS_FOB = 16384;
#define WS_G 32768
#define WS_KMAX 40960
#define WS_KVIMG 65536
#define WS_KVP 262144
#define WS_OAF (2 << 20)
#define WS_OBF (5 << 20)

constexpr unsigned SLOT = 36864, WOFF = 16384;
constexpr unsigned L_WKV = 110592;
constexpr unsigned L_RED = 152064, L_RES = 159744;
constexpr unsigned L_CNT = 161024, L_WPR = 161152;
constexpr unsigned L_WPHI = 152064, L_KVT = 157184;
constexpr unsigned L_PHIK = 0, L_VB = 20480;
constexpr unsigned L_KMAX = 161152, L_WOS = 37888;
constexpr unsigned KB_BYTES = 2240, STEP_BYTES = 9472, KR_OFF = 8960;
constexpr unsigned LDS_BYTES = 163840;
constexpr unsigned C_LAND0 = L_CNT, C_LAND1 = L_CNT + 4, C_LAND2 = L_CNT + 8, C_PHI = L_CNT + 12, C_ASY = L_CNT + 16, C_BPROG = L_CNT + 20, C_BDONE = L_CNT + 24, C_RAW = L_CNT + 28, C_WKVF = L_CNT + 32;
constexpr unsigned POISON = 0xffffffffu;

#define STAMP(k) do { } while (0)

__device__ __forceinline__ unsigned cvtpk(float lo, float hi) { f32x2 v = {lo, hi}; return __builtin_bit_cast(unsigned, __builtin_convertvector(v, bf16x2)); }
__device__ __forceinline__ int uni(int v) { return __builtin_amdgcn_readfirstlane(v); }

__device__ __forceinline__ void dma_s(unsigned lds, const void* sbase, unsigned voff) {
    asm volatile("s_mov_b32 m0, %0\n\ts_nop 0\n\tglobal_load_lds_dwordx4 %1, %2" :: "s"(lds), "v"(voff), "s"(sbase) : "memory", "m0");
}
__device__ __forceinline__ void dma_s_sc1(unsigned lds, const void* sbase, unsigned voff) {
    asm volatile("s_mov_b32 m0, %0\n\ts_nop 0\n\tglobal_load_lds_dwordx4 %1, %2 sc1" :: "s"(lds), "v"(voff), "s"(sbase) : "memory", "m0");
}
__device__ __forceinline__ void dma_v(unsigned lds, const void* vaddr) {
    asm volatile("s_mov_b32 m0, %0\n\ts_nop 0\n\tglobal_load_lds_dwordx4 %1, off" :: "s"(lds), "v"(vaddr) : "memory", "m0");
}
#define WAITV(n) asm volatile("s_waitcnt vmcnt(" #n ")" ::: "memory")
#define BAR_L() asm volatile("s_waitcnt lgkmcnt(0)\n\ts_barrier" ::: "memory")
template <int N> __device__ __forceinline__ void waitv() { asm volatile("s_waitcnt vmcnt(%0)" :: "n"(N) : "memory"); }
template <int N> __device__ __forceinline__ void bar_vl() { asm volatile("s_waitcnt vmcnt(%0) lgkmcnt(0)\n\ts_barrier" :: "n"(N) : "memory"); }

__device__ __forceinline__ u32x2 trd(unsigned a) { return __builtin_bit_cast(u32x2, __builtin_amdgcn_ds_read_tr16_b64_v4i16((AS3 s16x4*)(uintptr_t)a)); }
__device__ __forceinline__ u32x4 trd2(unsigned a0, unsigned a1) { const u32x2 p = trd(a0), q = trd(a1); return (u32x4){p[0], p[1], q[0], q[1]}; }

template <int CTRL> __device__ __forceinline__ float dppf(float v) { return __builtin_bit_cast(float, __builtin_amdgcn_update_dpp(0, __builtin_bit_cast(int, v), CTRL, 0xf, 0xf, true)); }
__device__ __forceinline__ float wave_max(float v) {
    v = __builtin_fmaxf(v, dppf<0x121>(v)); v = __builtin_fmaxf(v, dppf<0x122>(v)); v = __builtin_fmaxf(v, dppf<0x124>(v)); v = __builtin_fmaxf(v, dppf<0x128>(v));
    { const auto t = __builtin_amdgcn_permlane16_swap(__float_as_uint(v), __float_as_uint(v), false, false); v = __builtin_fmaxf(__uint_as_float(t[0]), __uint_as_float(t[1])); }
    { const auto t = __builtin_amdgcn_permlane32_swap(__float_as_uint(v), __float_as_uint(v), false, false); v = __builtin_fmaxf(__uint_as_float(t[0]), __uint_as_float(t[1])); }
    return v;
}
__device__ __forceinline__ u64 mix64(u64 z) { z += 0x9E3779B97F4A7C15ull; z = (z ^ (z >> 30)) * 0xBF58476D1CE4E5B9ull; z = (z ^ (z >> 27)) * 0x94D049BB133111EBull; return z ^ (z >> 31); }

constexpr unsigned SPIN_MAX = 1u << 20;
__device__ __forceinline__ void poll_flags(const char* ws, unsigned off, int n, u64 tag, int lane, int slp) {
    const gu64* p = (const gu64*)(ws + off) + (lane < n ? lane : 0);
    for (unsigned s = 0;; ++s) {
        const u64 v = __hip_atomic_load(p, __ATOMIC_RELAXED, __HIP_MEMORY_SCOPE_AGENT);
        if (__all(v == tag)) break;
        if (s > SPIN_MAX) { if (lane == 0) __hip_atomic_store((gu32*)(ws + WS_TMO), 0x100u + off, __ATOMIC_RELAXED, __HIP_MEMORY_SCOPE_AGENT); break; }
        if (slp) __builtin_amdgcn_s_sleep(8); else __builtin_amdgcn_s_sleep(1);
    }
    FENCE();
}
__device__ __forceinline__ void poll4(const char* ws, u64 tag, int lane) {
    const gu64* p = (const gu64*)(ws + WS_FKV) + lane;
    for (unsigned s = 0;; ++s) {
        const u64 v0 = __hip_atomic_load(p, __ATOMIC_RELAXED, __HIP_MEMORY_SCOPE_AGENT), v1 = __hip_atomic_load(p + 64, __ATOMIC_RELAXED, __HIP_MEMORY_SCOPE_AGENT);
        const u64 v2 = __hip_atomic_load(p + 128, __ATOMIC_RELAXED, __HIP_MEMORY_SCOPE_AGENT), v3 = __hip_atomic_load(p + 192, __ATOMIC_RELAXED, __HIP_MEMORY_SCOPE_AGENT);
        if (__all(v0 == tag && v1 == tag && v2 == tag && v3 == tag)) break;
        if (s > SPIN_MAX) { if (lane == 0) __hip_atomic_store((gu32*)(ws + WS_TMO), 0x100u, __ATOMIC_RELAXED, __HIP_MEMORY_SCOPE_AGENT); break; }
        __builtin_amdgcn_s_sleep(1);
    }
    FENCE();
}
__device__ __forceinline__ void put_flag(char* ws, unsigned off, u64 tag) { __hip_atomic_store((gu64*)(ws + off), tag, __ATOMIC_RELAXED, __HIP_MEMORY_SCOPE_AGENT); }
__device__ __forceinline__ void lds_inc(unsigned a, int lane) { if (lane == 0) __hip_atomic_fetch_add((AS3 unsigned*)(uintptr_t)a, 1u, __ATOMIC_RELAXED, __HIP_MEMORY_SCOPE_WORKGROUP); }
template <int SLP> __device__ __forceinline__ void lds_wait(unsigned a, unsigned target, const char* ws) {
    for (unsigned s = 0;; ++s) {
        const unsigned v = uni(__hip_atomic_load((AS3 unsigned*)(uintptr_t)a, __ATOMIC_RELAXED, __HIP_MEMORY_SCOPE_WORKGROUP));
        if (v >= target) break;
        if (s > SPIN_MAX) { __hip_atomic_store((gu32*)(ws + WS_TMO), 0x10u + (a & 15), __ATOMIC_RELAXED, __HIP_MEMORY_SCOPE_AGENT); break; }
        __builtin_amdgcn_s_sleep(SLP);
    }
    FENCE();
}
__device__ __forceinline__ void async_bar(unsigned& gen, int lane, const char* ws) {
    asm volatile("s_waitcnt lgkmcnt(0)" ::: "memory");
    gen += 4; lds_inc(C_ASY, lane); lds_wait<2>(C_ASY, gen, ws);
}

__device__ __forceinline__ void features(const f32x16& a0, float p16, float p17, float p18, float p19, int lane, unsigned (&out)[18]) {
    const u32x4 b0 = {cvtpk(a0[0], a0[1]), cvtpk(a0[2], a0[3]), cvtpk(a0[4], a0[5]), cvtpk(a0[6], a0[7])};
    const u32x4 b1 = {cvtpk(a0[8], a0[9]), cvtpk(a0[10], a0[11]), cvtpk(a0[12], a0[13]), cvtpk(a0[14], a0[15])};
    const u32x2 b2 = {cvtpk(p16, p17), cvtpk(p18, p19)};
    const unsigned wa = L_WPHI + lane * 16, wr = L_WPHI + 2048 + lane * 8;
    f32x16 P0 = {}, P1 = {};
    P0 = MFMA(LA(u32x4, wa), b0, P0); P1 = MFMA(LA(u32x4, wa + 2560), b0, P1);
    P0 = MFMA(LA(u32x4, wa + 1024), b1, P0); P1 = MFMA(LA(u32x4, wa + 2560 + 1024), b1, P1);
    P0 = MFMA8(LA(u32x2, wr), b2, P0); P1 = MFMA8(LA(u32x2, wr + 2560), b2, P1);
    float p[18];
#pragma unroll
    for (int i = 0; i < 16; ++i) p[i] = P0[i];
    p[16] = P1[0]; p[17] = P1[1];
    float mx = p[0], mn = p[0];
#pragma unroll
    for (int i = 1; i < 18; ++i) { mx = __builtin_fmaxf(mx, p[i]); mn = __builtin_fminf(mn, p[i]); }
    const float L = 1.4426950408889634f, mxl = -mx * L, mnl = mn * L;
    float ep[18], en[18], sp = 0.f, sn = 0.f;
#pragma unroll
    for (int i = 0; i < 18; ++i) { ep[i] = EX(__builtin_fmaf(p[i], L, mxl)); en[i] = EX(__builtin_fmaf(p[i], -L, mnl)); sp += ep[i]; sn += en[i]; }
    const float rp = __builtin_amdgcn_rcpf(sp), rn = __builtin_amdgcn_rcpf(sn);
#pragma unroll
    for (int u = 0; u < 9; ++u) { out[u] = cvtpk(ep[2 * u] * rp, ep[2 * u + 1] * rp); out[9 + u] = cvtpk(en[2 * u] * rn, en[2 * u + 1] * rn); }
}

struct Ptrs { const char *x, *wqkv, *wphi, *woA, *wq, *wk, *wv, *woB, *gate; char *out, *ws; };

template <bool ISB>
__device__ __forceinline__ void role_main(const Ptrs& P, const int wid, const int lane) {
    const int b = blockIdx.x, x8 = b & 7, r = b >> 3;
    const int ti = 8 * (x8 & 1) + (r & 7), tj = 4 * (x8 >> 1) + (r >> 3);
    const int tb = wid & 3, n = lane & 31, hi = lane >> 5, n16 = lane & 15, g4 = lane >> 4;
    const int tid = wid * 64 + lane;
    char* const ws = P.ws;
    const __amdgpu_buffer_rsrc_t rws = __builtin_amdgcn_make_buffer_rsrc((void*)ws, (short)0, 0x1000000, 0x00020000);
    STAMP(0);
    if (tid == 0) { LW(u32x4, L_CNT) = (u32x4){0u, 0u, 0u, 0u}; LW(u32x4, L_CNT + 16) = (u32x4){0u, 0u, 0u, 0u}; LW(u32x4, L_CNT + 32) = (u32x4){0u, 0u, 0u, 0u}; }
    BAR_L();
    u32x2 Ev;
    const unsigned obf = WS_OBF + (unsigned)(4 * ti + tb) * 36 * 1024, obm = obf + 2 * tj * 1024 + lane * 16;
    const unsigned obr = obf + (32 + (tj >> 2)) * 1024 + (((tj >> 1) & 1) * 32 + n) * 16 + (tj & 1) * 8;
    unsigned xo[4]; u64 wb[5]; unsigned wst[5];
    auto dma1 = [&](int c, int i) {
        const unsigned slot = (c & 3) * SLOT;
        if (i < 4) dma_s(slot + (4 * tb + i) * 1024, P.x + c * 128, xo[i]);
        else { dma_v(slot + WOFF + (5 * tb + i - 4) * 1024, (const char*)wb[i - 4]); wb[i - 4] += wst[i - 4]; }
    };
    auto issue = [&](int c) {
#pragma unroll
        for (int i = 0; i < 9; ++i) dma1(c, i);
    };
#pragma unroll
    for (int i = 0; i < 4; ++i) xo[i] = (unsigned)(128 * ti + 32 * tb + 8 * i + (lane >> 3)) * 1152u + (((lane & 7) ^ (4 * (i & 1) + (lane >> 4))) * 16);
    if (!ISB) {
        u32x4 xf[5], gf[5];
        __builtin_amdgcn_s_setprio(APRIO);
        {
            const unsigned go = (32 * tb + 8 * g4) * 2, g4o = tb < 2 ? go + 1024 : go, zo = 0;
            const unsigned xr = (unsigned)(128 * ti + 8 * tj + (n16 & 7)) * 1152u, xa = xr + go, x4 = xr + g4o;
            asm volatile("global_load_dwordx2 %0, %11, %16 sc1\n\t"
                         "global_load_dwordx4 %1, %12, %17\n\tglobal_load_dwordx4 %2, %12, %17 offset:256\n\tglobal_load_dwordx4 %3, %12, %17 offset:512\n\t"
                         "global_load_dwordx4 %4, %12, %17 offset:768\n\tglobal_load_dwordx4 %5, %13, %17\n\t"
                         "global_load_dwordx4 %6, %14, %18\n\tglobal_load_dwordx4 %7, %14, %18 offset:256\n\tglobal_load_dwordx4 %8, %14, %18 offset:512\n\t"
                         "global_load_dwordx4 %9, %14, %18 offset:768\n\tglobal_load_dwordx4 %10, %15, %18"
                         : "=&v"(Ev), "=&v"(xf[0]), "=&v"(xf[1]), "=&v"(xf[2]), "=&v"(xf[3]), "=&v"(xf[4]), "=&v"(gf[0]), "=&v"(gf[1]), "=&v"(gf[2]), "=&v"(gf[3]), "=&v"(gf[4])
                         : "v"(zo), "v"(xa), "v"(x4), "v"(go), "v"(g4o), "s"(ws), "s"(P.x), "s"(P.gate) : "memory");
        }
#pragma unroll
        for (int t = 0; t < 10; ++t) {
            const int p = tb + 4 * t;
            if (t != 5 || tb != 0) dma_s(L_WKV + p * 1024, p < 20 ? P.wk + p * 1024 : P.wv + (p * 1024 - 20736), lane * 16);
            else { if (lane < 16) dma_s(L_WKV + 20 * 1024, P.wk + 20 * 1024, lane * 16); else dma_s(L_WKV + 20 * 1024, P.wv - 256, lane * 16); }
        }
        if (tb == 1) { if (lane < 32) dma_s(L_WKV + 40 * 1024, P.wv + (40 * 1024 - 20736), lane * 16); }
        if (tb == 2) { dma_s(L_WPR, P.wphi + tj * 1296, lane * 16); if (lane < 17) dma_s(L_WPR + 1024, P.wphi + tj * 1296 + 1024, lane * 16); }
        STAMP(16);
        asm volatile("s_waitcnt vmcnt(0)" : "+v"(Ev), "+v"(xf[0]), "+v"(xf[1]), "+v"(xf[2]), "+v"(xf[3]), "+v"(xf[4]), "+v"(gf[0]), "+v"(gf[1]), "+v"(gf[2]), "+v"(gf[3]), "+v"(gf[4]) :: "memory");
        lds_inc(C_RAW, lane); lds_wait<1>(C_RAW, 4, ws);
        STAMP(1);
        const int i = lane & 15;
        const unsigned rowa = L_WKV + (32 * tb + 8 * g4 + 2 * (i >> 2)) * 36;
        const unsigned am = rowa + 8 * (i & 3), ar = rowa + (i & 1) * 20736 + 32;
        u32x2 we0[5], we1[5], wr[5], wo0[5], wo1[5];
#pragma unroll
        for (int s = 0; s < 5; ++s) {
            const unsigned o = (s < 4 || tb < 2 ? s : 0) * 128 * 36;
            we0[s] = trd(am + o); we1[s] = trd(am + o + 20736); wr[s] = trd(ar + o); wo0[s] = trd(am + o + 40); wo1[s] = trd(am + o + 40 + 20736);
        }
        SBAR();
#pragma unroll
        for (int s = 0; s < 5; ++s) { PIN(we0[s]); PIN(we1[s]); PIN(wr[s]); PIN(wo0[s]); PIN(wo1[s]); }
        lds_inc(C_WKVF, lane);
        {
            const u32x4 pz = {POISON, POISON, POISON, POISON};
            __builtin_amdgcn_raw_buffer_store_b128(pz, rws, obm, 0, 16);
            __builtin_amdgcn_raw_buffer_store_b128(pz, rws, obm + 1024, 0, 16);
            __builtin_amdgcn_raw_buffer_store_b64((u32x2){POISON, POISON}, rws, obr, 0, 16);
        }
        f32x4 E0 = {}, E1 = {}, E2 = {}, O0 = {}, O1 = {}, O2 = {}, G = {};
#pragma unroll
        for (int s = 0; s < 5; ++s) {
            if (s < 4 || tb < 2) {
                const u32x2 be = {__builtin_amdgcn_perm(xf[s][1], xf[s][0], 0x05040100u), __builtin_amdgcn_perm(xf[s][3], xf[s][2], 0x05040100u)};
                const u32x2 bo = {__builtin_amdgcn_perm(xf[s][1], xf[s][0], 0x07060302u), __builtin_amdgcn_perm(xf[s][3], xf[s][2], 0x07060302u)};
                E0 = MFMA1616(we0[s], be, E0); E1 = MFMA1616(we1[s], be, E1); E2 = MFMA1616(wr[s], be, E2);
                O0 = MFMA1616(wo0[s], bo, O0); O1 = MFMA1616(wo1[s], bo, O1); O2 = MFMA1616(wr[s], bo, O2);
                G = MFMA16(gf[s], xf[s], G);
            }
        }
        const int ra_ = ((lane - 16) & 63) * 4;
        auto rot16 = [&](float v) { return __int_as_float(__builtin_amdgcn_ds_bpermute(ra_, __float_as_int(v))); };
        const float k0 = rot16(O0[2]), k1 = rot16(O0[3]), v0 = rot16(O1[2]), v1 = rot16(O1[3]), w0 = rot16(O2[2]), w1 = rot16(O2[3]);
        const bool g0 = g4 == 0, up = n16 >= 8;
        const f32x4 K = {E0[0] + (g0 ? O2[2] : k0), E0[1] + (g0 ? O2[3] : k1), E0[2] + O0[0], E0[3] + O0[1]};
        const f32x4 V = {E1[0] + (g0 ? w0 : v0), E1[1] + (g0 ? w1 : v1), E1[2] + O1[0], E1[3] + O1[1]};
        const unsigned ra = L_RED + tb * 1280 + (n16 & 7) * 160;
        LW(f32x4, ra + (up ? 64 : 0) + g4 * 16) = up ? V : K;
        const f32x2 rm = g0 ? (f32x2){E2[0] + k0, E2[1] + k1} : (g4 == 3 ? (f32x2){E2[0] + O1[2], E2[1] + O1[3]} : (f32x2){G[0], G[0]});
        if (!up && g4 != 2) LW(f32x2, ra + (g0 ? 128 : (g4 == 3 ? 136 : 144))) = rm;
        __builtin_amdgcn_s_setprio(0);
        STAMP(2);
    } else {
        { const unsigned zo = 0; asm volatile("global_load_dwordx2 %0, %1, %2 sc1" : "=&v"(Ev) : "v"(zo), "s"(ws) : "memory"); }
        const int c0 = 72 * tj - 8 * (tj & 1);
#pragma unroll
        for (int i = 0; i < 5; ++i) {
            const unsigned q = (5 * tb + i) * 64 + lane, row = q / 20, wi = q - row * 20, ty = wi / 5, pc = wi - ty * 5;
            const bool isq = ty == 3;
            const unsigned rs = isq ? 1152u : 3456u, to = ty == 0 ? 1152u : (ty == 2 ? 2304u : 0u);
            wb[i] = (isq ? (u64)P.wq : (u64)P.wqkv) + (row * rs + to + c0 + pc * 16);
            wst[i] = rs * 64;
        }
        STAMP(17);
        issue(0);
        issue(1);
        issue(2);
        STAMP(18);
    }
    f32x16 acc0 = {}, acc1 = {}, acc2 = {};
    unsigned wa[2][3], xk[2][4];
    {
        const int i = lane & 15, sl = 2 * (g4 & 1) + ((i & 3) >> 1);
        const unsigned base = WOFF + (8 * (g4 >> 1) + (i >> 2)) * 320 + 8 * (tj & 1) + 32 * (i & 1);
        if (ISB) {
            wa[0][0] = base + 240 + 8 * sl;
            wa[0][1] = base + (sl == 0 ? 240 + 32 : 160 + 8 * (sl - 1));
            wa[0][2] = 0;
        } else {
            wa[0][0] = base + 8 * sl;
            wa[0][1] = base + 80 + 8 * sl;
            wa[0][2] = base + (sl == 0 ? 32 : (sl == 1 ? 80 + 32 : 160 + 24 + 8 * (sl - 2)));
        }
        const unsigned xa = (32 * tb + n) * 128 + ((hi ^ ((n >> 1) & 7)) * 16);
#pragma unroll
        for (int k = 0; k < 4; ++k) { xk[0][k] = xa ^ (k * 32); xk[1][k] = xk[0][k] + 2 * SLOT; PIN(xk[0][k]); PIN(xk[1][k]); }
#pragma unroll
        for (int m = 0; m < (ISB ? 2 : 3); ++m) { wa[1][m] = wa[0][m] + 2 * SLOT; PIN(wa[0][m]); PIN(wa[1][m]); }
    }
#define WPHI_AT 2
    constexpr bool NODMA = false;
#define HM(acc, a, b) acc = MFMA(a, b, acc)
    constexpr bool HRD = true;
    u32x4 FX[4], FW0[4], FW1[4], FW2[4];
    auto rdx = [&](int c, int ks) { return LA(u32x4, xk[(c >> 1) & 1][ks] + (c & 1) * SLOT); };
    auto rdw = [&](int c, int ks, int m) { const unsigned a = wa[(c >> 1) & 1][m] + (c & 1) * SLOT + ks * 5120; return trd2(a, a + 1280); };
    if (!ISB) BAR_L();
    else asm volatile("s_waitcnt vmcnt(18)\n\ts_barrier" : "+v"(Ev) :: "memory");
    const u64 tag = mix64(((u64)(unsigned)uni(Ev[1]) << 32) | (unsigned)uni(Ev[0]));
    STAMP(3);
    if (!ISB) {
        if (tb == 3) {
            const int t = lane & 7, bi = tj >> 2, pc = tj & 3;
            const int rho = 16 * (pc >> 1) + 8 * (t >> 2) + 4 * (pc & 1) + (t & 3);
            const int d = lane - 16;
            unsigned base, strd;
            if (lane < 16) { base = t * 40 + 8 * (lane >> 3); strd = 1; }
            else if (lane < 40) { base = d < 16 ? 16 + d : (d < 18 ? 18 + d : 0); strd = 40; }
            else if (lane < 48) { base = t * 40 + 32; strd = 1; }
            else { base = t * 40 + 36; strd = 0; }
            float v[8];
#pragma unroll
            for (int i = 0; i < 8; ++i) {
                const unsigned ad = L_RED + (base + i * strd) * 4;
                v[i] = (LA(float, ad) + LA(float, ad + 1280)) + (LA(float, ad + 2560) + LA(float, ad + 3840));
            }
            float ss = v[0] * v[0] + v[1] * v[1];
            float s6 = 0.f;
#pragma unroll
            for (int i = 2; i < 8; ++i) s6 += v[i] * v[i];
            if (lane < 16) ss += s6;
            u32x4 o = {cvtpk(v[0], v[1]), cvtpk(v[2], v[3]), cvtpk(v[4], v[5]), cvtpk(v[6], v[7])};
            if (lane == 34) o = (u32x4){0x3f803f80u, 0x3f803f80u, 0x3f803f80u, 0x3f803f80u};
            const unsigned img = WS_KVIMG + ti * STEP_BYTES;
            const unsigned o16 = lane < 16 ? img + bi * KB_BYTES + ((lane >> 3) * 32 + rho) * 16 : img + bi * KB_BYTES + 1024 + pc * 304 + d * 16;
            if (lane < 35) __builtin_amdgcn_raw_buffer_store_b128(o, rws, o16, 0, 16);
            float tot = __shfl(ss, t) + __shfl(ss, 8 + t) + __shfl(ss, 40 + t);
            tot = __builtin_fmaxf(tot, dppf<0xb1>(tot)); tot = __builtin_fmaxf(tot, dppf<0x4e>(tot)); tot = __builtin_fmaxf(tot, dppf<0x141>(tot));
            const float gv = __builtin_amdgcn_rcpf(1.f + EX(-1.4426950408889634f * v[0]));
            unsigned o4, d4;
            if (lane < 48) { o4 = img + KR_OFF + (bi >> 1) * 256 + rho * 8 + (bi & 1) * 4; d4 = o[0]; }
            else if (lane < 56) { o4 = WS_G + (128 * ti + 8 * tj + t) * 4; d4 = __float_as_uint(gv); }
            else { o4 = WS_KMAX + (16 * ti + tj) * 4; d4 = __float_as_uint(tot); }
            if (lane >= 40 && lane <= 56) __builtin_amdgcn_raw_buffer_store_b32(d4, rws, o4, 0, 16);
        }
    }
    auto build_wphi = [&]() {
            const int hm = (n >> 2) & 1;
            auto wval = [&](int mb, int pos) {
                const int f = 16 * mb + 4 * (n >> 3) + (n & 3), d = pos - 2 * hi;
                const bool ok = hm == hi && f < 18 && d >= 0 && d < 18;
                const unsigned e = LA(unsigned short, L_WPR + (ok ? (hi * 324 + d * 18 + f) * 2 : 0));
                return ok ? e : 0u;
            };
            if (tb < 2) {
                const int mb = tb;
                unsigned v[16];
#pragma unroll
                for (int pos = 0; pos < 16; ++pos) v[pos] = wval(mb, pos);
                LW(u32x4, L_WPHI + mb * 2560 + lane * 16) = (u32x4){v[0] | (v[1] << 16), v[2] | (v[3] << 16), v[4] | (v[5] << 16), v[6] | (v[7] << 16)};
                LW(u32x4, L_WPHI + mb * 2560 + 1024 + lane * 16) = (u32x4){v[8] | (v[9] << 16), v[10] | (v[11] << 16), v[12] | (v[13] << 16), v[14] | (v[15] << 16)};
            } else {
#pragma unroll
                for (int mb = 0; mb < 2; ++mb)
                    LW(u32x2, L_WPHI + mb * 2560 + 2048 + lane * 8) = (u32x2){wval(mb, 16) | (wval(mb, 17) << 16), wval(mb, 18) | (wval(mb, 19) << 16)};
            }
    };
    SBAR();
#pragma unroll
    for (int ks = 0; ks < 2; ++ks) { FX[ks] = rdx(0, ks); FW0[ks] = rdw(0, ks, 0); FW1[ks] = rdw(0, ks, 1); if (!ISB) FW2[ks] = rdw(0, ks, 2); }
    u32x2 fl0, fl1, fl2, fl3;
    SBAR();
#pragma unroll
    for (int c = 0; c < 9; ++c) {
#pragma unroll
        for (int ks = 0; ks < 4; ++ks) {
            if (ks == 0 && c == 0 && ISB) { lds_wait<1>(C_WKVF, 4, ws); STAMP(28); }
            if (ks == 0 && c == 6 && !ISB) {
                if (wid == 7) {
                    const unsigned fo = WS_FKV + lane * 8;
                    asm volatile("global_load_dwordx2 %0, %4, %5 sc1\n\tglobal_load_dwordx2 %1, %4, %5 offset:512 sc1\n\t"
                                 "global_load_dwordx2 %2, %4, %5 offset:1024 sc1\n\tglobal_load_dwordx2 %3, %4, %5 offset:1536 sc1"
                                 : "=&v"(fl0), "=&v"(fl1), "=&v"(fl2), "=&v"(fl3) : "v"(fo), "s"(ws) : "memory");
                }
            }
            if (ks == 2) {
                if (ISB) {
                    if (c == 0) bar_vl<18>(); else if (c == 1) bar_vl<14>(); else if (c <= 5) bar_vl<10>(); else if (c == 6) bar_vl<5>(); else if (c == 7) bar_vl<0>(); else BAR_L();
                } else if (c == 1) {
                    bar_vl<4>();
                    if (tb == 3 && lane == 0) put_flag(ws, WS_FKV + (16 * ti + tj) * 8, tag);
                } else if (c >= 3 && c <= 5) bar_vl<8>();
                else if (c == 6) { if (tb == 3) WAITV(8); else WAITV(4); BAR_L(); }
                else if (c == 7) {
                    if (tb == 3) {
                        asm volatile("s_waitcnt vmcnt(0)" : "+v"(fl0), "+v"(fl1), "+v"(fl2), "+v"(fl3) :: "memory");
                        const unsigned t0 = (unsigned)tag, t1 = (unsigned)(tag >> 32);
                        const bool seen = fl0[0] == t0 && fl0[1] == t1 && fl1[0] == t0 && fl1[1] == t1 && fl2[0] == t0 && fl2[1] == t1 && fl3[0] == t0 && fl3[1] == t1;
                        if (!__all(seen)) poll4(ws, tag, lane);
                        if (b == 0 && lane == 0) __hip_atomic_store((gu64*)ws, tag, __ATOMIC_RELAXED, __HIP_MEMORY_SCOPE_AGENT);
                    }
                    bar_vl<0>();
                } else BAR_L();
                if (!ISB && c == WPHI_AT) { if (tb < 3) build_wphi(); }
                if (c == 0) STAMP(20);
                if (c == 1) STAMP(21);
                if (c == 3) STAMP(4);
                if (c == 5) STAMP(22);
                if (c == 7) STAMP(23);
                if (c == 8) STAMP(6);
            }
            const int lc = ks < 2 ? c : c + 1, lk = (ks + 2) & 3;
            const bool ld = lc < 9;
            const int dc = ks >= 2 ? c + 4 : c + 3;
            const int p0 = dc == 3 ? 5 * ks : (ISB ? (ks == 2 ? 4 : (ks == 3 ? 5 : (ks == 0 ? 6 : 8))) : (ks + 2) & 3);
            const int p1 = NODMA || dc >= 9 ? p0 : (dc == 3 ? (!ISB ? p0 : (ks == 0 ? 5 : 9)) : (ISB ? (ks == 2 ? 5 : (ks == 3 ? 6 : (ks == 0 ? 8 : 9))) : p0 + 1));
            const int pm = p0 + (p1 - p0 + 1) / 2;
            const int ei = 4 * (c - 7) + ks - 2;
            auto early = [&](int e) {
                if (e < 9) { const unsigned p = 37 + tb + 4 * e; dma_s_sc1(p * 1024, ws + WS_KVIMG + p * 1024, lane * 16); }
                else dma_s_sc1(tb == 1 ? L_KMAX : 73 * 1024, ws + (tb == 1 ? WS_KMAX : WS_KVIMG + 73 * 1024), lane * 16);
            };
            HM(acc0, FW0[ks], FX[ks]); SBAR();
            if (ld && HRD) { FX[lk] = rdx(lc, lk); FW0[lk] = rdw(lc, lk, 0); } SBAR();
#pragma unroll
            for (int p = p0; p < pm; ++p) dma1(dc, p);
            if (ISB && ei >= 0 && ei < 4) { early(ei < 2 ? 3 * ei : 2 * ei + 2); if (ei < 2) early(3 * ei + 1); }
            SBAR();
            HM(acc1, FW1[ks], FX[ks]); SBAR();
            if (ld && HRD) FW1[lk] = rdw(lc, lk, 1); SBAR();
#pragma unroll
            for (int p = pm; p < p1; ++p) dma1(dc, p);
            if (ISB && ei >= 0 && ei < 4) early(ei < 2 ? 3 * ei + 2 : 2 * ei + 3);
            SBAR();
            if (!ISB) { HM(acc2, FW2[ks], FX[ks]); SBAR(); if (ld && HRD) FW2[lk] = rdw(lc, lk, 2); SBAR(); }
        }
    }
    STAMP(5);
    const unsigned vrow = L_VB + (32 * tb + n) * 112 + hi * 52 + 8;

    if (ISB) {
#pragma unroll
        for (int u = 0; u < 6; ++u) LW(unsigned, vrow + 4 * u) = cvtpk(acc1[4 + 2 * u], acc1[5 + 2 * u]);
        asm volatile("s_waitcnt vmcnt(0) lgkmcnt(0)" ::: "memory");
        lds_inc(C_PHI, lane); lds_inc(C_LAND0, lane);
        const float gate = __uint_as_float(__builtin_amdgcn_raw_buffer_load_b32(rws, WS_G + (128 * ti + 32 * tb + n) * 4, 0, 16));
        u32x4 QA, QB; u32x2 QRA0, QRA1, QRB0, QRB1; f32x16 MA, MB;
        {
            const float sc = 0.23570226039551584f * 1.4426950408889634f;
            float q[20];
#pragma unroll
            for (int i = 0; i < 16; ++i) q[i] = acc0[i] * sc;
#pragma unroll
            for (int i = 0; i < 4; ++i) q[16 + i] = acc1[i] * sc;
            float n2 = hi ? q[18] * q[18] + q[19] * q[19] : q[0] * q[0] + q[1] * q[1];
#pragma unroll
            for (int i = 2; i < 18; ++i) n2 = __builtin_fmaf(q[i], q[i], n2);
            unsigned cc[10], D[9];
#pragma unroll
            for (int u = 0; u < 10; ++u) cc[u] = cvtpk(q[2 * u], q[2 * u + 1]);
#pragma unroll
            for (int u = 0; u < 9; ++u) D[u] = hi ? cc[u + 1] : cc[u];
#pragma unroll
            for (int u = 0; u < 4; ++u) { const auto sw = __builtin_amdgcn_permlane32_swap(D[u], D[u + 4], false, false); QA[u] = sw[0]; QB[u] = sw[1]; }
            QRA0 = (u32x2){hi ? 0u : D[8], 0u}; QRA1 = (u32x2){0u, hi ? 0u : D[8]};
            QRB0 = (u32x2){hi ? D[8] : 0u, 0u}; QRB1 = (u32x2){0u, hi ? D[8] : 0u};
            SBAR();
            lds_wait<1>(C_LAND0, 4, ws);
            const f32x4 km4 = LA(f32x4, L_KMAX + lane * 16);
            const float km = wave_max(__builtin_fmaxf(__builtin_fmaxf(km4[0], km4[1]), __builtin_fmaxf(km4[2], km4[3])));
            const float m = -1.01f * __builtin_amdgcn_sqrtf(n2 * km);
            const auto sw = __builtin_amdgcn_permlane32_swap(__float_as_uint(m), __float_as_uint(m), false, false);
            const float ma = __uint_as_float(sw[0]), mb = __uint_as_float(sw[1]);
#pragma unroll
            for (int i = 0; i < 16; ++i) { MA[i] = ma; MB[i] = mb; }
            PIN(MA); PIN(MB); PIN(QA); PIN(QB); PIN(QRA0); PIN(QRA1); PIN(QRB0); PIN(QRB1);
        }
        STAMP(7);
        unsigned kp = 4 * STEP_BYTES + lane * 16, rp = 4 * STEP_BYTES + KR_OFF + n * 8, vp = 4 * STEP_BYTES + 1024 + hi * 304 + (n < 18 ? n : 18) * 16;
        if (n > 18) vp = 0x80000;
        PIN(kp); PIN(rp); PIN(vp);
        f32x16 OA = {}, OB = {}, S0A, S0B, S1A, S1B;
        u32x4 KMa, KMb, V0a, V0b, V1a, V1b = {}, P0 = {}, P1 = {}; u32x2 KRX, KRY;
        float e0, e1, e2, e3, f0 = 0.f, f1 = 0.f, f2 = 0.f, f3 = 0.f;
        KRX = LA(u32x2, rp); KMa = LA(u32x4, kp);
        S0A = MFMA(KMa, QA, MA); S0B = MFMA(KMa, QB, MB); S0A = MFMA8(KRX, QRA0, S0A); S0B = MFMA8(KRX, QRB0, S0B);
        SBAR();
        KMb = LA(u32x4, kp + KB_BYTES); V0a = LA(u32x4, vp); V1a = LA(u32x4, vp + 608);
        __builtin_amdgcn_s_waitcnt(0xC07F);
        SBAR();
#define LD(dst, T, p) dst = LA(T, p)
#define NOP_ (void)0
#define SLOTB(MF, POST, d0, d1, d2, d3, S, g, Pq, i, a0, a1, a2, a3) do { MF; SBAR(); POST; SBAR(); \
        d0 = EX(S[4 * (g)]); d1 = EX(S[4 * (g) + 1]); PIN(d0); PIN(d1); SBAR(); \
        Pq[i] = cvtpk(a0, a1); Pq[i + 1] = cvtpk(a2, a3); PIN(Pq); SBAR(); \
        d2 = EX(S[4 * (g) + 2]); d3 = EX(S[4 * (g) + 3]); PIN(d2); PIN(d3); SBAR(); } while (0)
#define E_ e0, e1, e2, e3
#define F_ f0, f1, f2, f3
#define SLOT_(...) SLOTB(__VA_ARGS__)
#define WAITL __builtin_amdgcn_s_waitcnt(0xC07F)
#define PERIOD(SA, SB, NA, NB, KMu, KMl, KRu, QRAx, QRBx, V0u, V1u, V0l, V1l, va, ka, PA3) do { \
        SLOT_(NA = MFMA(KMu, QA, MA),     LD(KMl, u32x4, ka),              E_, SA, 0, P1, 2, F_); \
        SLOT_(OB = MFMA(V1l, P1, OB),     LD(V1l, u32x4, (va) + 608),      F_, SA, 1, P0, 0, E_); \
        SLOT_(NA = MFMA8(KRu, QRAx, NA),  LD(V0l, u32x4, va),              E_, SA, 2, P0, 2, F_); \
        SLOT_(OA = MFMA(V0u, P0, OA),     PA3,                             F_, SA, 3, P1, 0, E_); \
        SLOT_(NB = MFMA(KMu, QB, MB),     NOP_,                            E_, SB, 0, P1, 2, F_); \
        SLOT_(OA = MFMA(V1u, P1, OA),     NOP_,                            F_, SB, 1, P0, 0, E_); \
        SLOT_(NB = MFMA8(KRu, QRBx, NB),  WAITL,                           E_, SB, 2, P0, 2, F_); \
        SLOT_(OB = MFMA(V0u, P0, OB),     NOP_,                            F_, SB, 3, P1, 0, E_); \
    } while (0)
#pragma unroll 1
        for (int seg = 0; seg < 4; ++seg) {
            if (seg == 1) lds_wait<1>(C_LAND1, 4, ws);
            if (seg == 2) { lds_inc(C_BPROG, lane); STAMP(8); }
            if (seg == 3) lds_wait<1>(C_LAND2, 4, ws);
            const int ns = seg == 0 ? 3 : (seg == 1 ? 6 : (seg == 2 ? 2 : 5));
            unsigned dl = seg == 3 ? 0u - 15 * STEP_BYTES : STEP_BYTES;
#pragma unroll 1
            for (int s = 0; s < ns; ++s) {
                unsigned kn = kp + dl, vn = vp + dl; PIN(kn); PIN(vn); SBAR();
                PERIOD(S0A, S0B, S1A, S1B, KMb, KMa, KRX, QRA1, QRB1, V0a, V1a, V0b, V1b, vp + KB_BYTES, kp + 2 * KB_BYTES, LD(KRY, u32x2, rp + 256));
                PERIOD(S1A, S1B, S0A, S0B, KMa, KMb, KRY, QRA0, QRB0, V0b, V1b, V0a, V1a, vp + 2 * KB_BYTES, kp + 3 * KB_BYTES, NOP_);
                rp += dl; PIN(rp); SBAR();
                PERIOD(S0A, S0B, S1A, S1B, KMb, KMa, KRY, QRA1, QRB1, V0a, V1a, V0b, V1b, vp + 3 * KB_BYTES, kn, LD(KRX, u32x2, rp));
                PERIOD(S1A, S1B, S0A, S0B, KMa, KMb, KRX, QRA0, QRB0, V0b, V1b, V0a, V1a, vn, kn + KB_BYTES, NOP_);
                kp = kn; vp = vn; dl = STEP_BYTES;
            }
        }
        P1[2] = cvtpk(f0, f1); P1[3] = cvtpk(f2, f3); PIN(P1); SBAR();
        OB = MFMA(V1b, P1, OB);
        STAMP(10);
        auto rowsum = [&](const f32x16& O) { const auto sw = __builtin_amdgcn_permlane32_swap(__float_as_uint(O[10]), __float_as_uint(O[10]), false, false); return __uint_as_float(sw[0]); };
        float lA = rowsum(OA), lB = rowsum(OB);
        if (__any(!(lA >= 0x1p-80f) || !(lB >= 0x1p-80f))) {
            const unsigned vo = WS_KVIMG + 1024 + hi * 304 + (n < 18 ? n : 18) * 16;
#pragma unroll 1
            for (int which = 0; which < 2; ++which) {
                const u32x4 Q = which ? QB : QA; const u32x2 R0 = which ? QRB0 : QRA0, R1 = which ? QRB1 : QRA1;
                float mx = -__builtin_inff();
#pragma unroll 1
                for (int kb = 0; kb < 64; ++kb) {
                    const unsigned bo = (kb >> 2) * STEP_BYTES;
                    const u32x4 km = __builtin_bit_cast(u32x4, __builtin_amdgcn_raw_buffer_load_b128(rws, WS_KVIMG + bo + (kb & 3) * KB_BYTES + lane * 16, 0, 16));
                    const u32x2 kr = __builtin_bit_cast(u32x2, __builtin_amdgcn_raw_buffer_load_b64(rws, WS_KVIMG + bo + KR_OFF + ((kb >> 1) & 1) * 256 + n * 8, 0, 16));
                    f32x16 S = {}; S = MFMA(km, Q, S); S = MFMA8(kr, (kb & 1) ? R1 : R0, S);
#pragma unroll
                    for (int i = 0; i < 16; ++i) mx = __builtin_fmaxf(mx, S[i]);
                }
                { const auto sw = __builtin_amdgcn_permlane32_swap(__float_as_uint(mx), __float_as_uint(mx), false, false); mx = __builtin_fmaxf(__uint_as_float(sw[0]), __uint_as_float(sw[1])); }
                f32x16 O = {}, C;
#pragma unroll
                for (int i = 0; i < 16; ++i) C[i] = -mx;
#pragma unroll 1
                for (int kb = 0; kb < 64; ++kb) {
                    const unsigned bo = (kb >> 2) * STEP_BYTES, bb = bo + (kb & 3) * KB_BYTES;
                    const u32x4 km = __builtin_bit_cast(u32x4, __builtin_amdgcn_raw_buffer_load_b128(rws, WS_KVIMG + bb + lane * 16, 0, 16));
                    const u32x2 kr = __builtin_bit_cast(u32x2, __builtin_amdgcn_raw_buffer_load_b64(rws, WS_KVIMG + bo + KR_OFF + ((kb >> 1) & 1) * 256 + n * 8, 0, 16));
                    const u32x4 v0 = __builtin_bit_cast(u32x4, __builtin_amdgcn_raw_buffer_load_b128(rws, vo + bb, 0, 16));
                    const u32x4 v1 = __builtin_bit_cast(u32x4, __builtin_amdgcn_raw_buffer_load_b128(rws, vo + bb + 608, 0, 16));
                    f32x16 S = MFMA(km, Q, C); S = MFMA8(kr, (kb & 1) ? R1 : R0, S);
                    float p[16];
#pragma unroll
                    for (int i = 0; i < 16; ++i) p[i] = EX(S[i]);
                    const u32x4 p0 = {cvtpk(p[0], p[1]), cvtpk(p[2], p[3]), cvtpk(p[4], p[5]), cvtpk(p[6], p[7])};
                    const u32x4 p1 = {cvtpk(p[8], p[9]), cvtpk(p[10], p[11]), cvtpk(p[12], p[13]), cvtpk(p[14], p[15])};
                    O = MFMA(v0, p0, O); O = MFMA(v1, p1, O);
                }
                const float lo = rowsum(O);
                if (which) { OB = O; lB = lo; } else { OA = O; lA = lo; }
            }
        }
        {
            const float ga = (1.f - gate) * __builtin_amdgcn_rcpf(lA), gb = (1.f - gate) * __builtin_amdgcn_rcpf(lB);
            auto word = [&](float lo, float hi_) { const unsigned w = cvtpk(lo, hi_); return w < POISON ? w : POISON - 1; };
            const u32x4 fa = {word(OA[0] * ga, OA[1] * ga), word(OA[2] * ga, OA[3] * ga), word(OA[4] * ga, OA[5] * ga), word(OA[6] * ga, OA[7] * ga)};
            const u32x4 fb = {word(OB[0] * gb, OB[1] * gb), word(OB[2] * gb, OB[3] * gb), word(OB[4] * gb, OB[5] * gb), word(OB[6] * gb, OB[7] * gb)};
            const u32x2 rr = {word(OA[8] * ga, OA[9] * ga), word(OB[8] * gb, OB[9] * gb)};
            __builtin_amdgcn_raw_buffer_store_b128(fa, rws, obm, 0, 16);
            __builtin_amdgcn_raw_buffer_store_b128(fb, rws, obm + 1024, 0, 16);
            if (!hi) __builtin_amdgcn_raw_buffer_store_b64(rr, rws, obr, 0, 16);
            lds_inc(C_BDONE, lane);
        }
        STAMP(11);
    } else {
        __builtin_amdgcn_s_setprio(1);
        {
            unsigned ph[18];
            features(acc0, acc2[0], acc2[1], acc2[2], acc2[3], lane, ph);
            const unsigned pa = L_PHIK + (32 * tb + n) * 160 + hi * 80;
#pragma unroll
            for (int u = 0; u < 4; ++u) LW(u32x4, pa + 16 * u) = (u32x4){ph[4 * u], ph[4 * u + 1], ph[4 * u + 2], ph[4 * u + 3]};
            LW(u32x2, pa + 64) = (u32x2){ph[16], ph[17]};
            LW(unsigned, vrow + 24) = cvtpk(acc2[8], acc2[9]); LW(unsigned, vrow + 28) = cvtpk(acc2[10], acc2[11]);
            LW(unsigned, vrow + 32) = cvtpk(acc2[12], acc2[13]); LW(unsigned, vrow + 36) = cvtpk(acc2[14], acc2[15]);
            LW(unsigned, vrow + 36 + 4 * hi) = 0x00003f80u;
            asm volatile("s_waitcnt lgkmcnt(0)" ::: "memory");
            lds_inc(C_PHI, lane);
        }
#pragma unroll 1
        for (int i = 0; i < 19; ++i) { const int p = 74 + tb + 4 * i < 147 ? 74 + tb + 4 * i : 147; dma_s_sc1(p * 1024, ws + WS_KVIMG + p * 1024, lane * 16); }
        const float gate = __uint_as_float(__builtin_amdgcn_raw_buffer_load_b32(rws, WS_G + (128 * ti + 32 * tb + n) * 4, 0, 16));
        STAMP(7);
        lds_wait<2>(C_PHI, 8, ws);
        __builtin_amdgcn_s_setprio(2);
        unsigned gen = 0;
        {
            const int hh = tb & 1, i = lane & 15;
            const int tok = 8 * g4 + (i >> 2), gr = i & 3;
            const unsigned va = L_VB + tok * 112 + hh * 56 + 8 + gr * 8;
            const int ntask = tb < 2 ? 2 : 1;
#pragma unroll 1
            for (int t = 0; t < ntask; ++t) {
                const int mbf = tb < 2 ? 2 * t : 1;
                const unsigned pa = L_PHIK + tok * 160 + hh * 80 + mbf * 32 + gr * 8;
                f32x4 d0 = {}, d1 = {};
#pragma unroll
                for (int ks = 0; ks < 4; ++ks) {
                    const u32x4 a = trd2(pa + ks * 32 * 160, pa + ks * 32 * 160 + 4 * 160);
                    const u32x4 b0 = trd2(va + ks * 32 * 112, va + ks * 32 * 112 + 4 * 112);
                    const u32x4 b1 = trd2(va + ks * 32 * 112 + 32, va + ks * 32 * 112 + 4 * 112 + 32);
                    d0 = MFMA16(a, b0, d0); d1 = MFMA16(a, b1, d1);
                }
                const unsigned ko = WS_KVP + (unsigned)(16 * tj + ti) * 5472 + hh * 2736 + (16 * mbf + 4 * g4) * 4;
                if (mbf < 2 || g4 == 0) {
                    __builtin_amdgcn_raw_buffer_store_b128(__builtin_bit_cast(u32x4, d0), rws, ko + n16 * 144, 0, 16);
                    if (n16 < 3) __builtin_amdgcn_raw_buffer_store_b128(__builtin_bit_cast(u32x4, d1), rws, ko + (16 + n16) * 144, 0, 16);
                }
            }
        }
        WAITV(0);
        lds_inc(C_LAND1, lane);
        async_bar(gen, lane, ws);
        if (tb == 0 && lane == 0) put_flag(ws, WS_FKVP + (16 * tj + ti) * 8, tag);
        STAMP(8);
        for (int p = tb; p < 37; p += 4) dma_s_sc1(p * 1024, ws + WS_KVIMG + p * 1024, lane * 16);
        unsigned pq[18];
        features(acc1, acc2[4], acc2[5], acc2[6], acc2[7], lane, pq);
        WAITV(0);
        lds_inc(C_LAND2, lane);
        poll_flags(ws, WS_FKVP + 16 * tj * 8, 16, tag, lane, 1);
        STAMP(9);
        {
            const int at = tb * 64 + lane;
#pragma unroll 1
            for (int rd = 0; rd < 2; ++rd) {
                const int it = rd == 0 ? at : 256 + tb * 22 + lane;
                const bool ok = rd == 0 || (lane < 22 && it < 342);
                const unsigned ko = WS_KVP + (unsigned)(16 * tj) * 5472 + (ok ? it : 0) * 16;
                f32x4 v[16];
#pragma unroll
                for (int i = 0; i < 16; ++i) v[i] = __builtin_bit_cast(f32x4, __builtin_amdgcn_raw_buffer_load_b128(rws, ko + i * 5472, 0, 16));
                f32x4 s = v[0];
#pragma unroll
                for (int i = 1; i < 16; ++i) s += v[i];
                const int hh = it >= 171, rem = it - 171 * hh, d = rem / 9, fq = rem - 9 * d;
                if (ok) LW(u32x2, L_KVT + hh * 1600 + d * 80 + fq * 8) = (u32x2){cvtpk(s[0], s[1]), cvtpk(s[2], s[3])};
            }
            if (tb == 3 && lane < 10) LW(u32x4, L_KVT + (lane >= 5) * 1600 + 19 * 80 + (lane % 5) * 16) = (u32x4){0u, 0u, 0u, 0u};
        }
        async_bar(gen, lane, ws);
        {
            const int hm = (n >> 2) & 1, d0 = 4 * (n >> 3) + (n & 3);
            const unsigned a0 = L_KVT + (hm == hi ? hm * 1600 + d0 * 80 : 19 * 80);
            const unsigned a1 = L_KVT + (hm == hi && d0 < 3 ? hm * 1600 + (16 + d0) * 80 : 19 * 80);
            f32x16 N0 = {}, N1 = {};
#pragma unroll
            for (int s = 0; s < 4; ++s) {
                const u32x4 bq = {pq[4 * s], pq[4 * s + 1], pq[4 * s + 2], pq[4 * s + 3]};
                N0 = MFMA(LA(u32x4, a0 + 16 * s), bq, N0); N1 = MFMA(LA(u32x4, a1 + 16 * s), bq, N1);
            }
            const u32x2 br = {pq[16], pq[17]};
            N0 = MFMA8(LA(u32x2, a0 + 64), br, N0); N1 = MFMA8(LA(u32x2, a1 + 64), br, N1);
            const float sc = gate / (N1[2] + 1e-6f);
            const unsigned fo = WS_OAF + (unsigned)(4 * ti + tb) * 36 * 1024;
            const u32x4 f0 = {cvtpk(N0[0] * sc, N0[1] * sc), cvtpk(N0[2] * sc, N0[3] * sc), cvtpk(N0[4] * sc, N0[5] * sc), cvtpk(N0[6] * sc, N0[7] * sc)};
            const u32x4 f1 = {cvtpk(N0[8] * sc, N0[9] * sc), cvtpk(N0[10] * sc, N0[11] * sc), cvtpk(N0[12] * sc, N0[13] * sc), cvtpk(N0[14] * sc, N0[15] * sc)};
            __builtin_amdgcn_raw_buffer_store_b128(f0, rws, fo + (2 * tj) * 1024 + lane * 16, 0, 16);
            __builtin_amdgcn_raw_buffer_store_b128(f1, rws, fo + (2 * tj + 1) * 1024 + lane * 16, 0, 16);
            __builtin_amdgcn_raw_buffer_store_b32(cvtpk(N1[0] * sc, N1[1] * sc), rws, fo + (32 + (tj >> 2)) * 1024 + (((tj >> 1) & 1) * 32 + n) * 16 + (2 * (tj & 1) + hi) * 4, 0, 16);
        }
        WAITV(0);
        async_bar(gen, lane, ws);
        if (tb == 0 && lane == 0) put_flag(ws, WS_FOA + (16 * ti + tj) * 8, tag);
        STAMP(10);
        const int tt = 4 * x8 + (r & 3), cg = r >> 2;
        unsigned wo[9];
#pragma unroll
        for (int i = 0; i < 9; ++i) { const unsigned q = (tb + 4 * i) * 64 + lane, row = q / 9; wo[i] = row * 1152 + (q - row * 9) * 16; }
        auto stage = [&](const char* w) {
            const char* wsl = w + cg * 144;
#pragma unroll
            for (int i = 0; i < 21; ++i) {
                const int p = tb + 4 * i;
                if (i < 20 || tb == 0) dma_s(L_WOS + p * 1024, wsl + (i / 9) * 256 * 1152, wo[i % 9]);
            }
        };
        const int i16 = lane & 15, gr = i16 & 3, hk = g4 & 1, fh = g4 >> 1;
        const unsigned fro = (unsigned)(2 * tt + (tb >> 1)) * 36 * 1024 + fh * 1024 + (hk * 32 + n16 + 16 * (tb & 1)) * 16;
        const unsigned remo = L_WOS + (18 * (8 * fh + 4 * hk + (i16 >> 3)) + 16 + ((i16 >> 2) & 1)) * 144 + gr * 8;
        f32x4 T[5] = {};
        auto wfrag = [&](int ks, int cb, unsigned maino, unsigned r4s) {
            const unsigned a = (ks < 16 ? maino + ks * 36 * 144 : remo + (ks - 16) * 16 * 18 * 144) + cb * 32;
            return trd2(a, a + (ks < 16 ? r4s : 2 * 18 * 144));
        };
        auto gemm = [&](const u32x4 (&bf)[18], unsigned maino, unsigned r4s, auto ord) {
            u32x4 wf[2][5];
#pragma unroll
            for (int cb = 0; cb < 5; ++cb) wf[0][cb] = wfrag(ord(0), cb, maino, r4s);
            SBAR();
#pragma unroll
            for (int q = 0; q < 18; ++q) {
#pragma unroll
                for (int cb = 0; cb < 5; ++cb) {
                    T[cb] = MFMA16(wf[q & 1][cb], bf[q], T[cb]); SBAR();
                    if (q < 17) wf[(q + 1) & 1][cb] = wfrag(ord(q + 1), cb, maino, r4s);
                    SBAR();
                }
            }
        };
        u32x4 bf[18];
        poll_flags(ws, WS_FOA + 16 * (tt >> 1) * 8, 16, tag, lane, 1);
#pragma unroll
        for (int ks = 0; ks < 18; ++ks) bf[ks] = __builtin_bit_cast(u32x4, __builtin_amdgcn_raw_buffer_load_b128(rws, WS_OAF + fro + ks * 2048, 0, 16));
        SBAR();
        lds_wait<16>(C_BPROG, 4, ws);
        stage(P.woA);
        WAITV(0);
        async_bar(gen, lane, ws);
        STAMP(11);
        gemm(bf, L_WOS + (18 * hk + 8 * fh + (i16 >> 2)) * 144 + gr * 8, 4 * 144, [](int q) { return q; });
        async_bar(gen, lane, ws);
        STAMP(12);
        stage(P.woB);
        WAITV(0);
        async_bar(gen, lane, ws);
        STAMP(13);
        {
            const unsigned maino = L_WOS + (18 * fh + 4 * hk + (i16 >> 2)) * 144 + gr * 8;
            const unsigned remg = L_WOS + (36 * g4 + 16 + 18 * (i16 >> 3) + ((i16 >> 2) & 1)) * 144 + gr * 8;
            const unsigned rro = WS_OBF + (unsigned)(2 * tt + (tb >> 1)) * 36 * 1024 + 32 * 1024 + ((g4 >> 1) * 32 + 16 * (tb & 1) + n16) * 16 + (g4 & 1) * 8;
            f32x4 G[4][5];
#pragma unroll
            for (int cb = 0; cb < 5; ++cb) { G[0][cb] = T[cb]; G[1][cb] = (f32x4){}; G[2][cb] = (f32x4){}; G[3][cb] = (f32x4){}; }
            u32x2 br[4];
            unsigned have = 0, done = 0;
            bool bdone = false;
#pragma unroll 1
            for (unsigned s = 0;; ++s) {
#pragma unroll
                for (int g = 0; g < 4; ++g) {
                    if (!((have >> g) & 1)) {
#pragma unroll
                        for (int u = 0; u < 4; ++u) bf[4 * g + u] = __builtin_bit_cast(u32x4, __builtin_amdgcn_raw_buffer_load_b128(rws, WS_OBF + fro + (4 * g + u) * 2048, 0, 16));
                        br[g] = __builtin_bit_cast(u32x2, __builtin_amdgcn_raw_buffer_load_b64(rws, rro + g * 1024, 0, 16));
                    }
                }
#pragma unroll
                for (int g = 0; g < 4; ++g) {
                    if (!((have >> g) & 1)) {
                        unsigned m = br[g][0] > br[g][1] ? br[g][0] : br[g][1];
#pragma unroll
                        for (int u = 0; u < 4; ++u) {
                            const u32x4 w = bf[4 * g + u];
                            const unsigned m0 = w[0] > w[1] ? w[0] : w[1], m1 = w[2] > w[3] ? w[2] : w[3];
                            m = m > m0 ? m : m0; m = m > m1 ? m : m1;
                        }
                        if (__all(m != POISON)) have |= 1u << g;
                    }
                }
                if (!bdone) { bdone = uni(__hip_atomic_load((AS3 unsigned*)(uintptr_t)C_BDONE, __ATOMIC_RELAXED, __HIP_MEMORY_SCOPE_WORKGROUP)) >= 4; if (bdone) STAMP(14); }
                if (bdone) {
#pragma unroll
                    for (int g = 0; g < 4; ++g) {
                        if (((have & ~done) >> g) & 1) {
                            u32x4 wf[2][5]; u32x2 wr[5];
#pragma unroll
                            for (int cb = 0; cb < 5; ++cb) wf[0][cb] = wfrag(4 * g, cb, maino, 8 * 144);
                            SBAR();
#pragma unroll
                            for (int u = 0; u < 4; ++u) {
#pragma unroll
                                for (int cb = 0; cb < 5; ++cb) {
                                    G[g][cb] = MFMA16(wf[u & 1][cb], bf[4 * g + u], G[g][cb]); SBAR();
                                    if (u < 3) wf[(u + 1) & 1][cb] = wfrag(4 * g + u + 1, cb, maino, 8 * 144);
                                    else wr[cb] = trd(remg + g * 144 * 144 + cb * 32);
                                    SBAR();
                                }
                            }
#pragma unroll
                            for (int cb = 0; cb < 5; ++cb) G[g][cb] = MFMA1616(wr[cb], br[g], G[g][cb]);
                            done |= 1u << g;
                        }
                    }
                    if (done == 15) {
                        break;
                    }
                }
                if (s > SPIN_MAX) { if (lane == 0) __hip_atomic_store((gu32*)(ws + WS_TMO), 0x300u, __ATOMIC_RELAXED, __HIP_MEMORY_SCOPE_AGENT); break; }
                if (bdone) __builtin_amdgcn_s_sleep(1); else __builtin_amdgcn_s_sleep(16);
            }
#pragma unroll
            for (int cb = 0; cb < 5; ++cb) T[cb] = (G[0][cb] + G[1][cb]) + (G[2][cb] + G[3][cb]);
        }
        STAMP(19);
        char* op = P.out + (size_t)(64 * tt + 16 * tb + n16) * 1152 + cg * 144 + g4 * 8;
#pragma unroll
        for (int cb = 0; cb < 5; ++cb) {
            const u32x2 o = {cvtpk(T[cb][0], T[cb][1]), cvtpk(T[cb][2], T[cb][3])};
            if (cb < 4 || g4 < 2) *(u32x2*)(op + cb * 32) = o;
        }
    }
}

__global__ __launch_bounds__(512, 2) void gated_mix_kernel(const void* x, const void* wqkv, const void* wphi, const void* woA, const void* wq, const void* wk,
                                                           const void* wv, const void* woB, const void* gate, void* out, void* ws) {
    extern __shared__ __attribute__((aligned(16))) char lds[];
    const int lane = threadIdx.x & 63, wid = uni(threadIdx.x >> 6);
    const Ptrs P = {(const char*)x, (const char*)wqkv, (const char*)wphi, (const char*)woA, (const char*)wq, (const char*)wk, (const char*)wv, (const char*)woB, (const char*)gate, (char*)out, (char*)ws};
    if (wid < 4) role_main<true>(P, wid, lane); else role_main<false>(P, wid, lane);
}
}

extern "C" void kernel_launch(void* const* d_in, const int* in_sizes, int n_in, void* d_out, int out_size, void* d_ws, size_t ws_size, hipStream_t stream) {
    (void)in_sizes; (void)n_in; (void)out_size; (void)ws_size;
    (void)hipFuncSetAttribute((const void*)fk::gated_mix_kernel, hipFuncAttributeMaxDynamicSharedMemorySize, fk::LDS_BYTES);
    hipLaunchKernelGGL(fk::gated_mix_kernel, dim3(256), dim3(512), fk::LDS_BYTES, stream,
                       (const void*)d_in[0], (const void*)d_in[1], (const void*)d_in[2], (const void*)d_in[3], (const void*)d_in[5], (const void*)d_in[6],
                       (const void*)d_in[7], (const void*)d_in[8], (const void*)d_in[9], d_out, d_ws);
}
```

```cpp
#include <hip/hip_runtime.h>
#include <hip/hip_bf16.h>
#include <stdint.h>

namespace fk {
typedef __bf16 bf16x2 __attribute__((ext_vector_type(2)));
typedef __bf16 bf16x8 __attribute__((ext_vector_type(8)));
typedef short s16x4 __attribute__((ext_vector_type(4)));
typedef float f32x2 __attribute__((ext_vector_type(2)));
typedef float f32x4 __attribute__((ext_vector_type(4)));
typedef float f32x16 __attribute__((ext_vector_type(16)));
typedef unsigned u32x2 __attribute__((ext_vector_type(2)));
typedef unsigned u32x4 __attribute__((ext_vector_type(4)));
typedef unsigned long long u64;
typedef __attribute__((address_space(1))) u64 gu64;
typedef __attribute__((address_space(1))) unsigned gu32;

#define AS3 __attribute__((address_space(3)))
#define SBAR() __builtin_amdgcn_sched_barrier(0)
#define PIN(x) asm volatile("" : "+v"(x))
#define MFMA(a, b, c) __builtin_amdgcn_mfma_f32_32x32x16_bf16(__builtin_bit_cast(bf16x8, a), __builtin_bit_cast(bf16x8, b), c, 0, 0, 0)
#define MFMA8(a, b, c) __builtin_amdgcn_mfma_f32_32x32x8bf16_1k(__builtin_bit_cast(s16x4, a), __builtin_bit_cast(s16x4, b), c, 0, 0, 0)
#define MFMA1616(a, b, c) __builtin_amdgcn_mfma_f32_16x16x16bf16_1k(__builtin_bit_cast(s16x4, a), __builtin_bit_cast(s16x4, b), c, 0, 0, 0)
#define MFMA16(a, b, c) __builtin_amdgcn_mfma_f32_16x16x32_bf16(__builtin_bit_cast(bf16x8, a), __builtin_bit_cast(bf16x8, b), c, 0, 0, 0)
#define LA(T, a) (*(const AS3 T*)(uintptr_t)(a))
#define LW(T, a) (*(AS3 T*)(uintptr_t)(a))
#define APRIO 3
#define FENCE() asm volatile("" ::: "memory")
#define EX(x) __builtin_amdgcn_exp2f(x)

#define WS_TMO 64
constexpr unsigned WS_FKV = 4096, WS_FKVP = 8192, WS_FOA = 12288, WS_FOB = 16384;
#define WS_G 32768
#define WS_KMAX 40960
#define WS_KVIMG 65536
#define WS_KVP 262144
#define WS_OAF (2 << 20)
#define WS_OBF (5 << 20)

constexpr unsigned SLOT = 36864, WOFF = 16384;
constexpr unsigned L_WKV = 110592;
constexpr unsigned L_RED = 152064, L_RES = 159744;
constexpr unsigned L_CNT = 161024, L_WPR = 161152;
constexpr unsigned L_WPHI = 152064, L_KVT = 157184;
constexpr unsigned L_PHIK = 0, L_VB = 20480;
constexpr unsigned L_KMAX = 161152, L_WOS = 37888;
constexpr unsigned KB_BYTES = 2240, STEP_BYTES = 9472, KR_OFF = 8960;
constexpr unsigned LDS_BYTES = 163840;
constexpr unsigned C_LAND0 = L_CNT, C_LAND1 = L_CNT + 4, C_LAND2 = L_CNT + 8, C_PHI = L_CNT + 12, C_ASY = L_CNT + 16, C_BPROG = L_CNT + 20, C_BDONE = L_CNT + 24, C_RAW = L_CNT + 28, C_WKVF = L_CNT + 32;
constexpr unsigned POISON = 0xffffffffu;

#define STAMP(k) do { } while (0)

__device__ __forceinline__ unsigned cvtpk(float lo, float hi) { f32x2 v = {lo, hi}; return __builtin_bit_cast(unsigned, __builtin_convertvector(v, bf16x2)); }
__device__ __forceinline__ int uni(int v) { return __builtin_amdgcn_readfirstlane(v); }

__device__ __forceinline__ void dma_s(unsigned lds, const void* sbase, unsigned voff) {
    asm volatile("s_mov_b32 m0, %0\n\ts_nop 0\n\tglobal_load_lds_dwordx4 %1, %2" :: "s"(lds), "v"(voff), "s"(sbase) : "memory", "m0");
}
__device__ __forceinline__ void dma_s_sc1(unsigned lds, const void* sbase, unsigned voff) {
    asm volatile("s_mov_b32 m0, %0\n\ts_nop 0\n\tglobal_load_lds_dwordx4 %1, %2 sc1" :: "s"(lds), "v"(voff), "s"(sbase) : "memory", "m0");
}
__device__ __forceinline__ void dma_v(unsigned lds, const void* vaddr) {
    asm volatile("s_mov_b32 m0, %0\n\ts_nop 0\n\tglobal_load_lds_dwordx4 %1, off" :: "s"(lds), "v"(vaddr) : "memory", "m0");
}
#define WAITV(n) asm volatile("s_waitcnt vmcnt(" #n ")" ::: "memory")
#define BAR_L() asm volatile("s_waitcnt lgkmcnt(0)\n\ts_barrier" ::: "memory")
template <int N> __device__ __forceinline__ void waitv() { asm volatile("s_waitcnt vmcnt(%0)" :: "n"(N) : "memory"); }
template <int N> __device__ __forceinline__ void bar_vl() { asm volatile("s_waitcnt vmcnt(%0) lgkmcnt(0)\n\ts_barrier" :: "n"(N) : "memory"); }

__device__ __forceinline__ u32x2 trd(unsigned a) { return __builtin_bit_cast(u32x2, __builtin_amdgcn_ds_read_tr16_b64_v4i16((AS3 s16x4*)(uintptr_t)a)); }
__device__ __forceinline__ u32x4 trd2(unsigned a0, unsigned a1) { const u32x2 p = trd(a0), q = trd(a1); return (u32x4){p[0], p[1], q[0], q[1]}; }

template <int CTRL> __device__ __forceinline__ float dppf(float v) { return __builtin_bit_cast(float, __builtin_amdgcn_update_dpp(0, __builtin_bit_cast(int, v), CTRL, 0xf, 0xf, true)); }
__device__ __forceinline__ float wave_max(float v) {
    v = __builtin_fmaxf(v, dppf<0x121>(v)); v = __builtin_fmaxf(v, dppf<0x122>(v)); v = __builtin_fmaxf(v, dppf<0x124>(v)); v = __builtin_fmaxf(v, dppf<0x128>(v));
    { const auto t = __builtin_amdgcn_permlane16_swap(__float_as_uint(v), __float_as_uint(v), false, false); v = __builtin_fmaxf(__uint_as_float(t[0]), __uint_as_float(t[1])); }
    { const auto t = __builtin_amdgcn_permlane32_swap(__float_as_uint(v), __float_as_uint(v), false, false); v = __builtin_fmaxf(__uint_as_float(t[0]), __uint_as_float(t[1])); }
    return v;
}
__device__ __forceinline__ u64 mix64(u64 z) { z += 0x9E3779B97F4A7C15ull; z = (z ^ (z >> 30)) * 0xBF58476D1CE4E5B9ull; z = (z ^ (z >> 27)) * 0x94D049BB133111EBull; return z ^ (z >> 31); }

constexpr unsigned SPIN_MAX = 1u << 20;
__device__ __forceinline__ void poll_flags(const char* ws, unsigned off, int n, u64 tag, int lane, int slp) {
    const gu64* p = (const gu64*)(ws + off) + (lane < n ? lane : 0);
    for (unsigned s = 0;; ++s) {
        const u64 v = __hip_atomic_load(p, __ATOMIC_RELAXED, __HIP_MEMORY_SCOPE_AGENT);
        if (__all(v == tag)) break;
        if (s > SPIN_MAX) { if (lane == 0) __hip_atomic_store((gu32*)(ws + WS_TMO), 0x100u + off, __ATOMIC_RELAXED, __HIP_MEMORY_SCOPE_AGENT); break; }
        if (slp) __builtin_amdgcn_s_sleep(8); else __builtin_amdgcn_s_sleep(1);
    }
    FENCE();
}
__device__ __forceinline__ void poll4(const char* ws, u64 tag, int lane) {
    const gu64* p = (const gu64*)(ws + WS_FKV) + lane;
    for (unsigned s = 0;; ++s) {
        const u64 v0 = __hip_atomic_load(p, __ATOMIC_RELAXED, __HIP_MEMORY_SCOPE_AGENT), v1 = __hip_atomic_load(p + 64, __ATOMIC_RELAXED, __HIP_MEMORY_SCOPE_AGENT);
        const u64 v2 = __hip_atomic_load(p + 128, __ATOMIC_RELAXED, __HIP_MEMORY_SCOPE_AGENT), v3 = __hip_atomic_load(p + 192, __ATOMIC_RELAXED, __HIP_MEMORY_SCOPE_AGENT);
        if (__all(v0 == tag && v1 == tag && v2 == tag && v3 == tag)) break;
        if (s > SPIN_MAX) { if (lane == 0) __hip_atomic_store((gu32*)(ws + WS_TMO), 0x100u, __ATOMIC_RELAXED, __HIP_MEMORY_SCOPE_AGENT); break; }
        __builtin_amdgcn_s_sleep(1);
    }
    FENCE();
}
__device__ __forceinline__ void put_flag(char* ws, unsigned off, u64 tag) { __hip_atomic_store((gu64*)(ws + off), tag, __ATOMIC_RELAXED, __HIP_MEMORY_SCOPE_AGENT); }
__device__ __forceinline__ void lds_inc(unsigned a, int lane) { if (lane == 0) __hip_atomic_fetch_add((AS3 unsigned*)(uintptr_t)a, 1u, __ATOMIC_RELAXED, __HIP_MEMORY_SCOPE_WORKGROUP); }
template <int SLP> __device__ __forceinline__ void lds_wait(unsigned a, unsigned target, const char* ws) {
    for (unsigned s = 0;; ++s) {
        const unsigned v = uni(__hip_atomic_load((AS3 unsigned*)(uintptr_t)a, __ATOMIC_RELAXED, __HIP_MEMORY_SCOPE_WORKGROUP));
        if (v >= target) break;
        if (s > SPIN_MAX) { __hip_atomic_store((gu32*)(ws + WS_TMO), 0x10u + (a & 15), __ATOMIC_RELAXED, __HIP_MEMORY_SCOPE_AGENT); break; }
        __builtin_amdgcn_s_sleep(SLP);
    }
    FENCE();
}
__device__ __forceinline__ void async_bar(unsigned& gen, int lane, const char* ws) {
    asm volatile("s_waitcnt lgkmcnt(0)" ::: "memory");
    gen += 4; lds_inc(C_ASY, lane); lds_wait<2>(C_ASY, gen, ws);
}

__device__ __forceinline__ void features(const f32x16& a0, float p16, float p17, float p18, float p19, int lane, unsigned (&out)[18]) {
    const u32x4 b0 = {cvtpk(a0[0], a0[1]), cvtpk(a0[2], a0[3]), cvtpk(a0[4], a0[5]), cvtpk(a0[6], a0[7])};
    const u32x4 b1 = {cvtpk(a0[8], a0[9]), cvtpk(a0[10], a0[11]), cvtpk(a0[12], a0[13]), cvtpk(a0[14], a0[15])};
    const u32x2 b2 = {cvtpk(p16, p17), cvtpk(p18, p19)};
    const unsigned wa = L_WPHI + lane * 16, wr = L_WPHI + 2048 + lane * 8;
    f32x16 P0 = {}, P1 = {};
    P0 = MFMA(LA(u32x4, wa), b0, P0); P1 = MFMA(LA(u32x4, wa + 2560), b0, P1);
    P0 = MFMA(LA(u32x4, wa + 1024), b1, P0); P1 = MFMA(LA(u32x4, wa + 2560 + 1024), b1, P1);
    P0 = MFMA8(LA(u32x2, wr), b2, P0); P1 = MFMA8(LA(u32x2, wr + 2560), b2, P1);
    float p[18];
#pragma unroll
    for (int i = 0; i < 16; ++i) p[i] = P0[i];
    p[16] = P1[0]; p[17] = P1[1];
    float mx = p[0], mn = p[0];
#pragma unroll
    for (int i = 1; i < 18; ++i) { mx = __builtin_fmaxf(mx, p[i]); mn = __builtin_fminf(mn, p[i]); }
    const float L = 1.4426950408889634f, mxl = -mx * L, mnl = mn * L;
    float ep[18], en[18], sp = 0.f, sn = 0.f;
#pragma unroll
    for (int i = 0; i < 18; ++i) { ep[i] = EX(__builtin_fmaf(p[i], L, mxl)); en[i] = EX(__builtin_fmaf(p[i], -L, mnl)); sp += ep[i]; sn += en[i]; }
    const float rp = __builtin_amdgcn_rcpf(sp), rn = __builtin_amdgcn_rcpf(sn);
#pragma unroll
    for (int u = 0; u < 9; ++u) { out[u] = cvtpk(ep[2 * u] * rp, ep[2 * u + 1] * rp); out[9 + u] = cvtpk(en[2 * u] * rn, en[2 * u + 1] * rn); }
}

struct Ptrs { const char *x, *wqkv, *wphi, *woA, *wq, *wk, *wv, *woB, *gate; char *out, *ws; };

template <bool ISB>
__device__ __forceinline__ void role_main(const Ptrs& P, const int wid, const int lane) {
    const int b = blockIdx.x, x8 = b & 7, r = b >> 3;
    const int ti = 8 * (x8 & 1) + (r & 7), tj = 4 * (x8 >> 1) + (r >> 3);
    const int tb = wid & 3, n = lane & 31, hi = lane >> 5, n16 = lane & 15, g4 = lane >> 4;
    const int tid = wid * 64 + lane;
    char* const ws = P.ws;
    const __amdgpu_buffer_rsrc_t rws = __builtin_amdgcn_make_buffer_rsrc((void*)ws, (short)0, 0x1000000, 0x00020000);
    STAMP(0);
    if (tid == 0) { LW(u32x4, L_CNT) = (u32x4){0u, 0u, 0u, 0u}; LW(u32x4, L_CNT + 16) = (u32x4){0u, 0u, 0u, 0u}; LW(u32x4, L_CNT + 32) = (u32x4){0u, 0u, 0u, 0u}; }
    BAR_L();
    u32x2 Ev;
    unsigned pf = 0;
    const unsigned obf = WS_OBF + (unsigned)(4 * ti + tb) * 36 * 1024, obm = obf + 2 * tj * 1024 + lane * 16;
    const unsigned obr = obf + (32 + (tj >> 2)) * 1024 + (((tj >> 1) & 1) * 32 + n) * 16 + (tj & 1) * 8;
    auto touch_w = [&](int s) {
        const unsigned l = lane < 36 ? lane : 35, cc = l / 12, ty = (l - 12 * cc) / 3, ln = l - 12 * cc - 3 * ty;
        const unsigned row = 64 * (3 * s + cc) + 32 * (x8 & 1) + r;
        u64 p0 = (u64)P.wqkv, p1 = (u64)P.wq;
        asm volatile("" : "+s"(p0), "+s"(p1));
        const u64 ad = (ty < 3 ? p0 + (row * 3456 + ty * 1152) : p1 + row * 1152) + (2 * (x8 >> 1) + ln) * 128;
        asm volatile("global_load_dword %0, %1, off" : "=&v"(pf) : "v"(ad) : "memory");
    };
    unsigned xo[4]; u64 wb[5]; unsigned wst[5];
    auto dma1 = [&](int c, int i) {
        const unsigned slot = (c & 3) * SLOT;
        if (i < 4) dma_s(slot + (4 * tb + i) * 1024, P.x + c * 128, xo[i]);
        else { dma_v(slot + WOFF + (5 * tb + i - 4) * 1024, (const char*)wb[i - 4]); wb[i - 4] += wst[i - 4]; }
    };
    auto issue = [&](int c) {
#pragma unroll
        for (int i = 0; i < 9; ++i) dma1(c, i);
    };
#pragma unroll
    for (int i = 0; i < 4; ++i) xo[i] = (unsigned)(128 * ti + 32 * tb + 8 * i + (lane >> 3)) * 1152u + (((lane & 7) ^ (4 * (i & 1) + (lane >> 4))) * 16);
    if (!ISB) {
        u32x4 xf[5], gf[5];
        __builtin_amdgcn_s_setprio(APRIO);
        {
            const unsigned go = (32 * tb + 8 * g4) * 2, g4o = tb < 2 ? go + 1024 : go, zo = 0;
            const unsigned xr = (unsigned)(128 * ti + 8 * tj + (n16 & 7)) * 1152u, xa = xr + go, x4 = xr + g4o;
            asm volatile("global_load_dwordx2 %0, %11, %16 sc1\n\t"
                         "global_load_dwordx4 %1, %12, %17\n\tglobal_load_dwordx4 %2, %12, %17 offset:256\n\tglobal_load_dwordx4 %3, %12, %17 offset:512\n\t"
                         "global_load_dwordx4 %4, %12, %17 offset:768\n\tglobal_load_dwordx4 %5, %13, %17\n\t"
                         "global_load_dwordx4 %6, %14, %18\n\tglobal_load_dwordx4 %7, %14, %18 offset:256\n\tglobal_load_dwordx4 %8, %14, %18 offset:512\n\t"
                         "global_load_dwordx4 %9, %14, %18 offset:768\n\tglobal_load_dwordx4 %10, %15, %18"
                         : "=&v"(Ev), "=&v"(xf[0]), "=&v"(xf[1]), "=&v"(xf[2]), "=&v"(xf[3]), "=&v"(xf[4]), "=&v"(gf[0]), "=&v"(gf[1]), "=&v"(gf[2]), "=&v"(gf[3]), "=&v"(gf[4])
                         : "v"(zo), "v"(xa), "v"(x4), "v"(go), "v"(g4o), "s"(ws), "s"(P.x), "s"(P.gate) : "memory");
        }
#pragma unroll
        for (int t = 0; t < 10; ++t) {
            const int p = tb + 4 * t;
            if (t != 5 || tb != 0) dma_s(L_WKV + p * 1024, p < 20 ? P.wk + p * 1024 : P.wv + (p * 1024 - 20736), lane * 16);
            else { if (lane < 16) dma_s(L_WKV + 20 * 1024, P.wk + 20 * 1024, lane * 16); else dma_s(L_WKV + 20 * 1024, P.wv - 256, lane * 16); }
        }
        if (tb == 1) { if (lane < 32) dma_s(L_WKV + 40 * 1024, P.wv + (40 * 1024 - 20736), lane * 16); }
        if (tb == 2) { dma_s(L_WPR, P.wphi + tj * 1296, lane * 16); if (lane < 17) dma_s(L_WPR + 1024, P.wphi + tj * 1296 + 1024, lane * 16); }
        STAMP(16);
        if (tb < 2) { touch_w(1 + tb); WAITV(1); } else WAITV(0);
        asm volatile("" : "+v"(Ev), "+v"(xf[0]), "+v"(xf[1]), "+v"(xf[2]), "+v"(xf[3]), "+v"(xf[4]), "+v"(gf[0]), "+v"(gf[1]), "+v"(gf[2]), "+v"(gf[3]), "+v"(gf[4]) :: "memory");
        lds_inc(C_RAW, lane); lds_wait<1>(C_RAW, 4, ws);
        STAMP(1);
        const int i = lane & 15;
        const unsigned rowa = L_WKV + (32 * tb + 8 * g4 + 2 * (i >> 2)) * 36;
        const unsigned am = rowa + 8 * (i & 3), ar = rowa + (i & 1) * 20736 + 32;
        u32x2 we0[5], we1[5], wr[5], wo0[5], wo1[5];
#pragma unroll
        for (int s = 0; s < 5; ++s) {
            const unsigned o = (s < 4 || tb < 2 ? s : 0) * 128 * 36;
            we0[s] = trd(am + o); we1[s] = trd(am + o + 20736); wr[s] = trd(ar + o); wo0[s] = trd(am + o + 40); wo1[s] = trd(am + o + 40 + 20736);
        }
        SBAR();
#pragma unroll
        for (int s = 0; s < 5; ++s) { PIN(we0[s]); PIN(we1[s]); PIN(wr[s]); PIN(wo0[s]); PIN(wo1[s]); }
        lds_inc(C_WKVF, lane);
        {
            const u32x4 pz = {POISON, POISON, POISON, POISON};
            __builtin_amdgcn_raw_buffer_store_b128(pz, rws, obm, 0, 16);
            __builtin_amdgcn_raw_buffer_store_b128(pz, rws, obm + 1024, 0, 16);
            __builtin_amdgcn_raw_buffer_store_b64((u32x2){POISON, POISON}, rws, obr, 0, 16);
        }
        f32x4 E0 = {}, E1 = {}, E2 = {}, O0 = {}, O1 = {}, O2 = {}, G = {};
#pragma unroll
        for (int s = 0; s < 5; ++s) {
            if (s < 4 || tb < 2) {
                const u32x2 be = {__builtin_amdgcn_perm(xf[s][1], xf[s][0], 0x05040100u), __builtin_amdgcn_perm(xf[s][3], xf[s][2], 0x05040100u)};
                const u32x2 bo = {__builtin_amdgcn_perm(xf[s][1], xf[s][0], 0x07060302u), __builtin_amdgcn_perm(xf[s][3], xf[s][2], 0x07060302u)};
                E0 = MFMA1616(we0[s], be, E0); E1 = MFMA1616(we1[s], be, E1); E2 = MFMA1616(wr[s], be, E2);
                O0 = MFMA1616(wo0[s], bo, O0); O1 = MFMA1616(wo1[s], bo, O1); O2 = MFMA1616(wr[s], bo, O2);
                G = MFMA16(gf[s], xf[s], G);
            }
        }
        const int ra_ = ((lane - 16) & 63) * 4;
        auto rot16 = [&](float v) { return __int_as_float(__builtin_amdgcn_ds_bpermute(ra_, __float_as_int(v))); };
        const float k0 = rot16(O0[2]), k1 = rot16(O0[3]), v0 = rot16(O1[2]), v1 = rot16(O1[3]), w0 = rot16(O2[2]), w1 = rot16(O2[3]);
        const bool g0 = g4 == 0, up = n16 >= 8;
        const f32x4 K = {E0[0] + (g0 ? O2[2] : k0), E0[1] + (g0 ? O2[3] : k1), E0[2] + O0[0], E0[3] + O0[1]};
        const f32x4 V = {E1[0] + (g0 ? w0 : v0), E1[1] + (g0 ? w1 : v1), E1[2] + O1[0], E1[3] + O1[1]};
        const unsigned ra = L_RED + tb * 1280 + (n16 & 7) * 160;
        LW(f32x4, ra + (up ? 64 : 0) + g4 * 16) = up ? V : K;
        const f32x2 rm = g0 ? (f32x2){E2[0] + k0, E2[1] + k1} : (g4 == 3 ? (f32x2){E2[0] + O1[2], E2[1] + O1[3]} : (f32x2){G[0], G[0]});
        if (!up && g4 != 2) LW(f32x2, ra + (g0 ? 128 : (g4 == 3 ? 136 : 144))) = rm;
        __builtin_amdgcn_s_setprio(0);
        STAMP(2);
    } else {
        { const unsigned zo = 0; asm volatile("global_load_dwordx2 %0, %1, %2 sc1" : "=&v"(Ev) : "v"(zo), "s"(ws) : "memory"); }
        if (tb == 0) touch_w(0);
        const int c0 = 72 * tj - 8 * (tj & 1);
#pragma unroll
        for (int i = 0; i < 5; ++i) {
            const unsigned q = (5 * tb + i) * 64 + lane, row = q / 20, wi = q - row * 20, ty = wi / 5, pc = wi - ty * 5;
            const bool isq = ty == 3;
            const unsigned rs = isq ? 1152u : 3456u, to = ty == 0 ? 1152u : (ty == 2 ? 2304u : 0u);
            wb[i] = (isq ? (u64)P.wq : (u64)P.wqkv) + (row * rs + to + c0 + pc * 16);
            wst[i] = rs * 64;
        }
        STAMP(17);
        issue(0);
        issue(1);
        issue(2);
        STAMP(18);
    }
    f32x16 acc0 = {}, acc1 = {}, acc2 = {};
    unsigned wa[2][3], xk[2][4];
    {
        const int i = lane & 15, sl = 2 * (g4 & 1) + ((i & 3) >> 1);
        const unsigned base = WOFF + (8 * (g4 >> 1) + (i >> 2)) * 320 + 8 * (tj & 1) + 32 * (i & 1);
        if (ISB) {
            wa[0][0] = base + 240 + 8 * sl;
            wa[0][1] = base + (sl == 0 ? 240 + 32 : 160 + 8 * (sl - 1));
            wa[0][2] = 0;
        } else {
            wa[0][0] = base + 8 * sl;
            wa[0][1] = base + 80 + 8 * sl;
            wa[0][2] = base + (sl == 0 ? 32 : (sl == 1 ? 80 + 32 : 160 + 24 + 8 * (sl - 2)));
        }
        const unsigned xa = (32 * tb + n) * 128 + ((hi ^ ((n >> 1) & 7)) * 16);
#pragma unroll
        for (int k = 0; k < 4; ++k) { xk[0][k] = xa ^ (k * 32); xk[1][k] = xk[0][k] + 2 * SLOT; PIN(xk[0][k]); PIN(xk[1][k]); }
#pragma unroll
        for (int m = 0; m < (ISB ? 2 : 3); ++m) { wa[1][m] = wa[0][m] + 2 * SLOT; PIN(wa[0][m]); PIN(wa[1][m]); }
    }
#define WPHI_AT 2
    constexpr bool NODMA = false;
#define HM(acc, a, b) acc = MFMA(a, b, acc)
    constexpr bool HRD = true;
    u32x4 FX[4], FW0[4], FW1[4], FW2[4];
    auto rdx = [&](int c, int ks) { return LA(u32x4, xk[(c >> 1) & 1][ks] + (c & 1) * SLOT); };
    auto rdw = [&](int c, int ks, int m) { const unsigned a = wa[(c >> 1) & 1][m] + (c & 1) * SLOT + ks * 5120; return trd2(a, a + 1280); };
    if (!ISB) BAR_L();
    else asm volatile("s_waitcnt vmcnt(18)\n\ts_barrier" : "+v"(Ev), "+v"(pf) :: "memory");
    const u64 tag = mix64(((u64)(unsigned)uni(Ev[1]) << 32) | (unsigned)uni(Ev[0]));
    STAMP(3);
    if (!ISB) {
        if (tb == 3) {
            const int t = lane & 7, bi = tj >> 2, pc = tj & 3;
            const int rho = 16 * (pc >> 1) + 8 * (t >> 2) + 4 * (pc & 1) + (t & 3);
            const int d = lane - 16;
            unsigned base, strd;
            if (lane < 16) { base = t * 40 + 8 * (lane >> 3); strd = 1; }
            else if (lane < 40) { base = d < 16 ? 16 + d : (d < 18 ? 18 + d : 0); strd = 40; }
            else if (lane < 48) { base = t * 40 + 32; strd = 1; }
            else { base = t * 40 + 36; strd = 0; }
            float v[8];
#pragma unroll
            for (int i = 0; i < 8; ++i) {
                const unsigned ad = L_RED + (base + i * strd) * 4;
                v[i] = (LA(float, ad) + LA(float, ad + 1280)) + (LA(float, ad + 2560) + LA(float, ad + 3840));
            }
            float ss = v[0] * v[0] + v[1] * v[1];
            float s6 = 0.f;
#pragma unroll
            for (int i = 2; i < 8; ++i) s6 += v[i] * v[i];
            if (lane < 16) ss += s6;
            u32x4 o = {cvtpk(v[0], v[1]), cvtpk(v[2], v[3]), cvtpk(v[4], v[5]), cvtpk(v[6], v[7])};
            if (lane == 34) o = (u32x4){0x3f803f80u, 0x3f803f80u, 0x3f803f80u, 0x3f803f80u};
            const unsigned img = WS_KVIMG + ti * STEP_BYTES;
            const unsigned o16 = lane < 16 ? img + bi * KB_BYTES + ((lane >> 3) * 32 + rho) * 16 : img + bi * KB_BYTES + 1024 + pc * 304 + d * 16;
            if (lane < 35) __builtin_amdgcn_raw_buffer_store_b128(o, rws, o16, 0, 16);
            float tot = __shfl(ss, t) + __shfl(ss, 8 + t) + __shfl(ss, 40 + t);
            tot = __builtin_fmaxf(tot, dppf<0xb1>(tot)); tot = __builtin_fmaxf(tot, dppf<0x4e>(tot)); tot = __builtin_fmaxf(tot, dppf<0x141>(tot));
            const float gv = __builtin_amdgcn_rcpf(1.f + EX(-1.4426950408889634f * v[0]));
            unsigned o4, d4;
            if (lane < 48) { o4 = img + KR_OFF + (bi >> 1) * 256 + rho * 8 + (bi & 1) * 4; d4 = o[0]; }
            else if (lane < 56) { o4 = WS_G + (128 * ti + 8 * tj + t) * 4; d4 = __float_as_uint(gv); }
            else { o4 = WS_KMAX + (16 * ti + tj) * 4; d4 = __float_as_uint(tot); }
            if (lane >= 40 && lane <= 56) __builtin_amdgcn_raw_buffer_store_b32(d4, rws, o4, 0, 16);
        }
    }
    auto build_wphi = [&]() {
            const int hm = (n >> 2) & 1;
            auto wval = [&](int mb, int pos) {
                const int f = 16 * mb + 4 * (n >> 3) + (n & 3), d = pos - 2 * hi;
                const bool ok = hm == hi && f < 18 && d >= 0 && d < 18;
                const unsigned e = LA(unsigned short, L_WPR + (ok ? (hi * 324 + d * 18 + f) * 2 : 0));
                return ok ? e : 0u;
            };
            if (tb < 2) {
                const int mb = tb;
                unsigned v[16];
#pragma unroll
                for (int pos = 0; pos < 16; ++pos) v[pos] = wval(mb, pos);
                LW(u32x4, L_WPHI + mb * 2560 + lane * 16) = (u32x4){v[0] | (v[1] << 16), v[2] | (v[3] << 16), v[4] | (v[5] << 16), v[6] | (v[7] << 16)};
                LW(u32x4, L_WPHI + mb * 2560 + 1024 + lane * 16) = (u32x4){v[8] | (v[9] << 16), v[10] | (v[11] << 16), v[12] | (v[13] << 16), v[14] | (v[15] << 16)};
            } else {
#pragma unroll
                for (int mb = 0; mb < 2; ++mb)
                    LW(u32x2, L_WPHI + mb * 2560 + 2048 + lane * 8) = (u32x2){wval(mb, 16) | (wval(mb, 17) << 16), wval(mb, 18) | (wval(mb, 19) << 16)};
            }
    };
    SBAR();
#pragma unroll
    for (int ks = 0; ks < 2; ++ks) { FX[ks] = rdx(0, ks); FW0[ks] = rdw(0, ks, 0); FW1[ks] = rdw(0, ks, 1); if (!ISB) FW2[ks] = rdw(0, ks, 2); }
    u32x2 fl0, fl1, fl2, fl3;
    SBAR();
#pragma unroll
    for (int c = 0; c < 9; ++c) {
#pragma unroll
        for (int ks = 0; ks < 4; ++ks) {
            if (ks == 0 && c == 0 && ISB) { lds_wait<1>(C_WKVF, 4, ws); STAMP(28); }
            if (ks == 0 && c == 6 && !ISB) {
                if (wid == 7) {
                    const unsigned fo = WS_FKV + lane * 8;
                    asm volatile("global_load_dwordx2 %0, %4, %5 sc1\n\tglobal_load_dwordx2 %1, %4, %5 offset:512 sc1\n\t"
                                 "global_load_dwordx2 %2, %4, %5 offset:1024 sc1\n\tglobal_load_dwordx2 %3, %4, %5 offset:1536 sc1"
                                 : "=&v"(fl0), "=&v"(fl1), "=&v"(fl2), "=&v"(fl3) : "v"(fo), "s"(ws) : "memory");
                }
            }
            if (ks == 2) {
                if (ISB) {
                    if (c == 0) bar_vl<18>(); else if (c == 1) bar_vl<14>(); else if (c <= 5) bar_vl<10>(); else if (c == 6) bar_vl<5>(); else if (c == 7) bar_vl<0>(); else BAR_L();
                } else if (c == 1) {
                    bar_vl<4>();
                    PIN(pf);
                    if (tb == 3 && lane == 0) put_flag(ws, WS_FKV + (16 * ti + tj) * 8, tag);
                } else if (c >= 3 && c <= 5) bar_vl<8>();
                else if (c == 6) { if (tb == 3) WAITV(8); else WAITV(4); BAR_L(); }
                else if (c == 7) {
                    if (tb == 3) {
                        asm volatile("s_waitcnt vmcnt(0)" : "+v"(fl0), "+v"(fl1), "+v"(fl2), "+v"(fl3) :: "memory");
                        const unsigned t0 = (unsigned)tag, t1 = (unsigned)(tag >> 32);
                        const bool seen = fl0[0] == t0 && fl0[1] == t1 && fl1[0] == t0 && fl1[1] == t1 && fl2[0] == t0 && fl2[1] == t1 && fl3[0] == t0 && fl3[1] == t1;
                        if (!__all(seen)) poll4(ws, tag, lane);
                        if (b == 0 && lane == 0) __hip_atomic_store((gu64*)ws, tag, __ATOMIC_RELAXED, __HIP_MEMORY_SCOPE_AGENT);
                    }
                    bar_vl<0>();
                } else BAR_L();
                if (!ISB && c == WPHI_AT) { if (tb < 3) build_wphi(); }
                if (c == 0) STAMP(20);
                if (c == 1) STAMP(21);
                if (c == 3) STAMP(4);
                if (c == 5) STAMP(22);
                if (c == 7) STAMP(23);
                if (c == 8) STAMP(6);
            }
            const int lc = ks < 2 ? c : c + 1, lk = (ks + 2) & 3;
            const bool ld = lc < 9;
            const int dc = ks >= 2 ? c + 4 : c + 3;
            const int p0 = dc == 3 ? 5 * ks : (ISB ? (ks == 2 ? 4 : (ks == 3 ? 5 : (ks == 0 ? 6 : 8))) : (ks + 2) & 3);
            const int p1 = NODMA || dc >= 9 ? p0 : (dc == 3 ? (!ISB ? p0 : (ks == 0 ? 5 : 9)) : (ISB ? (ks == 2 ? 5 : (ks == 3 ? 6 : (ks == 0 ? 8 : 9))) : p0 + 1));
            const int pm = p0 + (p1 - p0 + 1) / 2;
            const int ei = 4 * (c - 7) + ks - 2;
            auto early = [&](int e) {
                if (e < 9) { const unsigned p = 37 + tb + 4 * e; dma_s_sc1(p * 1024, ws + WS_KVIMG + p * 1024, lane * 16); }
                else dma_s_sc1(tb == 1 ? L_KMAX : 73 * 1024, ws + (tb == 1 ? WS_KMAX : WS_KVIMG + 73 * 1024), lane * 16);
            };
            HM(acc0, FW0[ks], FX[ks]); SBAR();
            if (ld && HRD) { FX[lk] = rdx(lc, lk); FW0[lk] = rdw(lc, lk, 0); } SBAR();
#pragma unroll
            for (int p = p0; p < pm; ++p) dma1(dc, p);
            if (ISB && ei >= 0 && ei < 4) { early(ei < 2 ? 3 * ei : 2 * ei + 2); if (ei < 2) early(3 * ei + 1); }
            SBAR();
            HM(acc1, FW1[ks], FX[ks]); SBAR();
            if (ld && HRD) FW1[lk] = rdw(lc, lk, 1); SBAR();
#pragma unroll
            for (int p = pm; p < p1; ++p) dma1(dc, p);
            if (ISB && ei >= 0 && ei < 4) early(ei < 2 ? 3 * ei + 2 : 2 * ei + 3);
            SBAR();
            if (!ISB) { HM(acc2, FW2[ks], FX[ks]); SBAR(); if (ld && HRD) FW2[lk] = rdw(lc, lk, 2); SBAR(); }
        }
    }
    STAMP(5);
    const unsigned vrow = L_VB + (32 * tb + n) * 112 + hi * 52 + 8;

    if (ISB) {
#pragma unroll
        for (int u = 0; u < 6; ++u) LW(unsigned, vrow + 4 * u) = cvtpk(acc1[4 + 2 * u], acc1[5 + 2 * u]);
        asm volatile("s_waitcnt vmcnt(0) lgkmcnt(0)" ::: "memory");
        lds_inc(C_PHI, lane); lds_inc(C_LAND0, lane);
        const float gate = __uint_as_float(__builtin_amdgcn_raw_buffer_load_b32(rws, WS_G + (128 * ti + 32 * tb + n) * 4, 0, 16));
        u32x4 QA, QB; u32x2 QRA0, QRA1, QRB0, QRB1; f32x16 MA, MB;
        {
            const float sc = 0.23570226039551584f * 1.4426950408889634f;
            float q[20];
#pragma unroll
            for (int i = 0; i < 16; ++i) q[i] = acc0[i] * sc;
#pragma unroll
            for (int i = 0; i < 4; ++i) q[16 + i] = acc1[i] * sc;
            float n2 = hi ? q[18] * q[18] + q[19] * q[19] : q[0] * q[0] + q[1] * q[1];
#pragma unroll
            for (int i = 2; i < 18; ++i) n2 = __builtin_fmaf(q[i], q[i], n2);
            unsigned cc[10], D[9];
#pragma unroll
            for (int u = 0; u < 10; ++u) cc[u] = cvtpk(q[2 * u], q[2 * u + 1]);
#pragma unroll
            for (int u = 0; u < 9; ++u) D[u] = hi ? cc[u + 1] : cc[u];
#pragma unroll
            for (int u = 0; u < 4; ++u) { const auto sw = __builtin_amdgcn_permlane32_swap(D[u], D[u + 4], false, false); QA[u] = sw[0]; QB[u] = sw[1]; }
            QRA0 = (u32x2){hi ? 0u : D[8], 0u}; QRA1 = (u32x2){0u, hi ? 0u : D[8]};
            QRB0 = (u32x2){hi ? D[8] : 0u, 0u}; QRB1 = (u32x2){0u, hi ? D[8] : 0u};
            SBAR();
            lds_wait<1>(C_LAND0, 4, ws);
            const f32x4 km4 = LA(f32x4, L_KMAX + lane * 16);
            const float km = wave_max(__builtin_fmaxf(__builtin_fmaxf(km4[0], km4[1]), __builtin_fmaxf(km4[2], km4[3])));
            const float m = -1.01f * __builtin_amdgcn_sqrtf(n2 * km);
            const auto sw = __builtin_amdgcn_permlane32_swap(__float_as_uint(m), __float_as_uint(m), false, false);
            const float ma = __uint_as_float(sw[0]), mb = __uint_as_float(sw[1]);
#pragma unroll
            for (int i = 0; i < 16; ++i) { MA[i] = ma; MB[i] = mb; }
            PIN(MA); PIN(MB); PIN(QA); PIN(QB); PIN(QRA0); PIN(QRA1); PIN(QRB0); PIN(QRB1);
        }
        STAMP(7);
        unsigned kp = 4 * STEP_BYTES + lane * 16, rp = 4 * STEP_BYTES + KR_OFF + n * 8, vp = 4 * STEP_BYTES + 1024 + hi * 304 + (n < 18 ? n : 18) * 16;
        if (n > 18) vp = 0x80000;
        PIN(kp); PIN(rp); PIN(vp);
        f32x16 OA = {}, OB = {}, S0A, S0B, S1A, S1B;
        u32x4 KMa, KMb, V0a, V0b, V1a, V1b = {}, P0 = {}, P1 = {}; u32x2 KRX, KRY;
        float e0, e1, e2, e3, f0 = 0.f, f1 = 0.f, f2 = 0.f, f3 = 0.f;
        KRX = LA(u32x2, rp); KMa = LA(u32x4, kp);
        S0A = MFMA(KMa, QA, MA); S0B = MFMA(KMa, QB, MB); S0A = MFMA8(KRX, QRA0, S0A); S0B = MFMA8(KRX, QRB0, S0B);
        SBAR();
        KMb = LA(u32x4, kp + KB_BYTES); V0a = LA(u32x4, vp); V1a = LA(u32x4, vp + 608);
        __builtin_amdgcn_s_waitcnt(0xC07F);
        SBAR();
#define LD(dst, T, p) dst = LA(T, p)
#define NOP_ (void)0
#define SLOTB(MF, POST, d0, d1, d2, d3, S, g, Pq, i, a0, a1, a2, a3) do { MF; SBAR(); POST; SBAR(); \
        d0 = EX(S[4 * (g)]); d1 = EX(S[4 * (g) + 1]); PIN(d0); PIN(d1); SBAR(); \
        Pq[i] = cvtpk(a0, a1); Pq[i + 1] = cvtpk(a2, a3); PIN(Pq); SBAR(); \
        d2 = EX(S[4 * (g) + 2]); d3 = EX(S[4 * (g) + 3]); PIN(d2); PIN(d3); SBAR(); } while (0)
#define E_ e0, e1, e2, e3
#define F_ f0, f1, f2, f3
#define SLOT_(...) SLOTB(__VA_ARGS__)
#define WAITL __builtin_amdgcn_s_waitcnt(0xC07F)
#define PERIOD(SA, SB, NA, NB, KMu, KMl, KRu, QRAx, QRBx, V0u, V1u, V0l, V1l, va, ka, PA3) do { \
        SLOT_(NA = MFMA(KMu, QA, MA),     LD(KMl, u32x4, ka),              E_, SA, 0, P1, 2, F_); \
        SLOT_(OB = MFMA(V1l, P1, OB),     LD(V1l, u32x4, (va) + 608),      F_, SA, 1, P0, 0, E_); \
        SLOT_(NA = MFMA8(KRu, QRAx, NA),  LD(V0l, u32x4, va),              E_, SA, 2, P0, 2, F_); \
        SLOT_(OA = MFMA(V0u, P0, OA),     PA3,                             F_, SA, 3, P1, 0, E_); \
        SLOT_(NB = MFMA(KMu, QB, MB),     NOP_,                            E_, SB, 0, P1, 2, F_); \
        SLOT_(OA = MFMA(V1u, P1, OA),     NOP_,                            F_, SB, 1, P0, 0, E_); \
        SLOT_(NB = MFMA8(KRu, QRBx, NB),  WAITL,                           E_, SB, 2, P0, 2, F_); \
        SLOT_(OB = MFMA(V0u, P0, OB),     NOP_,                            F_, SB, 3, P1, 0, E_); \
    } while (0)
#pragma unroll 1
        for (int seg = 0; seg < 4; ++seg) {
            if (seg == 1) lds_wait<1>(C_LAND1, 4, ws);
            if (seg == 2) { lds_inc(C_BPROG, lane); STAMP(8); }
            if (seg == 3) lds_wait<1>(C_LAND2, 4, ws);
            const int ns = seg == 0 ? 3 : (seg == 1 ? 6 : (seg == 2 ? 2 : 5));
            unsigned dl = seg == 3 ? 0u - 15 * STEP_BYTES : STEP_BYTES;
#pragma unroll 1
            for (int s = 0; s < ns; ++s) {
                unsigned kn = kp + dl, vn = vp + dl; PIN(kn); PIN(vn); SBAR();
                PERIOD(S0A, S0B, S1A, S1B, KMb, KMa, KRX, QRA1, QRB1, V0a, V1a, V0b, V1b, vp + KB_BYTES, kp + 2 * KB_BYTES, LD(KRY, u32x2, rp + 256));
                PERIOD(S1A, S1B, S0A, S0B, KMa, KMb, KRY, QRA0, QRB0, V0b, V1b, V0a, V1a, vp + 2 * KB_BYTES, kp + 3 * KB_BYTES, NOP_);
                rp += dl; PIN(rp); SBAR();
                PERIOD(S0A, S0B, S1A, S1B, KMb, KMa, KRY, QRA1, QRB1, V0a, V1a, V0b, V1b, vp + 3 * KB_BYTES, kn, LD(KRX, u32x2, rp));
                PERIOD(S1A, S1B, S0A, S0B, KMa, KMb, KRX, QRA0, QRB0, V0b, V1b, V0a, V1a, vn, kn + KB_BYTES, NOP_);
                kp = kn; vp = vn; dl = STEP_BYTES;
            }
        }
        P1[2] = cvtpk(f0, f1); P1[3] = cvtpk(f2, f3); PIN(P1); SBAR();
        OB = MFMA(V1b, P1, OB);
        STAMP(10);
        auto rowsum = [&](const f32x16& O) { const auto sw = __builtin_amdgcn_permlane32_swap(__float_as_uint(O[10]), __float_as_uint(O[10]), false, false); return __uint_as_float(sw[0]); };
        float lA = rowsum(OA), lB = rowsum(OB);
        if (__any(!(lA >= 0x1p-80f) || !(lB >= 0x1p-80f))) {
            const unsigned vo = WS_KVIMG + 1024 + hi * 304 + (n < 18 ? n : 18) * 16;
#pragma unroll 1
            for (int which = 0; which < 2; ++which) {
                const u32x4 Q = which ? QB : QA; const u32x2 R0 = which ? QRB0 : QRA0, R1 = which ? QRB1 : QRA1;
                float mx = -__builtin_inff();
#pragma unroll 1
                for (int kb = 0; kb < 64; ++kb) {
                    const unsigned bo = (kb >> 2) * STEP_BYTES;
                    const u32x4 km = __builtin_bit_cast(u32x4, __builtin_amdgcn_raw_buffer_load_b128(rws, WS_KVIMG + bo + (kb & 3) * KB_BYTES + lane * 16, 0, 16));
                    const u32x2 kr = __builtin_bit_cast(u32x2, __builtin_amdgcn_raw_buffer_load_b64(rws, WS_KVIMG + bo + KR_OFF + ((kb >> 1) & 1) * 256 + n * 8, 0, 16));
                    f32x16 S = {}; S = MFMA(km, Q, S); S = MFMA8(kr, (kb & 1) ? R1 : R0, S);
#pragma unroll
                    for (int i = 0; i < 16; ++i) mx = __builtin_fmaxf(mx, S[i]);
                }
                { const auto sw = __builtin_amdgcn_permlane32_swap(__float_as_uint(mx), __float_as_uint(mx), false, false); mx = __builtin_fmaxf(__uint_as_float(sw[0]), __uint_as_float(sw[1])); }
                f32x16 O = {}, C;
#pragma unroll
                for (int i = 0; i < 16; ++i) C[i] = -mx;
#pragma unroll 1
                for (int kb = 0; kb < 64; ++kb) {
                    const unsigned bo = (kb >> 2) * STEP_BYTES, bb = bo + (kb & 3) * KB_BYTES;
                    const u32x4 km = __builtin_bit_cast(u32x4, __builtin_amdgcn_raw_buffer_load_b128(rws, WS_KVIMG + bb + lane * 16, 0, 16));
                    const u32x2 kr = __builtin_bit_cast(u32x2, __builtin_amdgcn_raw_buffer_load_b64(rws, WS_KVIMG + bo + KR_OFF + ((kb >> 1) & 1) * 256 + n * 8, 0, 16));
                    const u32x4 v0 = __builtin_bit_cast(u32x4, __builtin_amdgcn_raw_buffer_load_b128(rws, vo + bb, 0, 16));
                    const u32x4 v1 = __builtin_bit_cast(u32x4, __builtin_amdgcn_raw_buffer_load_b128(rws, vo + bb + 608, 0, 16));
                    f32x16 S = MFMA(km, Q, C); S = MFMA8(kr, (kb & 1) ? R1 : R0, S);
                    float p[16];
#pragma unroll
                    for (int i = 0; i < 16; ++i) p[i] = EX(S[i]);
                    const u32x4 p0 = {cvtpk(p[0], p[1]), cvtpk(p[2], p[3]), cvtpk(p[4], p[5]), cvtpk(p[6], p[7])};
                    const u32x4 p1 = {cvtpk(p[8], p[9]), cvtpk(p[10], p[11]), cvtpk(p[12], p[13]), cvtpk(p[14], p[15])};
                    O = MFMA(v0, p0, O); O = MFMA(v1, p1, O);
                }
                const float lo = rowsum(O);
                if (which) { OB = O; lB = lo; } else { OA = O; lA = lo; }
            }
        }
        {
            const float ga = (1.f - gate) * __builtin_amdgcn_rcpf(lA), gb = (1.f - gate) * __builtin_amdgcn_rcpf(lB);
            auto word = [&](float lo, float hi_) { const unsigned w = cvtpk(lo, hi_); return w < POISON ? w : POISON - 1; };
            const u32x4 fa = {word(OA[0] * ga, OA[1] * ga), word(OA[2] * ga, OA[3] * ga), word(OA[4] * ga, OA[5] * ga), word(OA[6] * ga, OA[7] * ga)};
            const u32x4 fb = {word(OB[0] * gb, OB[1] * gb), word(OB[2] * gb, OB[3] * gb), word(OB[4] * gb, OB[5] * gb), word(OB[6] * gb, OB[7] * gb)};
            const u32x2 rr = {word(OA[8] * ga, OA[9] * ga), word(OB[8] * gb, OB[9] * gb)};
            __builtin_amdgcn_raw_buffer_store_b128(fa, rws, obm, 0, 16);
            __builtin_amdgcn_raw_buffer_store_b128(fb, rws, obm + 1024, 0, 16);
            if (!hi) __builtin_amdgcn_raw_buffer_store_b64(rr, rws, obr, 0, 16);
            lds_inc(C_BDONE, lane);
        }
        STAMP(11);
    } else {
        __builtin_amdgcn_s_setprio(1);
        {
            unsigned ph[18];
            features(acc0, acc2[0], acc2[1], acc2[2], acc2[3], lane, ph);
            const unsigned pa = L_PHIK + (32 * tb + n) * 160 + hi * 80;
#pragma unroll
            for (int u = 0; u < 4; ++u) LW(u32x4, pa + 16 * u) = (u32x4){ph[4 * u], ph[4 * u + 1], ph[4 * u + 2], ph[4 * u + 3]};
            LW(u32x2, pa + 64) = (u32x2){ph[16], ph[17]};
            LW(unsigned, vrow + 24) = cvtpk(acc2[8], acc2[9]); LW(unsigned, vrow + 28) = cvtpk(acc2[10], acc2[11]);
            LW(unsigned, vrow + 32) = cvtpk(acc2[12], acc2[13]); LW(unsigned, vrow + 36) = cvtpk(acc2[14], acc2[15]);
            LW(unsigned, vrow + 36 + 4 * hi) = 0x00003f80u;
            asm volatile("s_waitcnt lgkmcnt(0)" ::: "memory");
            lds_inc(C_PHI, lane);
        }
#pragma unroll 1
        for (int i = 0; i < 19; ++i) { const int p = 74 + tb + 4 * i < 147 ? 74 + tb + 4 * i : 147; dma_s_sc1(p * 1024, ws + WS_KVIMG + p * 1024, lane * 16); }
        const float gate = __uint_as_float(__builtin_amdgcn_raw_buffer_load_b32(rws, WS_G + (128 * ti + 32 * tb + n) * 4, 0, 16));
        STAMP(7);
        lds_wait<2>(C_PHI, 8, ws);
        __builtin_amdgcn_s_setprio(2);
        unsigned gen = 0;
        {
            const int hh = tb & 1, i = lane & 15;
            const int tok = 8 * g4 + (i >> 2), gr = i & 3;
            const unsigned va = L_VB + tok * 112 + hh * 56 + 8 + gr * 8;
            const int ntask = tb < 2 ? 2 : 1;
#pragma unroll 1
            for (int t = 0; t < ntask; ++t) {
                const int mbf = tb < 2 ? 2 * t : 1;
                const unsigned pa = L_PHIK + tok * 160 + hh * 80 + mbf * 32 + gr * 8;
                f32x4 d0 = {}, d1 = {};
#pragma unroll
                for (int ks = 0; ks < 4; ++ks) {
                    const u32x4 a = trd2(pa + ks * 32 * 160, pa + ks * 32 * 160 + 4 * 160);
                    const u32x4 b0 = trd2(va + ks * 32 * 112, va + ks * 32 * 112 + 4 * 112);
                    const u32x4 b1 = trd2(va + ks * 32 * 112 + 32, va + ks * 32 * 112 + 4 * 112 + 32);
                    d0 = MFMA16(a, b0, d0); d1 = MFMA16(a, b1, d1);
                }
                const unsigned ko = WS_KVP + (unsigned)(16 * tj + ti) * 5472 + hh * 2736 + (16 * mbf + 4 * g4) * 4;
                if (mbf < 2 || g4 == 0) {
                    __builtin_amdgcn_raw_buffer_store_b128(__builtin_bit_cast(u32x4, d0), rws, ko + n16 * 144, 0, 16);
                    if (n16 < 3) __builtin_amdgcn_raw_buffer_store_b128(__builtin_bit_cast(u32x4, d1), rws, ko + (16 + n16) * 144, 0, 16);
                }
            }
        }
        WAITV(0);
        lds_inc(C_LAND1, lane);
        async_bar(gen, lane, ws);
        if (tb == 0 && lane == 0) put_flag(ws, WS_FKVP + (16 * tj + ti) * 8, tag);
        STAMP(8);
        if (tb == 3) {
            const unsigned l0 = b * 21 + lane % 21;
            u64 p2 = (u64)P.woA, p3 = (u64)P.woB;
            asm volatile("" : "+s"(p2), "+s"(p3));
            const u64 ad = (lane < 21 ? p2 : p3) + (l0 < 5183 ? l0 : 5183) * 128;
            asm volatile("global_load_dword %0, %1, off" : "=&v"(pf) : "v"(ad) : "memory");
        }
        for (int p = tb; p < 37; p += 4) dma_s_sc1(p * 1024, ws + WS_KVIMG + p * 1024, lane * 16);
        unsigned pq[18];
        features(acc1, acc2[4], acc2[5], acc2[6], acc2[7], lane, pq);
        WAITV(0);
        PIN(pf);
        lds_inc(C_LAND2, lane);
        poll_flags(ws, WS_FKVP + 16 * tj * 8, 16, tag, lane, 1);
        STAMP(9);
        {
            const int at = tb * 64 + lane;
#pragma unroll 1
            for (int rd = 0; rd < 2; ++rd) {
                const int it = rd == 0 ? at : 256 + tb * 22 + lane;
                const bool ok = rd == 0 || (lane < 22 && it < 342);
                const unsigned ko = WS_KVP + (unsigned)(16 * tj) * 5472 + (ok ? it : 0) * 16;
                f32x4 v[16];
#pragma unroll
                for (int i = 0; i < 16; ++i) v[i] = __builtin_bit_cast(f32x4, __builtin_amdgcn_raw_buffer_load_b128(rws, ko + i * 5472, 0, 16));
                f32x4 s = v[0];
#pragma unroll
                for (int i = 1; i < 16; ++i) s += v[i];
                const int hh = it >= 171, rem = it - 171 * hh, d = rem / 9, fq = rem - 9 * d;
                if (ok) LW(u32x2, L_KVT + hh * 1600 + d * 80 + fq * 8) = (u32x2){cvtpk(s[0], s[1]), cvtpk(s[2], s[3])};
            }
            if (tb == 3 && lane < 10) LW(u32x4, L_KVT + (lane >= 5) * 1600 + 19 * 80 + (lane % 5) * 16) = (u32x4){0u, 0u, 0u, 0u};
        }
        async_bar(gen, lane, ws);
        {
            const int hm = (n >> 2) & 1, d0 = 4 * (n >> 3) + (n & 3);
            const unsigned a0 = L_KVT + (hm == hi ? hm * 1600 + d0 * 80 : 19 * 80);
            const unsigned a1 = L_KVT + (hm == hi && d0 < 3 ? hm * 1600 + (16 + d0) * 80 : 19 * 80);
            f32x16 N0 = {}, N1 = {};
#pragma unroll
            for (int s = 0; s < 4; ++s) {
                const u32x4 bq = {pq[4 * s], pq[4 * s + 1], pq[4 * s + 2], pq[4 * s + 3]};
                N0 = MFMA(LA(u32x4, a0 + 16 * s), bq, N0); N1 = MFMA(LA(u32x4, a1 + 16 * s), bq, N1);
            }
            const u32x2 br = {pq[16], pq[17]};
            N0 = MFMA8(LA(u32x2, a0 + 64), br, N0); N1 = MFMA8(LA(u32x2, a1 + 64), br, N1);
            const float sc = gate / (N1[2] + 1e-6f);
            const unsigned fo = WS_OAF + (unsigned)(4 * ti + tb) * 36 * 1024;
            const u32x4 f0 = {cvtpk(N0[0] * sc, N0[1] * sc), cvtpk(N0[2] * sc, N0[3] * sc), cvtpk(N0[4] * sc, N0[5] * sc), cvtpk(N0[6] * sc, N0[7] * sc)};
            const u32x4 f1 = {cvtpk(N0[8] * sc, N0[9] * sc), cvtpk(N0[10] * sc, N0[11] * sc), cvtpk(N0[12] * sc, N0[13] * sc), cvtpk(N0[14] * sc, N0[15] * sc)};
            __builtin_amdgcn_raw_buffer_store_b128(f0, rws, fo + (2 * tj) * 1024 + lane * 16, 0, 16);
            __builtin_amdgcn_raw_buffer_store_b128(f1, rws, fo + (2 * tj + 1) * 1024 + lane * 16, 0, 16);
            __builtin_amdgcn_raw_buffer_store_b32(cvtpk(N1[0] * sc, N1[1] * sc), rws, fo + (32 + (tj >> 2)) * 1024 + (((tj >> 1) & 1) * 32 + n) * 16 + (2 * (tj & 1) + hi) * 4, 0, 16);
        }
        WAITV(0);
        async_bar(gen, lane, ws);
        if (tb == 0 && lane == 0) put_flag(ws, WS_FOA + (16 * ti + tj) * 8, tag);
        STAMP(10);
        const int tt = 4 * x8 + (r & 3), cg = r >> 2;
        unsigned wo[9];
#pragma unroll
        for (int i = 0; i < 9; ++i) { const unsigned q = (tb + 4 * i) * 64 + lane, row = q / 9; wo[i] = row * 1152 + (q - row * 9) * 16; }
        auto stage = [&](const char* w) {
            const char* wsl = w + cg * 144;
#pragma unroll
            for (int i = 0; i < 21; ++i) {
                const int p = tb + 4 * i;
                if (i < 20 || tb == 0) dma_s(L_WOS + p * 1024, wsl + (i / 9) * 256 * 1152, wo[i % 9]);
            }
        };
        const int i16 = lane & 15, gr = i16 & 3, hk = g4 & 1, fh = g4 >> 1;
        const unsigned fro = (unsigned)(2 * tt + (tb >> 1)) * 36 * 1024 + fh * 1024 + (hk * 32 + n16 + 16 * (tb & 1)) * 16;
        const unsigned remo = L_WOS + (18 * (8 * fh + 4 * hk + (i16 >> 3)) + 16 + ((i16 >> 2) & 1)) * 144 + gr * 8;
        f32x4 T[5] = {};
        auto wfrag = [&](int ks, int cb, unsigned maino, unsigned r4s) {
            const unsigned a = (ks < 16 ? maino + ks * 36 * 144 : remo + (ks - 16) * 16 * 18 * 144) + cb * 32;
            return trd2(a, a + (ks < 16 ? r4s : 2 * 18 * 144));
        };
        auto gemm = [&](const u32x4 (&bf)[18], unsigned maino, unsigned r4s, auto ord) {
            u32x4 wf[2][5];
#pragma unroll
            for (int cb = 0; cb < 5; ++cb) wf[0][cb] = wfrag(ord(0), cb, maino, r4s);
            SBAR();
#pragma unroll
            for (int q = 0; q < 18; ++q) {
#pragma unroll
                for (int cb = 0; cb < 5; ++cb) {
                    T[cb] = MFMA16(wf[q & 1][cb], bf[q], T[cb]); SBAR();
                    if (q < 17) wf[(q + 1) & 1][cb] = wfrag(ord(q + 1), cb, maino, r4s);
                    SBAR();
                }
            }
        };
        u32x4 bf[18];
        poll_flags(ws, WS_FOA + 16 * (tt >> 1) * 8, 16, tag, lane, 1);
#pragma unroll
        for (int ks = 0; ks < 18; ++ks) bf[ks] = __builtin_bit_cast(u32x4, __builtin_amdgcn_raw_buffer_load_b128(rws, WS_OAF + fro + ks * 2048, 0, 16));
        SBAR();
        lds_wait<16>(C_BPROG, 4, ws);
        stage(P.woA);
        WAITV(0);
        async_bar(gen, lane, ws);
        STAMP(11);
        gemm(bf, L_WOS + (18 * hk + 8 * fh + (i16 >> 2)) * 144 + gr * 8, 4 * 144, [](int q) { return q; });
        async_bar(gen, lane, ws);
        STAMP(12);
        stage(P.woB);
        WAITV(0);
        async_bar(gen, lane, ws);
        STAMP(13);
        {
            const unsigned maino = L_WOS + (18 * fh + 4 * hk + (i16 >> 2)) * 144 + gr * 8;
            const unsigned remg = L_WOS + (36 * g4 + 16 + 18 * (i16 >> 3) + ((i16 >> 2) & 1)) * 144 + gr * 8;
            const unsigned rro = WS_OBF + (unsigned)(2 * tt + (tb >> 1)) * 36 * 1024 + 32 * 1024 + ((g4 >> 1) * 32 + 16 * (tb & 1) + n16) * 16 + (g4 & 1) * 8;
            f32x4 G[4][5];
#pragma unroll
            for (int cb = 0; cb < 5; ++cb) { G[0][cb] = T[cb]; G[1][cb] = (f32x4){}; G[2][cb] = (f32x4){}; G[3][cb] = (f32x4){}; }
            u32x2 br[4];
            unsigned have = 0, done = 0;
            bool bdone = false;
#pragma unroll 1
            for (unsigned s = 0;; ++s) {
#pragma unroll
                for (int g = 0; g < 4; ++g) {
                    if (!((have >> g) & 1)) {
#pragma unroll
                        for (int u = 0; u < 4; ++u) bf[4 * g + u] = __builtin_bit_cast(u32x4, __builtin_amdgcn_raw_buffer_load_b128(rws, WS_OBF + fro + (4 * g + u) * 2048, 0, 16));
                        br[g] = __builtin_bit_cast(u32x2, __builtin_amdgcn_raw_buffer_load_b64(rws, rro + g * 1024, 0, 16));
                    }
                }
#pragma unroll
                for (int g = 0; g < 4; ++g) {
                    if (!((have >> g) & 1)) {
                        unsigned m = br[g][0] > br[g][1] ? br[g][0] : br[g][1];
#pragma unroll
                        for (int u = 0; u < 4; ++u) {
                            const u32x4 w = bf[4 * g + u];
                            const unsigned m0 = w[0] > w[1] ? w[0] : w[1], m1 = w[2] > w[3] ? w[2] : w[3];
                            m = m > m0 ? m : m0; m = m > m1 ? m : m1;
                        }
                        if (__all(m != POISON)) have |= 1u << g;
                    }
                }
                if (!bdone) { bdone = uni(__hip_atomic_load((AS3 unsigned*)(uintptr_t)C_BDONE, __ATOMIC_RELAXED, __HIP_MEMORY_SCOPE_WORKGROUP)) >= 4; if (bdone) STAMP(14); }
                if (bdone) {
#pragma unroll
                    for (int g = 0; g < 4; ++g) {
                        if (((have & ~done) >> g) & 1) {
                            u32x4 wf[2][5]; u32x2 wr[5];
#pragma unroll
                            for (int cb = 0; cb < 5; ++cb) wf[0][cb] = wfrag(4 * g, cb, maino, 8 * 144);
                            SBAR();
#pragma unroll
                            for (int u = 0; u < 4; ++u) {
#pragma unroll
                                for (int cb = 0; cb < 5; ++cb) {
                                    G[g][cb] = MFMA16(wf[u & 1][cb], bf[4 * g + u], G[g][cb]); SBAR();
                                    if (u < 3) wf[(u + 1) & 1][cb] = wfrag(4 * g + u + 1, cb, maino, 8 * 144);
                                    else wr[cb] = trd(remg + g * 144 * 144 + cb * 32);
                                    SBAR();
                                }
                            }
#pragma unroll
                            for (int cb = 0; cb < 5; ++cb) G[g][cb] = MFMA1616(wr[cb], br[g], G[g][cb]);
                            done |= 1u << g;
                        }
                    }
                    if (done == 15) {
                        break;
                    }
                }
                if (s > SPIN_MAX) { if (lane == 0) __hip_atomic_store((gu32*)(ws + WS_TMO), 0x300u, __ATOMIC_RELAXED, __HIP_MEMORY_SCOPE_AGENT); break; }
                if (bdone) __builtin_amdgcn_s_sleep(1); else __builtin_amdgcn_s_sleep(16);
            }
#pragma unroll
            for (int cb = 0; cb < 5; ++cb) T[cb] = (G[0][cb] + G[1][cb]) + (G[2][cb] + G[3][cb]);
        }
        STAMP(19);
        char* op = P.out + (size_t)(64 * tt + 16 * tb + n16) * 1152 + cg * 144 + g4 * 8;
#pragma unroll
        for (int cb = 0; cb < 5; ++cb) {
            const u32x2 o = {cvtpk(T[cb][0], T[cb][1]), cvtpk(T[cb][2], T[cb][3])};
            if (cb < 4 || g4 < 2) *(u32x2*)(op + cb * 32) = o;
        }
    }
}

__global__ __launch_bounds__(512, 2) void gated_mix_kernel(const void* x, const void* wqkv, const void* wphi, const void* woA, const void* wq, const void* wk,
                                                           const void* wv, const void* woB, const void* gate, void* out, void* ws) {
    extern __shared__ __attribute__((aligned(16))) char lds[];
    const int lane = threadIdx.x & 63, wid = uni(threadIdx.x >> 6);
    const Ptrs P = {(const char*)x, (const char*)wqkv, (const char*)wphi, (const char*)woA, (const char*)wq, (const char*)wk, (const char*)wv, (const char*)woB, (const char*)gate, (char*)out, (char*)ws};
    if (wid < 4) role_main<true>(P, wid, lane); else role_main<false>(P, wid, lane);
}
}

extern "C" void kernel_launch(void* const* d_in, const int* in_sizes, int n_in, void* d_out, int out_size, void* d_ws, size_t ws_size, hipStream_t stream) {
    (void)in_sizes; (void)n_in; (void)out_size; (void)ws_size;
    (void)hipFuncSetAttribute((const void*)fk::gated_mix_kernel, hipFuncAttributeMaxDynamicSharedMemorySize, fk::LDS_BYTES);
    hipLaunchKernelGGL(fk::gated_mix_kernel, dim3(256), dim3(512), fk::LDS_BYTES, stream,
                       (const void*)d_in[0], (const void*)d_in[1], (const void*)d_in[2], (const void*)d_in[3], (const void*)d_in[5], (const void*)d_in[6],
                       (const void*)d_in[7], (const void*)d_in[8], (const void*)d_in[9], d_out, d_ws);
}
```

```cpp
#include <hip/hip_runtime.h>
#include <hip/hip_bf16.h>
#include <stdint.h>

namespace fk {
typedef __bf16 bf16x2 __attribute__((ext_vector_type(2)));
typedef __bf16 bf16x8 __attribute__((ext_vector_type(8)));
typedef short s16x4 __attribute__((ext_vector_type(4)));
typedef float f32x2 __attribute__((ext_vector_type(2)));
typedef float f32x4 __attribute__((ext_vector_type(4)));
typedef float f32x16 __attribute__((ext_vector_type(16)));
typedef unsigned u32x2 __attribute__((ext_vector_type(2)));
typedef unsigned u32x4 __attribute__((ext_vector_type(4)));
typedef unsigned long long u64;
typedef __attribute__((address_space(1))) u64 gu64;
typedef __attribute__((address_space(1))) unsigned gu32;

#define AS3 __attribute__((address_space(3)))
#define SBAR() __builtin_amdgcn_sched_barrier(0)
#define PIN(x) asm volatile("" : "+v"(x))
#define MFMA(a, b, c) __builtin_amdgcn_mfma_f32_32x32x16_bf16(__builtin_bit_cast(bf16x8, a), __builtin_bit_cast(bf16x8, b), c, 0, 0, 0)
#define MFMA8(a, b, c) __builtin_amdgcn_mfma_f32_32x32x8bf16_1k(__builtin_bit_cast(s16x4, a), __builtin_bit_cast(s16x4, b), c, 0, 0, 0)
#define MFMA1616(a, b, c) __builtin_amdgcn_mfma_f32_16x16x16bf16_1k(__builtin_bit_cast(s16x4, a), __builtin_bit_cast(s16x4, b), c, 0, 0, 0)
#define MFMA16(a, b, c) __builtin_amdgcn_mfma_f32_16x16x32_bf16(__builtin_bit_cast(bf16x8, a), __builtin_bit_cast(bf16x8, b), c, 0, 0, 0)
#define LA(T, a) (*(const AS3 T*)(uintptr_t)(a))
#define LW(T, a) (*(AS3 T*)(uintptr_t)(a))
#define APRIO 3
#define FENCE() asm volatile("" ::: "memory")
#define EX(x) __builtin_amdgcn_exp2f(x)

#define WS_TMO 64
constexpr unsigned WS_FKV = 4096, WS_FKVP = 8192, WS_FOA = 12288, WS_FOB = 16384;
#define WS_G 32768
#define WS_KMAX 40960
#define WS_KVIMG 65536
#define WS_KVP 262144
#define WS_OAF (2 << 20)
#define WS_OBF (5 << 20)

constexpr unsigned SLOT = 36864, WOFF = 16384;
constexpr unsigned L_WKV = 110592;
constexpr unsigned L_RED = 152064, L_RES = 159744;
constexpr unsigned L_CNT = 161024, L_WPR = 161152;
constexpr unsigned L_WPHI = 152064, L_KVT = 157184;
constexpr unsigned L_PHIK = 0, L_VB = 20480;
constexpr unsigned L_KMAX = 161152, L_WOS = 37888;
constexpr unsigned KB_BYTES = 2240, STEP_BYTES = 9472, KR_OFF = 8960;
constexpr unsigned LDS_BYTES = 163840;
constexpr unsigned C_LAND0 = L_CNT, C_LAND1 = L_CNT + 4, C_LAND2 = L_CNT + 8, C_PHI = L_CNT + 12, C_ASY = L_CNT + 16, C_BPROG = L_CNT + 20, C_BDONE = L_CNT + 24, C_RAW = L_CNT + 28, C_WKVF = L_CNT + 32;
constexpr unsigned POISON = 0xffffffffu;

#define STAMP(k) do { } while (0)

__device__ __forceinline__ unsigned cvtpk(float lo, float hi) { f32x2 v = {lo, hi}; return __builtin_bit_cast(unsigned, __builtin_convertvector(v, bf16x2)); }
__device__ __forceinline__ int uni(int v) { return __builtin_amdgcn_readfirstlane(v); }

__device__ __forceinline__ void dma_s(unsigned lds, const void* sbase, unsigned voff) {
    asm volatile("s_mov_b32 m0, %0\n\ts_nop 0\n\tglobal_load_lds_dwordx4 %1, %2" :: "s"(lds), "v"(voff), "s"(sbase) : "memory", "m0");
}
__device__ __forceinline__ void dma_s_sc1(unsigned lds, const void* sbase, unsigned voff) {
    asm volatile("s_mov_b32 m0, %0\n\ts_nop 0\n\tglobal_load_lds_dwordx4 %1, %2 sc1" :: "s"(lds), "v"(voff), "s"(sbase) : "memory", "m0");
}
__device__ __forceinline__ void dma_v(unsigned lds, const void* vaddr) {
    asm volatile("s_mov_b32 m0, %0\n\ts_nop 0\n\tglobal_load_lds_dwordx4 %1, off" :: "s"(lds), "v"(vaddr) : "memory", "m0");
}
#define WAITV(n) asm volatile("s_waitcnt vmcnt(" #n ")" ::: "memory")
#define BAR_L() asm volatile("s_waitcnt lgkmcnt(0)\n\ts_barrier" ::: "memory")
template <int N> __device__ __forceinline__ void waitv() { asm volatile("s_waitcnt vmcnt(%0)" :: "n"(N) : "memory"); }
template <int N> __device__ __forceinline__ void bar_vl() { asm volatile("s_waitcnt vmcnt(%0) lgkmcnt(0)\n\ts_barrier" :: "n"(N) : "memory"); }

__device__ __forceinline__ u32x2 trd(unsigned a) { return __builtin_bit_cast(u32x2, __builtin_amdgcn_ds_read_tr16_b64_v4i16((AS3 s16x4*)(uintptr_t)a)); }
__device__ __forceinline__ u32x4 trd2(unsigned a0, unsigned a1) { const u32x2 p = trd(a0), q = trd(a1); return (u32x4){p[0], p[1], q[0], q[1]}; }

template <int CTRL> __device__ __forceinline__ float dppf(float v) { return __builtin_bit_cast(float, __builtin_amdgcn_update_dpp(0, __builtin_bit_cast(int, v), CTRL, 0xf, 0xf, true)); }
__device__ __forceinline__ float wave_max(float v) {
    v = __builtin_fmaxf(v, dppf<0x121>(v)); v = __builtin_fmaxf(v, dppf<0x122>(v)); v = __builtin_fmaxf(v, dppf<0x124>(v)); v = __builtin_fmaxf(v, dppf<0x128>(v));
    { const auto t = __builtin_amdgcn_permlane16_swap(__float_as_uint(v), __float_as_uint(v), false, false); v = __builtin_fmaxf(__uint_as_float(t[0]), __uint_as_float(t[1])); }
    { const auto t = __builtin_amdgcn_permlane32_swap(__float_as_uint(v), __float_as_uint(v), false, false); v = __builtin_fmaxf(__uint_as_float(t[0]), __uint_as_float(t[1])); }
    return v;
}
__device__ __forceinline__ u64 mix64(u64 z) { z += 0x9E3779B97F4A7C15ull; z = (z ^ (z >> 30)) * 0xBF58476D1CE4E5B9ull; z = (z ^ (z >> 27)) * 0x94D049BB133111EBull; return z ^ (z >> 31); }

constexpr unsigned SPIN_MAX = 1u << 20;
__device__ __forceinline__ void poll_flags(const char* ws, unsigned off, int n, u64 tag, int lane, int slp) {
    const gu64* p = (const gu64*)(ws + off) + (lane < n ? lane : 0);
    for (unsigned s = 0;; ++s) {
        const u64 v = __hip_atomic_load(p, __ATOMIC_RELAXED, __HIP_MEMORY_SCOPE_AGENT);
        if (__all(v == tag)) break;
        if (s > SPIN_MAX) { if (lane == 0) __hip_atomic_store((gu32*)(ws + WS_TMO), 0x100u + off, __ATOMIC_RELAXED, __HIP_MEMORY_SCOPE_AGENT); break; }
        if (slp) __builtin_amdgcn_s_sleep(8); else __builtin_amdgcn_s_sleep(1);
    }
    FENCE();
}
__device__ __forceinline__ void poll4(const char* ws, u64 tag, int lane) {
    const gu64* p = (const gu64*)(ws + WS_FKV) + lane;
    for (unsigned s = 0;; ++s) {
        const u64 v0 = __hip_atomic_load(p, __ATOMIC_RELAXED, __HIP_MEMORY_SCOPE_AGENT), v1 = __hip_atomic_load(p + 64, __ATOMIC_RELAXED, __HIP_MEMORY_SCOPE_AGENT);
        const u64 v2 = __hip_atomic_load(p + 128, __ATOMIC_RELAXED, __HIP_MEMORY_SCOPE_AGENT), v3 = __hip_atomic_load(p + 192, __ATOMIC_RELAXED, __HIP_MEMORY_SCOPE_AGENT);
        if (__all(v0 == tag && v1 == tag && v2 == tag && v3 == tag)) break;
        if (s > SPIN_MAX) { if (lane == 0) __hip_atomic_store((gu32*)(ws + WS_TMO), 0x100u, __ATOMIC_RELAXED, __HIP_MEMORY_SCOPE_AGENT); break; }
        __builtin_amdgcn_s_sleep(1);
    }
    FENCE();
}
__device__ __forceinline__ void put_flag(char* ws, unsigned off, u64 tag) { __hip_atomic_store((gu64*)(ws + off), tag, __ATOMIC_RELAXED, __HIP_MEMORY_SCOPE_AGENT); }
__device__ __forceinline__ void lds_inc(unsigned a, int lane) { if (lane == 0) __hip_atomic_fetch_add((AS3 unsigned*)(uintptr_t)a, 1u, __ATOMIC_RELAXED, __HIP_MEMORY_SCOPE_WORKGROUP); }
template <int SLP> __device__ __forceinline__ void lds_wait(unsigned a, unsigned target, const char* ws) {
    for (unsigned s = 0;; ++s) {
        const unsigned v = uni(__hip_atomic_load((AS3 unsigned*)(uintptr_t)a, __ATOMIC_RELAXED, __HIP_MEMORY_SCOPE_WORKGROUP));
        if (v >= target) break;
        if (s > SPIN_MAX) { __hip_atomic_store((gu32*)(ws + WS_TMO), 0x10u + (a & 15), __ATOMIC_RELAXED, __HIP_MEMORY_SCOPE_AGENT); break; }
        __builtin_amdgcn_s_sleep(SLP);
    }
    FENCE();
}
__device__ __forceinline__ void async_bar(unsigned& gen, int lane, const char* ws) {
    asm volatile("s_waitcnt lgkmcnt(0)" ::: "memory");
    gen += 4; lds_inc(C_ASY, lane); lds_wait<2>(C_ASY, gen, ws);
}

__device__ __forceinline__ void features(const f32x16& a0, float p16, float p17, float p18, float p19, int lane, unsigned (&out)[18]) {
    const u32x4 b0 = {cvtpk(a0[0], a0[1]), cvtpk(a0[2], a0[3]), cvtpk(a0[4], a0[5]), cvtpk(a0[6], a0[7])};
    const u32x4 b1 = {cvtpk(a0[8], a0[9]), cvtpk(a0[10], a0[11]), cvtpk(a0[12], a0[13]), cvtpk(a0[14], a0[15])};
    const u32x2 b2 = {cvtpk(p16, p17), cvtpk(p18, p19)};
    const unsigned wa = L_WPHI + lane * 16, wr = L_WPHI + 2048 + lane * 8;
    f32x16 P0 = {}, P1 = {};
    P0 = MFMA(LA(u32x4, wa), b0, P0); P1 = MFMA(LA(u32x4, wa + 2560), b0, P1);
    P0 = MFMA(LA(u32x4, wa + 1024), b1, P0); P1 = MFMA(LA(u32x4, wa + 2560 + 1024), b1, P1);
    P0 = MFMA8(LA(u32x2, wr), b2, P0); P1 = MFMA8(LA(u32x2, wr + 2560), b2, P1);
    float p[18];
#pragma unroll
    for (int i = 0; i < 16; ++i) p[i] = P0[i];
    p[16] = P1[0]; p[17] = P1[1];
    float mx = p[0], mn = p[0];
#pragma unroll
    for (int i = 1; i < 18; ++i) { mx = __builtin_fmaxf(mx, p[i]); mn = __builtin_fminf(mn, p[i]); }
    const float L = 1.4426950408889634f, mxl = -mx * L, mnl = mn * L;
    float ep[18], en[18], sp = 0.f, sn = 0.f;
#pragma unroll
    for (int i = 0; i < 18; ++i) { ep[i] = EX(__builtin_fmaf(p[i], L, mxl)); en[i] = EX(__builtin_fmaf(p[i], -L, mnl)); sp += ep[i]; sn += en[i]; }
    const float rp = __builtin_amdgcn_rcpf(sp), rn = __builtin_amdgcn_rcpf(sn);
#pragma unroll
    for (int u = 0; u < 9; ++u) { out[u] = cvtpk(ep[2 * u] * rp, ep[2 * u + 1] * rp); out[9 + u] = cvtpk(en[2 * u] * rn, en[2 * u + 1] * rn); }
}

struct Ptrs { const char *x, *wqkv, *wphi, *woA, *wq, *wk, *wv, *woB, *gate; char *out, *ws; };

template <bool ISB>
__device__ __forceinline__ void role_main(const Ptrs& P, const int wid, const int lane) {
    const int b = blockIdx.x, x8 = b & 7, r = b >> 3;
    const int ti = 8 * (x8 & 1) + (r & 7), tj = 4 * (x8 >> 1) + (r >> 3);
    const int tb = wid & 3, n = lane & 31, hi = lane >> 5, n16 = lane & 15, g4 = lane >> 4;
    const int tid = wid * 64 + lane;
    char* const ws = P.ws;
    const __amdgpu_buffer_rsrc_t rws = __builtin_amdgcn_make_buffer_rsrc((void*)ws, (short)0, 0x1000000, 0x00020000);
    STAMP(0);
    if (tid == 0) { LW(u32x4, L_CNT) = (u32x4){0u, 0u, 0u, 0u}; LW(u32x4, L_CNT + 16) = (u32x4){0u, 0u, 0u, 0u}; LW(u32x4, L_CNT + 32) = (u32x4){0u, 0u, 0u, 0u}; }
    BAR_L();
    u32x2 Ev;
    unsigned pf = 0;
    const unsigned obf = WS_OBF + (unsigned)(4 * ti + tb) * 36 * 1024, obm = obf + 2 * tj * 1024 + lane * 16;
    const unsigned obr = obf + (32 + (tj >> 2)) * 1024 + (((tj >> 1) & 1) * 32 + n) * 16 + (tj & 1) * 8;
    auto touch_w = [&](int s) {
        const unsigned l = lane < 36 ? lane : 35, cc = l / 12, ty = (l - 12 * cc) / 3, ln = l - 12 * cc - 3 * ty;
        const unsigned row = 64 * (3 * s + cc) + 32 * (x8 & 1) + r;
        u64 p0 = (u64)P.wqkv, p1 = (u64)P.wq;
        asm volatile("" : "+s"(p0), "+s"(p1));
        const u64 ad = (ty < 3 ? p0 + (row * 3456 + ty * 1152) : p1 + row * 1152) + (2 * (x8 >> 1) + ln) * 128;
        asm volatile("global_load_dword %0, %1, off" : "=&v"(pf) : "v"(ad) : "memory");
    };
    unsigned xo[4]; u64 wb[5]; unsigned wst[5];
    auto dma1 = [&](int c, int i) {
        const unsigned slot = (c & 3) * SLOT;
        if (i < 4) dma_s(slot + (4 * tb + i) * 1024, P.x + c * 128, xo[i]);
        else { dma_v(slot + WOFF + (5 * tb + i - 4) * 1024, (const char*)wb[i - 4]); wb[i - 4] += wst[i - 4]; }
    };
    auto issue = [&](int c) {
#pragma unroll
        for (int i = 0; i < 9; ++i) dma1(c, i);
    };
#pragma unroll
    for (int i = 0; i < 4; ++i) xo[i] = (unsigned)(128 * ti + 32 * tb + 8 * i + (lane >> 3)) * 1152u + (((lane & 7) ^ (4 * (i & 1) + (lane >> 4))) * 16);
    if (!ISB) {
        u32x4 xf[5], gf[5];
        __builtin_amdgcn_s_setprio(APRIO);
        {
            const unsigned go = (32 * tb + 8 * g4) * 2, g4o = tb < 2 ? go + 1024 : go, zo = 0;
            const unsigned xr = (unsigned)(128 * ti + 8 * tj + (n16 & 7)) * 1152u, xa = xr + go, x4 = xr + g4o;
            asm volatile("global_load_dwordx2 %0, %11, %16 sc1\n\t"
                         "global_load_dwordx4 %1, %12, %17\n\tglobal_load_dwordx4 %2, %12, %17 offset:256\n\tglobal_load_dwordx4 %3, %12, %17 offset:512\n\t"
                         "global_load_dwordx4 %4, %12, %17 offset:768\n\tglobal_load_dwordx4 %5, %13, %17\n\t"
                         "global_load_dwordx4 %6, %14, %18\n\tglobal_load_dwordx4 %7, %14, %18 offset:256\n\tglobal_load_dwordx4 %8, %14, %18 offset:512\n\t"
                         "global_load_dwordx4 %9, %14, %18 offset:768\n\tglobal_load_dwordx4 %10, %15, %18"
                         : "=&v"(Ev), "=&v"(xf[0]), "=&v"(xf[1]), "=&v"(xf[2]), "=&v"(xf[3]), "=&v"(xf[4]), "=&v"(gf[0]), "=&v"(gf[1]), "=&v"(gf[2]), "=&v"(gf[3]), "=&v"(gf[4])
                         : "v"(zo), "v"(xa), "v"(x4), "v"(go), "v"(g4o), "s"(ws), "s"(P.x), "s"(P.gate) : "memory");
        }
#pragma unroll
        for (int t = 0; t < 10; ++t) {
            const int p = tb + 4 * t;
            if (t != 5 || tb != 0) dma_s(L_WKV + p * 1024, p < 20 ? P.wk + p * 1024 : P.wv + (p * 1024 - 20736), lane * 16);
            else { if (lane < 16) dma_s(L_WKV + 20 * 1024, P.wk + 20 * 1024, lane * 16); else dma_s(L_WKV + 20 * 1024, P.wv - 256, lane * 16); }
        }
        if (tb == 1) { if (lane < 32) dma_s(L_WKV + 40 * 1024, P.wv + (40 * 1024 - 20736), lane * 16); }
        if (tb == 2) { dma_s(L_WPR, P.wphi + tj * 1296, lane * 16); if (lane < 17) dma_s(L_WPR + 1024, P.wphi + tj * 1296 + 1024, lane * 16); }
        STAMP(16);
        if (tb < 2) { touch_w(1 + tb); WAITV(1); } else WAITV(0);
        asm volatile("" : "+v"(Ev), "+v"(xf[0]), "+v"(xf[1]), "+v"(xf[2]), "+v"(xf[3]), "+v"(xf[4]), "+v"(gf[0]), "+v"(gf[1]), "+v"(gf[2]), "+v"(gf[3]), "+v"(gf[4]) :: "memory");
        lds_inc(C_RAW, lane); lds_wait<1>(C_RAW, 4, ws);
        STAMP(1);
        const int i = lane & 15;
        const unsigned rowa = L_WKV + (32 * tb + 8 * g4 + 2 * (i >> 2)) * 36;
        const unsigned am = rowa + 8 * (i & 3), ar = rowa + (i & 1) * 20736 + 32;
        u32x2 we0[5], we1[5], wr[5], wo0[5], wo1[5];
#pragma unroll
        for (int s = 0; s < 5; ++s) {
            const unsigned o = (s < 4 || tb < 2 ? s : 0) * 128 * 36;
            we0[s] = trd(am + o); we1[s] = trd(am + o + 20736); wr[s] = trd(ar + o); wo0[s] = trd(am + o + 40); wo1[s] = trd(am + o + 40 + 20736);
        }
        SBAR();
#pragma unroll
        for (int s = 0; s < 5; ++s) { PIN(we0[s]); PIN(we1[s]); PIN(wr[s]); PIN(wo0[s]); PIN(wo1[s]); }
        lds_inc(C_WKVF, lane);
        {
            const u32x4 pz = {POISON, POISON, POISON, POISON};
            __builtin_amdgcn_raw_buffer_store_b128(pz, rws, obm, 0, 16);
            __builtin_amdgcn_raw_buffer_store_b128(pz, rws, obm + 1024, 0, 16);
            __builtin_amdgcn_raw_buffer_store_b64((u32x2){POISON, POISON}, rws, obr, 0, 16);
        }
        f32x4 E0 = {}, E1 = {}, E2 = {}, O0 = {}, O1 = {}, O2 = {}, G = {};
#pragma unroll
        for (int s = 0; s < 5; ++s) {
            if (s < 4 || tb < 2) {
                const u32x2 be = {__builtin_amdgcn_perm(xf[s][1], xf[s][0], 0x05040100u), __builtin_amdgcn_perm(xf[s][3], xf[s][2], 0x05040100u)};
                const u32x2 bo = {__builtin_amdgcn_perm(xf[s][1], xf[s][0], 0x07060302u), __builtin_amdgcn_perm(xf[s][3], xf[s][2], 0x07060302u)};
                E0 = MFMA1616(we0[s], be, E0); E1 = MFMA1616(we1[s], be, E1); E2 = MFMA1616(wr[s], be, E2);
                O0 = MFMA1616(wo0[s], bo, O0); O1 = MFMA1616(wo1[s], bo, O1); O2 = MFMA1616(wr[s], bo, O2);
                G = MFMA16(gf[s], xf[s], G);
            }
        }
        const int ra_ = ((lane - 16) & 63) * 4;
        auto rot16 = [&](float v) { return __int_as_float(__builtin_amdgcn_ds_bpermute(ra_, __float_as_int(v))); };
        const float k0 = rot16(O0[2]), k1 = rot16(O0[3]), v0 = rot16(O1[2]), v1 = rot16(O1[3]), w0 = rot16(O2[2]), w1 = rot16(O2[3]);
        const bool g0 = g4 == 0, up = n16 >= 8;
        const f32x4 K = {E0[0] + (g0 ? O2[2] : k0), E0[1] + (g0 ? O2[3] : k1), E0[2] + O0[0], E0[3] + O0[1]};
        const f32x4 V = {E1[0] + (g0 ? w0 : v0), E1[1] + (g0 ? w1 : v1), E1[2] + O1[0], E1[3] + O1[1]};
        const unsigned ra = L_RED + tb * 1280 + (n16 & 7) * 160;
        LW(f32x4, ra + (up ? 64 : 0) + g4 * 16) = up ? V : K;
        const f32x2 rm = g0 ? (f32x2){E2[0] + k0, E2[1] + k1} : (g4 == 3 ? (f32x2){E2[0] + O1[2], E2[1] + O1[3]} : (f32x2){G[0], G[0]});
        if (!up && g4 != 2) LW(f32x2, ra + (g0 ? 128 : (g4 == 3 ? 136 : 144))) = rm;
        __builtin_amdgcn_s_setprio(0);
        STAMP(2);
    } else {
        { const unsigned zo = 0; asm volatile("global_load_dwordx2 %0, %1, %2 sc1" : "=&v"(Ev) : "v"(zo), "s"(ws) : "memory"); }
        if (tb == 0) touch_w(0);
        const int c0 = 72 * tj - 8 * (tj & 1);
#pragma unroll
        for (int i = 0; i < 5; ++i) {
            const unsigned q = (5 * tb + i) * 64 + lane, row = q / 20, wi = q - row * 20, ty = wi / 5, pc = wi - ty * 5;
            const bool isq = ty == 3;
            const unsigned rs = isq ? 1152u : 3456u, to = ty == 0 ? 1152u : (ty == 2 ? 2304u : 0u);
            wb[i] = (isq ? (u64)P.wq : (u64)P.wqkv) + (row * rs + to + c0 + pc * 16);
            wst[i] = rs * 64;
        }
        STAMP(17);
        issue(0);
        issue(1);
        issue(2);
        STAMP(18);
    }
    f32x16 acc0 = {}, acc1 = {}, acc2 = {};
    unsigned wa[2][3], xk[2][4];
    {
        const int i = lane & 15, sl = 2 * (g4 & 1) + ((i & 3) >> 1);
        const unsigned base = WOFF + (8 * (g4 >> 1) + (i >> 2)) * 320 + 8 * (tj & 1) + 32 * (i & 1);
        if (ISB) {
            wa[0][0] = base + 240 + 8 * sl;
            wa[0][1] = base + (sl == 0 ? 240 + 32 : 160 + 8 * (sl - 1));
            wa[0][2] = 0;
        } else {
            wa[0][0] = base + 8 * sl;
            wa[0][1] = base + 80 + 8 * sl;
            wa[0][2] = base + (sl == 0 ? 32 : (sl == 1 ? 80 + 32 : 160 + 24 + 8 * (sl - 2)));
        }
        const unsigned xa = (32 * tb + n) * 128 + ((hi ^ ((n >> 1) & 7)) * 16);
#pragma unroll
        for (int k = 0; k < 4; ++k) { xk[0][k] = xa ^ (k * 32); xk[1][k] = xk[0][k] + 2 * SLOT; PIN(xk[0][k]); PIN(xk[1][k]); }
#pragma unroll
        for (int m = 0; m < (ISB ? 2 : 3); ++m) { wa[1][m] = wa[0][m] + 2 * SLOT; PIN(wa[0][m]); PIN(wa[1][m]); }
    }
#define WPHI_AT 2
    constexpr bool NODMA = false;
#define HM(acc, a, b) acc = MFMA(a, b, acc)
    constexpr bool HRD = true;
    u32x4 FX[4], FW0[4], FW1[4], FW2[4];
    auto rdx = [&](int c, int ks) { return LA(u32x4, xk[(c >> 1) & 1][ks] + (c & 1) * SLOT); };
    auto rdw = [&](int c, int ks, int m) { const unsigned a = wa[(c >> 1) & 1][m] + (c & 1) * SLOT + ks * 5120; return trd2(a, a + 1280); };
    if (!ISB) BAR_L();
    else asm volatile("s_waitcnt vmcnt(18)\n\ts_barrier" : "+v"(Ev), "+v"(pf) :: "memory");
    const u64 tag = mix64(((u64)(unsigned)uni(Ev[1]) << 32) | (unsigned)uni(Ev[0]));
    STAMP(3);
    if (!ISB) {
        if (tb == 3) {
            const int t = lane & 7, bi = tj >> 2, pc = tj & 3;
            const int rho = 16 * (pc >> 1) + 8 * (t >> 2) + 4 * (pc & 1) + (t & 3);
            const int d = lane - 16;
            unsigned base, strd;
            if (lane < 16) { base = t * 40 + 8 * (lane >> 3); strd = 1; }
            else if (lane < 40) { base = d < 16 ? 16 + d : (d < 18 ? 18 + d : 0); strd = 40; }
            else if (lane < 48) { base = t * 40 + 32; strd = 1; }
            else { base = t * 40 + 36; strd = 0; }
            float v[8];
#pragma unroll
            for (int i = 0; i < 8; ++i) {
                const unsigned ad = L_RED + (base + i * strd) * 4;
                v[i] = (LA(float, ad) + LA(float, ad + 1280)) + (LA(float, ad + 2560) + LA(float, ad + 3840));
            }
            float ss = v[0] * v[0] + v[1] * v[1];
            float s6 = 0.f;
#pragma unroll
            for (int i = 2; i < 8; ++i) s6 += v[i] * v[i];
            if (lane < 16) ss += s6;
            u32x4 o = {cvtpk(v[0], v[1]), cvtpk(v[2], v[3]), cvtpk(v[4], v[5]), cvtpk(v[6], v[7])};
            if (lane == 34) o = (u32x4){0x3f803f80u, 0x3f803f80u, 0x3f803f80u, 0x3f803f80u};
            const unsigned img = WS_KVIMG + ti * STEP_BYTES;
            const unsigned o16 = lane < 16 ? img + bi * KB_BYTES + ((lane >> 3) * 32 + rho) * 16 : img + bi * KB_BYTES + 1024 + pc * 304 + d * 16;
            if (lane < 35) __builtin_amdgcn_raw_buffer_store_b128(o, rws, o16, 0, 16);
            float tot = __shfl(ss, t) + __shfl(ss, 8 + t) + __shfl(ss, 40 + t);
            tot = __builtin_fmaxf(tot, dppf<0xb1>(tot)); tot = __builtin_fmaxf(tot, dppf<0x4e>(tot)); tot = __builtin_fmaxf(tot, dppf<0x141>(tot));
            const float gv = __builtin_amdgcn_rcpf(1.f + EX(-1.4426950408889634f * v[0]));
            unsigned o4, d4;
            if (lane < 48) { o4 = img + KR_OFF + (bi >> 1) * 256 + rho * 8 + (bi & 1) * 4; d4 = o[0]; }
            else if (lane < 56) { o4 = WS_G + (128 * ti + 8 * tj + t) * 4; d4 = __float_as_uint(gv); }
            else { o4 = WS_KMAX + (16 * ti + tj) * 4; d4 = __float_as_uint(tot); }
            if (lane >= 40 && lane <= 56) __builtin_amdgcn_raw_buffer_store_b32(d4, rws, o4, 0, 16);
        }
    }
    auto build_wphi = [&]() {
            const int hm = (n >> 2) & 1;
            auto wval = [&](int mb, int pos) {
                const int f = 16 * mb + 4 * (n >> 3) + (n & 3), d = pos - 2 * hi;
                const bool ok = hm == hi && f < 18 && d >= 0 && d < 18;
                const unsigned e = LA(unsigned short, L_WPR + (ok ? (hi * 324 + d * 18 + f) * 2 : 0));
                return ok ? e : 0u;
            };
            if (tb < 2) {
                const int mb = tb;
                unsigned v[16];
#pragma unroll
                for (int pos = 0; pos < 16; ++pos) v[pos] = wval(mb, pos);
                LW(u32x4, L_WPHI + mb * 2560 + lane * 16) = (u32x4){v[0] | (v[1] << 16), v[2] | (v[3] << 16), v[4] | (v[5] << 16), v[6] | (v[7] << 16)};
                LW(u32x4, L_WPHI + mb * 2560 + 1024 + lane * 16) = (u32x4){v[8] | (v[9] << 16), v[10] | (v[11] << 16), v[12] | (v[13] << 16), v[14] | (v[15] << 16)};
            } else {
#pragma unroll
                for (int mb = 0; mb < 2; ++mb)
                    LW(u32x2, L_WPHI + mb * 2560 + 2048 + lane * 8) = (u32x2){wval(mb, 16) | (wval(mb, 17) << 16), wval(mb, 18) | (wval(mb, 19) << 16)};
            }
    };
    SBAR();
#pragma unroll
    for (int ks = 0; ks < 2; ++ks) { FX[ks] = rdx(0, ks); FW0[ks] = rdw(0, ks, 0); FW1[ks] = rdw(0, ks, 1); if (!ISB) FW2[ks] = rdw(0, ks, 2); }
    u32x2 fl0, fl1, fl2, fl3;
    SBAR();
#pragma unroll
    for (int c = 0; c < 9; ++c) {
#pragma unroll
        for (int ks = 0; ks < 4; ++ks) {
            if (ks == 0 && c == 0 && ISB) { lds_wait<1>(C_WKVF, 4, ws); STAMP(28); }
            if (ks == 0 && c == 6 && !ISB) {
                if (wid == 7) {
                    const unsigned fo = WS_FKV + lane * 8;
                    asm volatile("global_load_dwordx2 %0, %4, %5 sc1\n\tglobal_load_dwordx2 %1, %4, %5 offset:512 sc1\n\t"
                                 "global_load_dwordx2 %2, %4, %5 offset:1024 sc1\n\tglobal_load_dwordx2 %3, %4, %5 offset:1536 sc1"
                                 : "=&v"(fl0), "=&v"(fl1), "=&v"(fl2), "=&v"(fl3) : "v"(fo), "s"(ws) : "memory");
                }
            }
            if (ks == 2) {
                if (ISB) {
                    if (c == 0) bar_vl<18>(); else if (c == 1) bar_vl<14>(); else if (c <= 5) bar_vl<10>(); else if (c == 6) bar_vl<5>(); else if (c == 7) bar_vl<0>(); else BAR_L();
                } else if (c == 1) {
                    bar_vl<4>();
                    PIN(pf);
                    if (tb == 3 && lane == 0) put_flag(ws, WS_FKV + (16 * ti + tj) * 8, tag);
                } else if (c >= 3 && c <= 5) bar_vl<8>();
                else if (c == 6) { if (tb == 3) WAITV(8); else WAITV(4); BAR_L(); }
                else if (c == 7) {
                    if (tb == 3) {
                        asm volatile("s_waitcnt vmcnt(0)" : "+v"(fl0), "+v"(fl1), "+v"(fl2), "+v"(fl3) :: "memory");
                        const unsigned t0 = (unsigned)tag, t1 = (unsigned)(tag >> 32);
                        const bool seen = fl0[0] == t0 && fl0[1] == t1 && fl1[0] == t0 && fl1[1] == t1 && fl2[0] == t0 && fl2[1] == t1 && fl3[0] == t0 && fl3[1] == t1;
                        if (!__all(seen)) poll4(ws, tag, lane);
                        if (b == 0 && lane == 0) __hip_atomic_store((gu64*)ws, tag, __ATOMIC_RELAXED, __HIP_MEMORY_SCOPE_AGENT);
                    }
                    bar_vl<0>();
                } else BAR_L();
                if (!ISB && c == WPHI_AT) { if (tb < 3) build_wphi(); }
                if (c == 0) STAMP(20);
                if (c == 1) STAMP(21);
                if (c == 3) STAMP(4);
                if (c == 5) STAMP(22);
                if (c == 7) STAMP(23);
                if (c == 8) STAMP(6);
            }
            const int lc = ks < 2 ? c : c + 1, lk = (ks + 2) & 3;
            const bool ld = lc < 9;
            const int dc = ks >= 2 ? c + 4 : c + 3;
            const int p0 = dc == 3 ? 5 * ks : (ISB ? (ks == 2 ? 4 : (ks == 3 ? 5 : (ks == 0 ? 6 : 8))) : (ks + 2) & 3);
            const int p1 = NODMA || dc >= 9 ? p0 : (dc == 3 ? (!ISB ? p0 : (ks == 0 ? 5 : 9)) : (ISB ? (ks == 2 ? 5 : (ks == 3 ? 6 : (ks == 0 ? 8 : 9))) : p0 + 1));
            const int pm = p0 + (p1 - p0 + 1) / 2;
            const int ei = 4 * (c - 7) + ks - 2;
            auto early = [&](int e) {
                if (e < 9) { const unsigned p = 37 + tb + 4 * e; dma_s_sc1(p * 1024, ws + WS_KVIMG + p * 1024, lane * 16); }
                else dma_s_sc1(tb == 1 ? L_KMAX : 73 * 1024, ws + (tb == 1 ? WS_KMAX : WS_KVIMG + 73 * 1024), lane * 16);
            };
            HM(acc0, FW0[ks], FX[ks]); SBAR();
            if (ld && HRD) { FX[lk] = rdx(lc, lk); FW0[lk] = rdw(lc, lk, 0); } SBAR();
#pragma unroll
            for (int p = p0; p < pm; ++p) dma1(dc, p);
            if (ISB && ei >= 0 && ei < 4) { early(ei < 2 ? 3 * ei : 2 * ei + 2); if (ei < 2) early(3 * ei + 1); }
            SBAR();
            HM(acc1, FW1[ks], FX[ks]); SBAR();
            if (ld && HRD) FW1[lk] = rdw(lc, lk, 1); SBAR();
#pragma unroll
            for (int p = pm; p < p1; ++p) dma1(dc, p);
            if (ISB && ei >= 0 && ei < 4) early(ei < 2 ? 3 * ei + 2 : 2 * ei + 3);
            SBAR();
            if (!ISB) { HM(acc2, FW2[ks], FX[ks]); SBAR(); if (ld && HRD) FW2[lk] = rdw(lc, lk, 2); SBAR(); }
        }
    }
    STAMP(5);
    const unsigned vrow = L_VB + (32 * tb + n) * 112 + hi * 52 + 8;

    if (ISB) {
#pragma unroll
        for (int u = 0; u < 6; ++u) LW(unsigned, vrow + 4 * u) = cvtpk(acc1[4 + 2 * u], acc1[5 + 2 * u]);
        asm volatile("s_waitcnt vmcnt(0) lgkmcnt(0)" ::: "memory");
        lds_inc(C_PHI, lane); lds_inc(C_LAND0, lane);
        const float gate = __uint_as_float(__builtin_amdgcn_raw_buffer_load_b32(rws, WS_G + (128 * ti + 32 * tb + n) * 4, 0, 16));
        u32x4 QA, QB; u32x2 QRA0, QRA1, QRB0, QRB1; f32x16 MA, MB;
        {
            const float sc = 0.23570226039551584f * 1.4426950408889634f;
            float q[20];
#pragma unroll
            for (int i = 0; i < 16; ++i) q[i] = acc0[i] * sc;
#pragma unroll
            for (int i = 0; i < 4; ++i) q[16 + i] = acc1[i] * sc;
            float n2 = hi ? q[18] * q[18] + q[19] * q[19] : q[0] * q[0] + q[1] * q[1];
#pragma unroll
            for (int i = 2; i < 18; ++i) n2 = __builtin_fmaf(q[i], q[i], n2);
            unsigned cc[10], D[9];
#pragma unroll
            for (int u = 0; u < 10; ++u) cc[u] = cvtpk(q[2 * u], q[2 * u + 1]);
#pragma unroll
            for (int u = 0; u < 9; ++u) D[u] = hi ? cc[u + 1] : cc[u];
#pragma unroll
            for (int u = 0; u < 4; ++u) { const auto sw = __builtin_amdgcn_permlane32_swap(D[u], D[u + 4], false, false); QA[u] = sw[0]; QB[u] = sw[1]; }
            QRA0 = (u32x2){hi ? 0u : D[8], 0u}; QRA1 = (u32x2){0u, hi ? 0u : D[8]};
            QRB0 = (u32x2){hi ? D[8] : 0u, 0u}; QRB1 = (u32x2){0u, hi ? D[8] : 0u};
            SBAR();
            lds_wait<1>(C_LAND0, 4, ws);
            const f32x4 km4 = LA(f32x4, L_KMAX + lane * 16);
            const float km = wave_max(__builtin_fmaxf(__builtin_fmaxf(km4[0], km4[1]), __builtin_fmaxf(km4[2], km4[3])));
            const float m = -1.01f * __builtin_amdgcn_sqrtf(n2 * km);
            const auto sw = __builtin_amdgcn_permlane32_swap(__float_as_uint(m), __float_as_uint(m), false, false);
            const float ma = __uint_as_float(sw[0]), mb = __uint_as_float(sw[1]);
#pragma unroll
            for (int i = 0; i < 16; ++i) { MA[i] = ma; MB[i] = mb; }
            PIN(MA); PIN(MB); PIN(QA); PIN(QB); PIN(QRA0); PIN(QRA1); PIN(QRB0); PIN(QRB1);
        }
        STAMP(7);
        unsigned kp = 4 * STEP_BYTES + lane * 16, rp = 4 * STEP_BYTES + KR_OFF + n * 8, vp = 4 * STEP_BYTES + 1024 + hi * 304 + (n < 18 ? n : 18) * 16;
        if (n > 18) vp = 0x80000;
        PIN(kp); PIN(rp); PIN(vp);
        f32x16 OA = {}, OB = {}, S0A, S0B, S1A, S1B;
        u32x4 KMa, KMb, V0a, V0b, V1a, V1b = {}, P0 = {}, P1 = {}; u32x2 KRX, KRY;
        float e0, e1, e2, e3, f0 = 0.f, f1 = 0.f, f2 = 0.f, f3 = 0.f;
        KRX = LA(u32x2, rp); KMa = LA(u32x4, kp);
        S0A = MFMA(KMa, QA, MA); S0B = MFMA(KMa, QB, MB); S0A = MFMA8(KRX, QRA0, S0A); S0B = MFMA8(KRX, QRB0, S0B);
        SBAR();
        KMb = LA(u32x4, kp + KB_BYTES); V0a = LA(u32x4, vp); V1a = LA(u32x4, vp + 608);
        __builtin_amdgcn_s_waitcnt(0xC07F);
        SBAR();
#define LD(dst, T, p) dst = LA(T, p)
#define NOP_ (void)0
#define SLOTB(MF, POST, d0, d1, d2, d3, S, g, Pq, i, a0, a1, a2, a3) do { MF; SBAR(); POST; SBAR(); \
        d0 = EX(S[4 * (g)]); d1 = EX(S[4 * (g) + 1]); PIN(d0); PIN(d1); SBAR(); \
        Pq[i] = cvtpk(a0, a1); Pq[i + 1] = cvtpk(a2, a3); PIN(Pq); SBAR(); \
        d2 = EX(S[4 * (g) + 2]); d3 = EX(S[4 * (g) + 3]); PIN(d2); PIN(d3); SBAR(); } while (0)
#define E_ e0, e1, e2, e3
#define F_ f0, f1, f2, f3
#define SLOT_(...) SLOTB(__VA_ARGS__)
#define WAITL __builtin_amdgcn_s_waitcnt(0xC07F)
#define PERIOD(SA, SB, NA, NB, KMu, KMl, KRu, QRAx, QRBx, V0u, V1u, V0l, V1l, va, ka, PA3) do { \
        SLOT_(NA = MFMA(KMu, QA, MA),     LD(KMl, u32x4, ka),              E_, SA, 0, P1, 2, F_); \
        SLOT_(OB = MFMA(V1l, P1, OB),     LD(V1l, u32x4, (va) + 608),      F_, SA, 1, P0, 0, E_); \
        SLOT_(NA = MFMA8(KRu, QRAx, NA),  LD(V0l, u32x4, va),              E_, SA, 2, P0, 2, F_); \
        SLOT_(OA = MFMA(V0u, P0, OA),     PA3,                             F_, SA, 3, P1, 0, E_); \
        SLOT_(NB = MFMA(KMu, QB, MB),     NOP_,                            E_, SB, 0, P1, 2, F_); \
        SLOT_(OA = MFMA(V1u, P1, OA),     NOP_,                            F_, SB, 1, P0, 0, E_); \
        SLOT_(NB = MFMA8(KRu, QRBx, NB),  WAITL,                           E_, SB, 2, P0, 2, F_); \
        SLOT_(OB = MFMA(V0u, P0, OB),     NOP_,                            F_, SB, 3, P1, 0, E_); \
    } while (0)
#pragma unroll 1
        for (int seg = 0; seg < 4; ++seg) {
            if (seg == 1) lds_wait<1>(C_LAND1, 4, ws);
            if (seg == 2) { lds_inc(C_BPROG, lane); STAMP(8); }
            if (seg == 3) lds_wait<1>(C_LAND2, 4, ws);
            const int ns = seg == 0 ? 3 : (seg == 1 ? 6 : (seg == 2 ? 2 : 5));
            unsigned dl = seg == 3 ? 0u - 15 * STEP_BYTES : STEP_BYTES;
#pragma unroll 1
            for (int s = 0; s < ns; ++s) {
                unsigned kn = kp + dl, vn = vp + dl; PIN(kn); PIN(vn); SBAR();
                PERIOD(S0A, S0B, S1A, S1B, KMb, KMa, KRX, QRA1, QRB1, V0a, V1a, V0b, V1b, vp + KB_BYTES, kp + 2 * KB_BYTES, LD(KRY, u32x2, rp + 256));
                PERIOD(S1A, S1B, S0A, S0B, KMa, KMb, KRY, QRA0, QRB0, V0b, V1b, V0a, V1a, vp + 2 * KB_BYTES, kp + 3 * KB_BYTES, NOP_);
                rp += dl; PIN(rp); SBAR();
                PERIOD(S0A, S0B, S1A, S1B, KMb, KMa, KRY, QRA1, QRB1, V0a, V1a, V0b, V1b, vp + 3 * KB_BYTES, kn, LD(KRX, u32x2, rp));
                PERIOD(S1A, S1B, S0A, S0B, KMa, KMb, KRX, QRA0, QRB0, V0b, V1b, V0a, V1a, vn, kn + KB_BYTES, NOP_);
                kp = kn; vp = vn; dl = STEP_BYTES;
            }
        }
        P1[2] = cvtpk(f0, f1); P1[3] = cvtpk(f2, f3); PIN(P1); SBAR();
        OB = MFMA(V1b, P1, OB);
        STAMP(10);
        auto rowsum = [&](const f32x16& O) { const auto sw = __builtin_amdgcn_permlane32_swap(__float_as_uint(O[10]), __float_as_uint(O[10]), false, false); return __uint_as_float(sw[0]); };
        float lA = rowsum(OA), lB = rowsum(OB);
        if (__any(!(lA >= 0x1p-80f) || !(lB >= 0x1p-80f))) {
            const unsigned vo = WS_KVIMG + 1024 + hi * 304 + (n < 18 ? n : 18) * 16;
#pragma unroll 1
            for (int which = 0; which < 2; ++which) {
                const u32x4 Q = which ? QB : QA; const u32x2 R0 = which ? QRB0 : QRA0, R1 = which ? QRB1 : QRA1;
                float mx = -__builtin_inff();
#pragma unroll 1
                for (int kb = 0; kb < 64; ++kb) {
                    const unsigned bo = (kb >> 2) * STEP_BYTES;
                    const u32x4 km = __builtin_bit_cast(u32x4, __builtin_amdgcn_raw_buffer_load_b128(rws, WS_KVIMG + bo + (kb & 3) * KB_BYTES + lane * 16, 0, 16));
                    const u32x2 kr = __builtin_bit_cast(u32x2, __builtin_amdgcn_raw_buffer_load_b64(rws, WS_KVIMG + bo + KR_OFF + ((kb >> 1) & 1) * 256 + n * 8, 0, 16));
                    f32x16 S = {}; S = MFMA(km, Q, S); S = MFMA8(kr, (kb & 1) ? R1 : R0, S);
#pragma unroll
                    for (int i = 0; i < 16; ++i) mx = __builtin_fmaxf(mx, S[i]);
                }
                { const auto sw = __builtin_amdgcn_permlane32_swap(__float_as_uint(mx), __float_as_uint(mx), false, false); mx = __builtin_fmaxf(__uint_as_float(sw[0]), __uint_as_float(sw[1])); }
                f32x16 O = {}, C;
#pragma unroll
                for (int i = 0; i < 16; ++i) C[i] = -mx;
#pragma unroll 1
                for (int kb = 0; kb < 64; ++kb) {
                    const unsigned bo = (kb >> 2) * STEP_BYTES, bb = bo + (kb & 3) * KB_BYTES;
                    const u32x4 km = __builtin_bit_cast(u32x4, __builtin_amdgcn_raw_buffer_load_b128(rws, WS_KVIMG + bb + lane * 16, 0, 16));
                    const u32x2 kr = __builtin_bit_cast(u32x2, __builtin_amdgcn_raw_buffer_load_b64(rws, WS_KVIMG + bo + KR_OFF + ((kb >> 1) & 1) * 256 + n * 8, 0, 16));
                    const u32x4 v0 = __builtin_bit_cast(u32x4, __builtin_amdgcn_raw_buffer_load_b128(rws, vo + bb, 0, 16));
                    const u32x4 v1 = __builtin_bit_cast(u32x4, __builtin_amdgcn_raw_buffer_load_b128(rws, vo + bb + 608, 0, 16));
                    f32x16 S = MFMA(km, Q, C); S = MFMA8(kr, (kb & 1) ? R1 : R0, S);
                    float p[16];
#pragma unroll
                    for (int i = 0; i < 16; ++i) p[i] = EX(S[i]);
                    const u32x4 p0 = {cvtpk(p[0], p[1]), cvtpk(p[2], p[3]), cvtpk(p[4], p[5]), cvtpk(p[6], p[7])};
                    const u32x4 p1 = {cvtpk(p[8], p[9]), cvtpk(p[10], p[11]), cvtpk(p[12], p[13]), cvtpk(p[14], p[15])};
                    O = MFMA(v0, p0, O); O = MFMA(v1, p1, O);
                }
                const float lo = rowsum(O);
                if (which) { OB = O; lB = lo; } else { OA = O; lA = lo; }
            }
        }
        {
            const float ga = (1.f - gate) * __builtin_amdgcn_rcpf(lA), gb = (1.f - gate) * __builtin_amdgcn_rcpf(lB);
            auto word = [&](float lo, float hi_) { const unsigned w = cvtpk(lo, hi_); return w < POISON ? w : POISON - 1; };
            const u32x4 fa = {word(OA[0] * ga, OA[1] * ga), word(OA[2] * ga, OA[3] * ga), word(OA[4] * ga, OA[5] * ga), word(OA[6] * ga, OA[7] * ga)};
            const u32x4 fb = {word(OB[0] * gb, OB[1] * gb), word(OB[2] * gb, OB[3] * gb), word(OB[4] * gb, OB[5] * gb), word(OB[6] * gb, OB[7] * gb)};
            const u32x2 rr = {word(OA[8] * ga, OA[9] * ga), word(OB[8] * gb, OB[9] * gb)};
            __builtin_amdgcn_raw_buffer_store_b128(fa, rws, obm, 0, 16);
            __builtin_amdgcn_raw_buffer_store_b128(fb, rws, obm + 1024, 0, 16);
            if (!hi) __builtin_amdgcn_raw_buffer_store_b64(rr, rws, obr, 0, 16);
            lds_inc(C_BDONE, lane);
        }
        STAMP(11);
    } else {
        __builtin_amdgcn_s_setprio(1);
        {
            unsigned ph[18];
            features(acc0, acc2[0], acc2[1], acc2[2], acc2[3], lane, ph);
            const unsigned pa = L_PHIK + (32 * tb + n) * 160 + hi * 80;
#pragma unroll
            for (int u = 0; u < 4; ++u) LW(u32x4, pa + 16 * u) = (u32x4){ph[4 * u], ph[4 * u + 1], ph[4 * u + 2], ph[4 * u + 3]};
            LW(u32x2, pa + 64) = (u32x2){ph[16], ph[17]};
            LW(unsigned, vrow + 24) = cvtpk(acc2[8], acc2[9]); LW(unsigned, vrow + 28) = cvtpk(acc2[10], acc2[11]);
            LW(unsigned, vrow + 32) = cvtpk(acc2[12], acc2[13]); LW(unsigned, vrow + 36) = cvtpk(acc2[14], acc2[15]);
            LW(unsigned, vrow + 36 + 4 * hi) = 0x00003f80u;
            asm volatile("s_waitcnt lgkmcnt(0)" ::: "memory");
            lds_inc(C_PHI, lane);
        }
#pragma unroll 1
        for (int i = 0; i < 19; ++i) { const int p = 74 + tb + 4 * i < 147 ? 74 + tb + 4 * i : 147; dma_s_sc1(p * 1024, ws + WS_KVIMG + p * 1024, lane * 16); }
        const float gate = __uint_as_float(__builtin_amdgcn_raw_buffer_load_b32(rws, WS_G + (128 * ti + 32 * tb + n) * 4, 0, 16));
        STAMP(7);
        lds_wait<2>(C_PHI, 8, ws);
        __builtin_amdgcn_s_setprio(2);
        unsigned gen = 0;
        {
            const int hh = tb & 1, i = lane & 15;
            const int tok = 8 * g4 + (i >> 2), gr = i & 3;
            const unsigned va = L_VB + tok * 112 + hh * 56 + 8 + gr * 8;
            const int ntask = tb < 2 ? 2 : 1;
#pragma unroll 1
            for (int t = 0; t < ntask; ++t) {
                const int mbf = tb < 2 ? 2 * t : 1;
                const unsigned pa = L_PHIK + tok * 160 + hh * 80 + mbf * 32 + gr * 8;
                f32x4 d0 = {}, d1 = {};
#pragma unroll
                for (int ks = 0; ks < 4; ++ks) {
                    const u32x4 a = trd2(pa + ks * 32 * 160, pa + ks * 32 * 160 + 4 * 160);
                    const u32x4 b0 = trd2(va + ks * 32 * 112, va + ks * 32 * 112 + 4 * 112);
                    const u32x4 b1 = trd2(va + ks * 32 * 112 + 32, va + ks * 32 * 112 + 4 * 112 + 32);
                    d0 = MFMA16(a, b0, d0); d1 = MFMA16(a, b1, d1);
                }
                const unsigned ko = WS_KVP + (unsigned)(16 * tj + ti) * 5472 + hh * 2736 + (16 * mbf + 4 * g4) * 4;
                if (mbf < 2 || g4 == 0) {
                    __builtin_amdgcn_raw_buffer_store_b128(__builtin_bit_cast(u32x4, d0), rws, ko + n16 * 144, 0, 16);
                    if (n16 < 3) __builtin_amdgcn_raw_buffer_store_b128(__builtin_bit_cast(u32x4, d1), rws, ko + (16 + n16) * 144, 0, 16);
                }
            }
        }
        WAITV(0);
        lds_inc(C_LAND1, lane);
        async_bar(gen, lane, ws);
        if (tb == 0 && lane == 0) put_flag(ws, WS_FKVP + (16 * tj + ti) * 8, tag);
        STAMP(8);
        if (tb == 3) {
            const unsigned l0 = b * 21 + lane % 21;
            u64 p2 = (u64)P.woA, p3 = (u64)P.woB;
            asm volatile("" : "+s"(p2), "+s"(p3));
            const u64 ad = (lane < 21 ? p2 : p3) + (l0 < 5183 ? l0 : 5183) * 128;
            asm volatile("global_load_dword %0, %1, off" : "=&v"(pf) : "v"(ad) : "memory");
        }
        for (int p = tb; p < 37; p += 4) dma_s_sc1(p * 1024, ws + WS_KVIMG + p * 1024, lane * 16);
        unsigned pq[18];
        features(acc1, acc2[4], acc2[5], acc2[6], acc2[7], lane, pq);
        WAITV(0);
        PIN(pf);
        lds_inc(C_LAND2, lane);
        poll_flags(ws, WS_FKVP + 16 * tj * 8, 16, tag, lane, 1);
        STAMP(9);
        {
            const int at = tb * 64 + lane;
#pragma unroll 1
            for (int rd = 0; rd < 2; ++rd) {
                const int it = rd == 0 ? at : 256 + tb * 22 + lane;
                const bool ok = rd == 0 || (lane < 22 && it < 342);
                const unsigned ko = WS_KVP + (unsigned)(16 * tj) * 5472 + (ok ? it : 0) * 16;
                f32x4 v[16];
#pragma unroll
                for (int i = 0; i < 16; ++i) v[i] = __builtin_bit_cast(f32x4, __builtin_amdgcn_raw_buffer_load_b128(rws, ko + i * 5472, 0, 16));
                f32x4 s = v[0];
#pragma unroll
                for (int i = 1; i < 16; ++i) s += v[i];
                const int hh = it >= 171, rem = it - 171 * hh, d = rem / 9, fq = rem - 9 * d;
                if (ok) LW(u32x2, L_KVT + hh * 1600 + d * 80 + fq * 8) = (u32x2){cvtpk(s[0], s[1]), cvtpk(s[2], s[3])};
            }
            if (tb == 3 && lane < 10) LW(u32x4, L_KVT + (lane >= 5) * 1600 + 19 * 80 + (lane % 5) * 16) = (u32x4){0u, 0u, 0u, 0u};
        }
        async_bar(gen, lane, ws);
        {
            const int hm = (n >> 2) & 1, d0 = 4 * (n >> 3) + (n & 3);
            const unsigned a0 = L_KVT + (hm == hi ? hm * 1600 + d0 * 80 : 19 * 80);
            const unsigned a1 = L_KVT + (hm == hi && d0 < 3 ? hm * 1600 + (16 + d0) * 80 : 19 * 80);
            f32x16 N0 = {}, N1 = {};
#pragma unroll
            for (int s = 0; s < 4; ++s) {
                const u32x4 bq = {pq[4 * s], pq[4 * s + 1], pq[4 * s + 2], pq[4 * s + 3]};
                N0 = MFMA(LA(u32x4, a0 + 16 * s), bq, N0); N1 = MFMA(LA(u32x4, a1 + 16 * s), bq, N1);
            }
            const u32x2 br = {pq[16], pq[17]};
            N0 = MFMA8(LA(u32x2, a0 + 64), br, N0); N1 = MFMA8(LA(u32x2, a1 + 64), br, N1);
            const float sc = gate / (N1[2] + 1e-6f);
            const unsigned fo = WS_OAF + (unsigned)(4 * ti + tb) * 36 * 1024;
            const u32x4 f0 = {cvtpk(N0[0] * sc, N0[1] * sc), cvtpk(N0[2] * sc, N0[3] * sc), cvtpk(N0[4] * sc, N0[5] * sc), cvtpk(N0[6] * sc, N0[7] * sc)};
            const u32x4 f1 = {cvtpk(N0[8] * sc, N0[9] * sc), cvtpk(N0[10] * sc, N0[11] * sc), cvtpk(N0[12] * sc, N0[13] * sc), cvtpk(N0[14] * sc, N0[15] * sc)};
            __builtin_amdgcn_raw_buffer_store_b128(f0, rws, fo + (2 * tj) * 1024 + lane * 16, 0, 16);
            __builtin_amdgcn_raw_buffer_store_b128(f1, rws, fo + (2 * tj + 1) * 1024 + lane * 16, 0, 16);
            __builtin_amdgcn_raw_buffer_store_b32(cvtpk(N1[0] * sc, N1[1] * sc), rws, fo + (32 + (tj >> 2)) * 1024 + (((tj >> 1) & 1) * 32 + n) * 16 + (2 * (tj & 1) + hi) * 4, 0, 16);
        }
        WAITV(0);
        async_bar(gen, lane, ws);
        if (tb == 0 && lane == 0) put_flag(ws, WS_FOA + (16 * ti + tj) * 8, tag);
        STAMP(10);
        const int tt = 4 * x8 + (r & 3), cg = r >> 2;
        unsigned wo[9];
#pragma unroll
        for (int i = 0; i < 9; ++i) { const unsigned q = (tb + 4 * i) * 64 + lane, row = q / 9; wo[i] = row * 1152 + (q - row * 9) * 16; }
        auto stage = [&](const char* w) {
            const char* wsl = w + cg * 144;
#pragma unroll
            for (int i = 0; i < 21; ++i) {
                const int p = tb + 4 * i;
                if (i < 20 || tb == 0) dma_s(L_WOS + p * 1024, wsl + (i / 9) * 256 * 1152, wo[i % 9]);
            }
        };
        const int i16 = lane & 15, gr = i16 & 3, hk = g4 & 1, fh = g4 >> 1;
        const unsigned fro = (unsigned)(2 * tt + (tb >> 1)) * 36 * 1024 + fh * 1024 + (hk * 32 + n16 + 16 * (tb & 1)) * 16;
        const unsigned remo = L_WOS + (18 * (8 * fh + 4 * hk + (i16 >> 3)) + 16 + ((i16 >> 2) & 1)) * 144 + gr * 8;
        f32x4 T[5] = {};
        auto wfrag = [&](int ks, int cb, unsigned maino, unsigned r4s) {
            const unsigned a = (ks < 16 ? maino + ks * 36 * 144 : remo + (ks - 16) * 16 * 18 * 144) + cb * 32;
            return trd2(a, a + (ks < 16 ? r4s : 2 * 18 * 144));
        };
        auto gemm = [&](const u32x4 (&bf)[18], unsigned maino, unsigned r4s, auto ord) {
            u32x4 wf[2][5];
#pragma unroll
            for (int cb = 0; cb < 5; ++cb) wf[0][cb] = wfrag(ord(0), cb, maino, r4s);
            SBAR();
#pragma unroll
            for (int q = 0; q < 18; ++q) {
#pragma unroll
                for (int cb = 0; cb < 5; ++cb) {
                    T[cb] = MFMA16(wf[q & 1][cb], bf[q], T[cb]); SBAR();
                    if (q < 17) wf[(q + 1) & 1][cb] = wfrag(ord(q + 1), cb, maino, r4s);
                    SBAR();
                }
            }
        };
        u32x4 bf[18];
        poll_flags(ws, WS_FOA + 16 * (tt >> 1) * 8, 16, tag, lane, 1);
#pragma unroll
        for (int ks = 0; ks < 18; ++ks) bf[ks] = __builtin_bit_cast(u32x4, __builtin_amdgcn_raw_buffer_load_b128(rws, WS_OAF + fro + ks * 2048, 0, 16));
        SBAR();
        lds_wait<16>(C_BPROG, 4, ws);
        stage(P.woA);
        WAITV(0);
        async_bar(gen, lane, ws);
        STAMP(11);
        gemm(bf, L_WOS + (18 * hk + 8 * fh + (i16 >> 2)) * 144 + gr * 8, 4 * 144, [](int q) { return q; });
        async_bar(gen, lane, ws);
        STAMP(12);
        stage(P.woB);
        WAITV(0);
        async_bar(gen, lane, ws);
        STAMP(13);
        {
            const unsigned maino = L_WOS + (18 * fh + 4 * hk + (i16 >> 2)) * 144 + gr * 8;
            const unsigned remg = L_WOS + (36 * g4 + 16 + 18 * (i16 >> 3) + ((i16 >> 2) & 1)) * 144 + gr * 8;
            const unsigned rro = WS_OBF + (unsigned)(2 * tt + (tb >> 1)) * 36 * 1024 + 32 * 1024 + ((g4 >> 1) * 32 + 16 * (tb & 1) + n16) * 16 + (g4 & 1) * 8;
            f32x4 G[4][5];
#pragma unroll
            for (int cb = 0; cb < 5; ++cb) { G[0][cb] = T[cb]; G[1][cb] = (f32x4){}; G[2][cb] = (f32x4){}; G[3][cb] = (f32x4){}; }
            u32x2 br[4];
            unsigned have = 0, done = 0;
            bool bdone = false;
#pragma unroll 1
            for (unsigned s = 0;; ++s) {
#pragma unroll
                for (int g = 0; g < 4; ++g) {
                    if (!((have >> g) & 1)) {
#pragma unroll
                        for (int u = 0; u < 4; ++u) bf[4 * g + u] = __builtin_bit_cast(u32x4, __builtin_amdgcn_raw_buffer_load_b128(rws, WS_OBF + fro + (4 * g + u) * 2048, 0, 16));
                        br[g] = __builtin_bit_cast(u32x2, __builtin_amdgcn_raw_buffer_load_b64(rws, rro + g * 1024, 0, 16));
                    }
                }
#pragma unroll
                for (int g = 0; g < 4; ++g) {
                    if (!((have >> g) & 1)) {
                        unsigned m = br[g][0] > br[g][1] ? br[g][0] : br[g][1];
#pragma unroll
                        for (int u = 0; u < 4; ++u) {
                            const u32x4 w = bf[4 * g + u];
                            const unsigned m0 = w[0] > w[1] ? w[0] : w[1], m1 = w[2] > w[3] ? w[2] : w[3];
                            m = m > m0 ? m : m0; m = m > m1 ? m : m1;
                        }
                        if (__all(m != POISON)) have |= 1u << g;
                    }
                }
                if (!bdone) { bdone = uni(__hip_atomic_load((AS3 unsigned*)(uintptr_t)C_BDONE, __ATOMIC_RELAXED, __HIP_MEMORY_SCOPE_WORKGROUP)) >= 4; if (bdone) STAMP(14); }
                if (bdone) {
#pragma unroll
                    for (int g = 0; g < 4; ++g) {
                        if (((have & ~done) >> g) & 1) {
                            u32x4 wf[2][5]; u32x2 wr[5];
#pragma unroll
                            for (int cb = 0; cb < 5; ++cb) wf[0][cb] = wfrag(4 * g, cb, maino, 8 * 144);
                            SBAR();
#pragma unroll
                            for (int u = 0; u < 4; ++u) {
#pragma unroll
                                for (int cb = 0; cb < 5; ++cb) {
                                    G[g][cb] = MFMA16(wf[u & 1][cb], bf[4 * g + u], G[g][cb]); SBAR();
                                    if (u < 3) wf[(u + 1) & 1][cb] = wfrag(4 * g + u + 1, cb, maino, 8 * 144);
                                    else wr[cb] = trd(remg + g * 144 * 144 + cb * 32);
                                    SBAR();
                                }
                            }
#pragma unroll
                            for (int cb = 0; cb < 5; ++cb) G[g][cb] = MFMA1616(wr[cb], br[g], G[g][cb]);
                            done |= 1u << g;
                        }
                    }
                    if (done == 15) {
                        break;
                    }
                }
                if (s > SPIN_MAX) { if (lane == 0) __hip_atomic_store((gu32*)(ws + WS_TMO), 0x300u, __ATOMIC_RELAXED, __HIP_MEMORY_SCOPE_AGENT); break; }
                if (bdone) __builtin_amdgcn_s_sleep(1); else __builtin_amdgcn_s_sleep(16);
            }
#pragma unroll
            for (int cb = 0; cb < 5; ++cb) T[cb] = (G[0][cb] + G[1][cb]) + (G[2][cb] + G[3][cb]);
        }
        STAMP(19);
#define OUTAUX 0
        const __amdgpu_buffer_rsrc_t rout = __builtin_amdgcn_make_buffer_rsrc((void*)P.out, (short)0, 2048 * 1152, 0x00020000);
        const unsigned op = (unsigned)(64 * tt + 16 * tb + n16) * 1152 + cg * 144 + g4 * 8;
#pragma unroll
        for (int cb = 0; cb < 5; ++cb) {
            const u32x2 o = {cvtpk(T[cb][0], T[cb][1]), cvtpk(T[cb][2], T[cb][3])};
            if (cb < 4 || g4 < 2) __builtin_amdgcn_raw_buffer_store_b64(o, rout, op + cb * 32, 0, OUTAUX);
        }
    }
}

__global__ __launch_bounds__(512, 2) void gated_mix_kernel(const void* x, const void* wqkv, const void* wphi, const void* woA, const void* wq, const void* wk,
                                                           const void* wv, const void* woB, const void* gate, void* out, void* ws) {
    extern __shared__ __attribute__((aligned(16))) char lds[];
    const int lane = threadIdx.x & 63, wid = uni(threadIdx.x >> 6);
    const Ptrs P = {(const char*)x, (const char*)wqkv, (const char*)wphi, (const char*)woA, (const char*)wq, (const char*)wk, (const char*)wv, (const char*)woB, (const char*)gate, (char*)out, (char*)ws};
    if (wid < 4) role_main<true>(P, wid, lane); else role_main<false>(P, wid, lane);
}
}

extern "C" void kernel_launch(void* const* d_in, const int* in_sizes, int n_in, void* d_out, int out_size, void* d_ws, size_t ws_size, hipStream_t stream) {
    (void)in_sizes; (void)n_in; (void)out_size; (void)ws_size;
    (void)hipFuncSetAttribute((const void*)fk::gated_mix_kernel, hipFuncAttributeMaxDynamicSharedMemorySize, fk::LDS_BYTES);
    hipLaunchKernelGGL(fk::gated_mix_kernel, dim3(256), dim3(512), fk::LDS_BYTES, stream,
                       (const void*)d_in[0], (const void*)d_in[1], (const void*)d_in[2], (const void*)d_in[3], (const void*)d_in[5], (const void*)d_in[6],
                       (const void*)d_in[7], (const void*)d_in[8], (const void*)d_in[9], d_out, d_ws);
}
```

```cpp
#include <hip/hip_runtime.h>
#include <hip/hip_bf16.h>
#include <stdint.h>

namespace fk {
typedef __bf16 bf16x2 __attribute__((ext_vector_type(2)));
typedef __bf16 bf16x8 __attribute__((ext_vector_type(8)));
typedef short s16x4 __attribute__((ext_vector_type(4)));
typedef float f32x2 __attribute__((ext_vector_type(2)));
typedef float f32x4 __attribute__((ext_vector_type(4)));
typedef float f32x16 __attribute__((ext_vector_type(16)));
typedef unsigned u32x2 __attribute__((ext_vector_type(2)));
typedef unsigned u32x4 __attribute__((ext_vector_type(4)));
typedef unsigned long long u64;
typedef __attribute__((address_space(1))) u64 gu64;
typedef __attribute__((address_space(1))) unsigned gu32;

#define AS3 __attribute__((address_space(3)))
#define SBAR() __builtin_amdgcn_sched_barrier(0)
#define PIN(x) asm volatile("" : "+v"(x))
#define MFMA(a, b, c) __builtin_amdgcn_mfma_f32_32x32x16_bf16(__builtin_bit_cast(bf16x8, a), __builtin_bit_cast(bf16x8, b), c, 0, 0, 0)
#define MFMA8(a, b, c) __builtin_amdgcn_mfma_f32_32x32x8bf16_1k(__builtin_bit_cast(s16x4, a), __builtin_bit_cast(s16x4, b), c, 0, 0, 0)
#define MFMA1616(a, b, c) __builtin_amdgcn_mfma_f32_16x16x16bf16_1k(__builtin_bit_cast(s16x4, a), __builtin_bit_cast(s16x4, b), c, 0, 0, 0)
#define MFMA16(a, b, c) __builtin_amdgcn_mfma_f32_16x16x32_bf16(__builtin_bit_cast(bf16x8, a), __builtin_bit_cast(bf16x8, b), c, 0, 0, 0)
#define LA(T, a) (*(const AS3 T*)(uintptr_t)(a))
#define LW(T, a) (*(AS3 T*)(uintptr_t)(a))
#define PFV 0
#define APRIO 3
#define FENCE() asm volatile("" ::: "memory")
#define EX(x) __builtin_amdgcn_exp2f(x)

#define WS_TMO 64
constexpr unsigned WS_FKV = 4096, WS_FKVP = 8192, WS_FOA = 12288, WS_FOB = 16384;
#define WS_G 32768
#define WS_KMAX 40960
#define WS_KVIMG 65536
#define WS_KVP 262144
#define WS_OAF (2 << 20)
#define WS_OBF (5 << 20)

constexpr unsigned SLOT = 36864, WOFF = 16384;
constexpr unsigned L_WKV = 110592;
constexpr unsigned L_XR = 152064, L_RED = 152064;
constexpr unsigned L_CNT = 163792, L_WPR = 157184, L_WPQ = 158480;
constexpr unsigned L_WPHI = 152064, L_KVT = 157184;
constexpr unsigned L_PHIK = 0, L_VB = 20480;
constexpr unsigned L_KMAX = 161152, L_WOS = 37888;
constexpr unsigned KB_BYTES = 2240, STEP_BYTES = 9472, KR_OFF = 8960;
constexpr unsigned LDS_BYTES = 163840;
constexpr unsigned C_LAND0 = L_CNT, C_LAND1 = L_CNT + 4, C_LAND2 = L_CNT + 8, C_PHI = L_CNT + 12, C_ASY = L_CNT + 16, C_BPROG = L_CNT + 20, C_BDONE = L_CNT + 24, C_RAW = L_CNT + 28, C_WKVF = L_CNT + 32;
constexpr unsigned POISON = 0xffffffffu;

#define STAMP(k) do { } while (0)

__device__ __forceinline__ unsigned cvtpk(float lo, float hi) { f32x2 v = {lo, hi}; return __builtin_bit_cast(unsigned, __builtin_convertvector(v, bf16x2)); }
__device__ __forceinline__ int uni(int v) { return __builtin_amdgcn_readfirstlane(v); }

__device__ __forceinline__ void dma_s(unsigned lds, const void* sbase, unsigned voff) {
    asm volatile("s_mov_b32 m0, %0\n\ts_nop 0\n\tglobal_load_lds_dwordx4 %1, %2" :: "s"(lds), "v"(voff), "s"(sbase) : "memory", "m0");
}
__device__ __forceinline__ void dma_s_sc1(unsigned lds, const void* sbase, unsigned voff) {
    asm volatile("s_mov_b32 m0, %0\n\ts_nop 0\n\tglobal_load_lds_dwordx4 %1, %2 sc1" :: "s"(lds), "v"(voff), "s"(sbase) : "memory", "m0");
}
__device__ __forceinline__ void dma_v(unsigned lds, const void* vaddr) {
    asm volatile("s_mov_b32 m0, %0\n\ts_nop 0\n\tglobal_load_lds_dwordx4 %1, off" :: "s"(lds), "v"(vaddr) : "memory", "m0");
}
#define WAITV(n) asm volatile("s_waitcnt vmcnt(" #n ")" ::: "memory")
#define BAR_L() asm volatile("s_waitcnt lgkmcnt(0)\n\ts_barrier" ::: "memory")
template <int N> __device__ __forceinline__ void waitv() { asm volatile("s_waitcnt vmcnt(%0)" :: "n"(N) : "memory"); }
template <int N> __device__ __forceinline__ void bar_vl() { asm volatile("s_waitcnt vmcnt(%0) lgkmcnt(0)\n\ts_barrier" :: "n"(N) : "memory"); }

__device__ __forceinline__ u32x2 trd(unsigned a) { return __builtin_bit_cast(u32x2, __builtin_amdgcn_ds_read_tr16_b64_v4i16((AS3 s16x4*)(uintptr_t)a)); }
__device__ __forceinline__ u32x4 trd2(unsigned a0, unsigned a1) { const u32x2 p = trd(a0), q = trd(a1); return (u32x4){p[0], p[1], q[0], q[1]}; }

template <int CTRL> __device__ __forceinline__ float dppf(float v) { return __builtin_bit_cast(float, __builtin_amdgcn_update_dpp(0, __builtin_bit_cast(int, v), CTRL, 0xf, 0xf, true)); }
__device__ __forceinline__ float wave_max(float v) {
    v = __builtin_fmaxf(v, dppf<0x121>(v)); v = __builtin_fmaxf(v, dppf<0x122>(v)); v = __builtin_fmaxf(v, dppf<0x124>(v)); v = __builtin_fmaxf(v, dppf<0x128>(v));
    { const auto t = __builtin_amdgcn_permlane16_swap(__float_as_uint(v), __float_as_uint(v), false, false); v = __builtin_fmaxf(__uint_as_float(t[0]), __uint_as_float(t[1])); }
    { const auto t = __builtin_amdgcn_permlane32_swap(__float_as_uint(v), __float_as_uint(v), false, false); v = __builtin_fmaxf(__uint_as_float(t[0]), __uint_as_float(t[1])); }
    return v;
}
__device__ __forceinline__ u64 mix64(u64 z) { z += 0x9E3779B97F4A7C15ull; z = (z ^ (z >> 30)) * 0xBF58476D1CE4E5B9ull; z = (z ^ (z >> 27)) * 0x94D049BB133111EBull; return z ^ (z >> 31); }

constexpr unsigned SPIN_MAX = 1u << 20;
__device__ __forceinline__ void poll_flags(const char* ws, unsigned off, int n, u64 tag, int lane, int slp) {
    const gu64* p = (const gu64*)(ws + off) + (lane < n ? lane : 0);
    for (unsigned s = 0;; ++s) {
        const u64 v = __hip_atomic_load(p, __ATOMIC_RELAXED, __HIP_MEMORY_SCOPE_AGENT);
        if (__all(v == tag)) break;
        if (s > SPIN_MAX) { if (lane == 0) __hip_atomic_store((gu32*)(ws + WS_TMO), 0x100u + off, __ATOMIC_RELAXED, __HIP_MEMORY_SCOPE_AGENT); break; }
        if (slp) __builtin_amdgcn_s_sleep(8); else __builtin_amdgcn_s_sleep(1);
    }
    FENCE();
}
__device__ __forceinline__ void poll4(const char* ws, u64 tag, int lane) {
    const gu64* p = (const gu64*)(ws + WS_FKV) + lane;
    for (unsigned s = 0;; ++s) {
        const u64 v0 = __hip_atomic_load(p, __ATOMIC_RELAXED, __HIP_MEMORY_SCOPE_AGENT), v1 = __hip_atomic_load(p + 64, __ATOMIC_RELAXED, __HIP_MEMORY_SCOPE_AGENT);
        const u64 v2 = __hip_atomic_load(p + 128, __ATOMIC_RELAXED, __HIP_MEMORY_SCOPE_AGENT), v3 = __hip_atomic_load(p + 192, __ATOMIC_RELAXED, __HIP_MEMORY_SCOPE_AGENT);
        if (__all(v0 == tag && v1 == tag && v2 == tag && v3 == tag)) break;
        if (s > SPIN_MAX) { if (lane == 0) __hip_atomic_store((gu32*)(ws + WS_TMO), 0x100u, __ATOMIC_RELAXED, __HIP_MEMORY_SCOPE_AGENT); break; }
        __builtin_amdgcn_s_sleep(1);
    }
    FENCE();
}
__device__ __forceinline__ void put_flag(char* ws, unsigned off, u64 tag) { __hip_atomic_store((gu64*)(ws + off), tag, __ATOMIC_RELAXED, __HIP_MEMORY_SCOPE_AGENT); }
__device__ __forceinline__ void lds_inc(unsigned a, int lane) { if (lane == 0) __hip_atomic_fetch_add((AS3 unsigned*)(uintptr_t)a, 1u, __ATOMIC_RELAXED, __HIP_MEMORY_SCOPE_WORKGROUP); }
template <int SLP> __device__ __forceinline__ void lds_wait(unsigned a, unsigned target, const char* ws) {
    for (unsigned s = 0;; ++s) {
        const unsigned v = uni(__hip_atomic_load((AS3 unsigned*)(uintptr_t)a, __ATOMIC_RELAXED, __HIP_MEMORY_SCOPE_WORKGROUP));
        if (v >= target) break;
        if (s > SPIN_MAX) { __hip_atomic_store((gu32*)(ws + WS_TMO), 0x10u + (a & 15), __ATOMIC_RELAXED, __HIP_MEMORY_SCOPE_AGENT); break; }
        __builtin_amdgcn_s_sleep(SLP);
    }
    FENCE();
}
__device__ __forceinline__ void async_bar(unsigned& gen, int lane, const char* ws) {
    asm volatile("s_waitcnt lgkmcnt(0)" ::: "memory");
    gen += 4; lds_inc(C_ASY, lane); lds_wait<2>(C_ASY, gen, ws);
}

__device__ __forceinline__ void features(const f32x16& a0, float p16, float p17, float p18, float p19, int lane, unsigned (&out)[18]) {
    const u32x4 b0 = {cvtpk(a0[0], a0[1]), cvtpk(a0[2], a0[3]), cvtpk(a0[4], a0[5]), cvtpk(a0[6], a0[7])};
    const u32x4 b1 = {cvtpk(a0[8], a0[9]), cvtpk(a0[10], a0[11]), cvtpk(a0[12], a0[13]), cvtpk(a0[14], a0[15])};
    const u32x2 b2 = {cvtpk(p16, p17), cvtpk(p18, p19)};
    const unsigned wa = L_WPHI + lane * 16, wr = L_WPHI + 2048 + lane * 8;
    f32x16 P0 = {}, P1 = {};
    P0 = MFMA(LA(u32x4, wa), b0, P0); P1 = MFMA(LA(u32x4, wa + 2560), b0, P1);
    P0 = MFMA(LA(u32x4, wa + 1024), b1, P0); P1 = MFMA(LA(u32x4, wa + 2560 + 1024), b1, P1);
    P0 = MFMA8(LA(u32x2, wr), b2, P0); P1 = MFMA8(LA(u32x2, wr + 2560), b2, P1);
    float p[18];
#pragma unroll
    for (int i = 0; i < 16; ++i) p[i] = P0[i];
    p[16] = P1[0]; p[17] = P1[1];
    float mx = p[0], mn = p[0];
#pragma unroll
    for (int i = 1; i < 18; ++i) { mx = __builtin_fmaxf(mx, p[i]); mn = __builtin_fminf(mn, p[i]); }
    const float L = 1.4426950408889634f, mxl = -mx * L, mnl = mn * L;
    float ep[18], en[18], sp = 0.f, sn = 0.f;
#pragma unroll
    for (int i = 0; i < 18; ++i) { ep[i] = EX(__builtin_fmaf(p[i], L, mxl)); en[i] = EX(__builtin_fmaf(p[i], -L, mnl)); sp += ep[i]; sn += en[i]; }
    const float rp = __builtin_amdgcn_rcpf(sp), rn = __builtin_amdgcn_rcpf(sn);
#pragma unroll
    for (int u = 0; u < 9; ++u) { out[u] = cvtpk(ep[2 * u] * rp, ep[2 * u + 1] * rp); out[9 + u] = cvtpk(en[2 * u] * rn, en[2 * u + 1] * rn); }
}

struct Ptrs { const char *x, *wqkv, *wphi, *woA, *wq, *wk, *wv, *woB, *gate; char *out, *ws; };

template <bool ISB>
__device__ __forceinline__ void role_main(const Ptrs& P, const int wid, const int lane) {
    const int b = blockIdx.x, x8 = b & 7, r = b >> 3;
    const int ti = 8 * (x8 & 1) + (r & 7), tj = 4 * (x8 >> 1) + (r >> 3);
    const int tq = tj ^ 1;
    const int tb = wid & 3, n = lane & 31, hi = lane >> 5, n16 = lane & 15, g4 = lane >> 4;
    const int tid = wid * 64 + lane;
    char* const ws = P.ws;
    const __amdgpu_buffer_rsrc_t rws = __builtin_amdgcn_make_buffer_rsrc((void*)ws, (short)0, 0x1000000, 0x00020000);
    STAMP(0);
    if (tid == 0) { LW(u32x4, L_CNT) = (u32x4){0u, 0u, 0u, 0u}; LW(u32x4, L_CNT + 16) = (u32x4){0u, 0u, 0u, 0u}; LW(u32x4, L_CNT + 32) = (u32x4){0u, 0u, 0u, 0u}; }
    BAR_L();
    u32x2 Ev;
    unsigned pf = 0;
    const unsigned obf = WS_OBF + (unsigned)(4 * ti + tb) * 36 * 1024, obm = obf + 2 * tq * 1024 + lane * 16;
    const unsigned obr = obf + (32 + (tq >> 2)) * 1024 + (((tq >> 1) & 1) * 32 + n) * 16 + (tq & 1) * 8;
    auto touch_w = [&](int s) {
        const unsigned l = lane < 36 ? lane : 35, cc = l / 12, ty = (l - 12 * cc) / 3, ln = l - 12 * cc - 3 * ty;
        const unsigned row = 64 * (3 * s + cc) + 32 * (x8 & 1) + r;
        u64 p0 = (u64)P.wqkv, p1 = (u64)P.wq;
        asm volatile("" : "+s"(p0), "+s"(p1));
        const u64 ad = (ty < 3 ? p0 + (row * 3456 + ty * 1152) : p1 + row * 1152) + (2 * (x8 >> 1) + ln) * 128;
        asm volatile("global_load_dword %0, %1, off" : "=&v"(pf) : "v"(ad) : "memory");
    };
    unsigned xo[4]; u64 wb[5]; unsigned wst[5];
    auto dma1 = [&](int c, int i) {
        const unsigned slot = (c & 3) * SLOT;
        if (i < 4) dma_s(slot + (4 * tb + i) * 1024, P.x + c * 128, xo[i]);
        else { dma_v(slot + WOFF + (5 * tb + i - 4) * 1024, (const char*)wb[i - 4]); wb[i - 4] += wst[i - 4]; }
    };
    auto issue = [&](int c) {
#pragma unroll
        for (int i = 0; i < 9; ++i) dma1(c, i);
    };
#pragma unroll
    for (int i = 0; i < 4; ++i) xo[i] = (unsigned)(128 * ti + 32 * tb + 8 * i + (lane >> 3)) * 1152u + (((lane & 7) ^ (4 * (i & 1) + (lane >> 4))) * 16);
    if (!ISB) {
        u32x4 xf[5], gf[5];
        __builtin_amdgcn_s_setprio(APRIO);
        const unsigned go = (32 * tb + 8 * g4) * 2, g4o = tb < 2 ? go + 1024 : go;
        {
            const unsigned zo = 0;
            asm volatile("s_mov_b32 exec_lo, 1\n\ts_mov_b32 exec_hi, 0\n\tglobal_load_dwordx2 %0, %6, %9 sc1\n\t"
                         "s_mov_b32 exec_lo, 0x00100010\n\ts_mov_b32 exec_hi, 0x00100010\n\t"
                         "global_load_dwordx4 %1, %7, %10\n\tglobal_load_dwordx4 %2, %7, %10 offset:256\n\tglobal_load_dwordx4 %3, %7, %10 offset:512\n\t"
                         "global_load_dwordx4 %4, %7, %10 offset:768\n\tglobal_load_dwordx4 %5, %8, %10\n\t"
                         "s_mov_b32 exec_lo, -1\n\ts_mov_b32 exec_hi, -1"
                         : "=&v"(Ev), "=&v"(gf[0]), "=&v"(gf[1]), "=&v"(gf[2]), "=&v"(gf[3]), "=&v"(gf[4])
                         : "v"(zo), "v"(go), "v"(g4o), "s"(ws), "s"(P.gate) : "memory");
        }
        {
            const char* xr = P.x + (unsigned)(128 * ti + 8 * tj) * 1152u;
            dma_s(L_XR + tb * 1024, xr + tb * 1024, lane * 16); dma_s(L_XR + (4 + tb) * 1024, xr + (4 + tb) * 1024, lane * 16);
            if (tb == 3) dma_s(L_XR + 8 * 1024, xr + 8 * 1024, lane * 16);
        }
#pragma unroll
        for (int t = 0; t < 10; ++t) {
            const int p = tb + 4 * t;
            if (t != 5 || tb != 0) dma_s(L_WKV + p * 1024, p < 20 ? P.wk + p * 1024 : P.wv + (p * 1024 - 20736), lane * 16);
            else { if (lane < 16) dma_s(L_WKV + 20 * 1024, P.wk + 20 * 1024, lane * 16); else dma_s(L_WKV + 20 * 1024, P.wv - 256, lane * 16); }
        }
        if (tb == 1) { if (lane < 32) dma_s(L_WKV + 40 * 1024, P.wv + (40 * 1024 - 20736), lane * 16); }
        {
            const u32x4 pz = {POISON, POISON, POISON, POISON};
            __builtin_amdgcn_raw_buffer_store_b128(pz, rws, obm, 0, 16);
            __builtin_amdgcn_raw_buffer_store_b128(pz, rws, obm + 1024, 0, 16);
            __builtin_amdgcn_raw_buffer_store_b64((u32x2){POISON, POISON}, rws, obr, 0, 16);
        }
        STAMP(16);
        WAITV(3);
        asm volatile("" : "+v"(Ev), "+v"(gf[0]), "+v"(gf[1]), "+v"(gf[2]), "+v"(gf[3]), "+v"(gf[4]) :: "memory");
        lds_inc(C_RAW, lane); lds_wait<1>(C_RAW, 4, ws);
        STAMP(1);
        if (tb < 2) touch_w(1 + tb);
#pragma unroll
        for (int s = 0; s < 5; ++s) xf[s] = LA(u32x4, L_XR + (n16 & 7) * 1152 + (s < 4 ? go + 256 * s : g4o));
        const int i = lane & 15;
        const unsigned rowa = L_WKV + (32 * tb + 8 * g4 + 2 * (i >> 2)) * 36;
        const unsigned am = rowa + 8 * (i & 3), ar = rowa + (i & 1) * 20736 + 32;
        u32x2 we0[5], we1[5], wr[5], wo0[5], wo1[5];
#pragma unroll
        for (int s = 0; s < 5; ++s) {
            const unsigned o = (s < 4 || tb < 2 ? s : 0) * 128 * 36;
            we0[s] = trd(am + o); we1[s] = trd(am + o + 20736); wr[s] = trd(ar + o); wo0[s] = trd(am + o + 40); wo1[s] = trd(am + o + 40 + 20736);
        }
        SBAR();
#pragma unroll
        for (int s = 0; s < 5; ++s) { PIN(we0[s]); PIN(we1[s]); PIN(wr[s]); PIN(wo0[s]); PIN(wo1[s]); PIN(xf[s]); }
        lds_inc(C_WKVF, lane);
        f32x4 E0 = {}, E1 = {}, E2 = {}, O0 = {}, O1 = {}, O2 = {}, G = {};
#pragma unroll
        for (int s = 0; s < 5; ++s) {
            if (s < 4 || tb < 2) {
                const u32x2 be = {__builtin_amdgcn_perm(xf[s][1], xf[s][0], 0x05040100u), __builtin_amdgcn_perm(xf[s][3], xf[s][2], 0x05040100u)};
                const u32x2 bo = {__builtin_amdgcn_perm(xf[s][1], xf[s][0], 0x07060302u), __builtin_amdgcn_perm(xf[s][3], xf[s][2], 0x07060302u)};
                E0 = MFMA1616(we0[s], be, E0); E1 = MFMA1616(we1[s], be, E1); E2 = MFMA1616(wr[s], be, E2);
                O0 = MFMA1616(wo0[s], bo, O0); O1 = MFMA1616(wo1[s], bo, O1); O2 = MFMA1616(wr[s], bo, O2);
                G = MFMA16(gf[s], xf[s], G);
            }
        }
        const int ra_ = ((lane - 16) & 63) * 4;
        auto rot16 = [&](float v) { return __int_as_float(__builtin_amdgcn_ds_bpermute(ra_, __float_as_int(v))); };
        const float k0 = rot16(O0[2]), k1 = rot16(O0[3]), v0 = rot16(O1[2]), v1 = rot16(O1[3]), w0 = rot16(O2[2]), w1 = rot16(O2[3]);
        const bool g0 = g4 == 0, up = n16 >= 8;
        const f32x4 K = {E0[0] + (g0 ? O2[2] : k0), E0[1] + (g0 ? O2[3] : k1), E0[2] + O0[0], E0[3] + O0[1]};
        const f32x4 V = {E1[0] + (g0 ? w0 : v0), E1[1] + (g0 ? w1 : v1), E1[2] + O1[0], E1[3] + O1[1]};
        lds_wait<1>(C_WKVF, 4, ws);
        const unsigned ra = L_RED + tb * 1280 + (n16 & 7) * 160;
        LW(f32x4, ra + (up ? 64 : 0) + g4 * 16) = up ? V : K;
        const f32x2 rm = g0 ? (f32x2){E2[0] + k0, E2[1] + k1} : (g4 == 3 ? (f32x2){E2[0] + O1[2], E2[1] + O1[3]} : (f32x2){G[0], G[0]});
        if (!up && g4 != 2) LW(f32x2, ra + (g0 ? 128 : (g4 == 3 ? 136 : 144))) = rm;
        __builtin_amdgcn_s_setprio(0);
        STAMP(2);
    } else {
        { const unsigned zo = 0; asm volatile("s_mov_b32 exec_lo, 1\n\ts_mov_b32 exec_hi, 0\n\tglobal_load_dwordx2 %0, %1, %2 sc1\n\ts_mov_b32 exec_lo, -1\n\ts_mov_b32 exec_hi, -1" : "=&v"(Ev) : "v"(zo), "s"(ws) : "memory"); }
        if (tb == 0) touch_w(0);
        const int c0 = 72 * tj - 8 * (tj & 1), c0q = 72 * tq - 8 * (tq & 1);
#pragma unroll
        for (int i = 0; i < 5; ++i) {
            const unsigned q = (5 * tb + i) * 64 + lane, row = q / 20, wi = q - row * 20, ty = wi / 5, pc = wi - ty * 5;
            const bool isq = ty == 3;
            const unsigned rs = isq ? 1152u : 3456u, to = ty == 0 ? 1152u : (ty == 2 ? 2304u : 0u);
            wb[i] = (isq ? (u64)P.wq : (u64)P.wqkv) + (row * rs + to + ((ty & 1) ? c0q : c0) + pc * 16);
            wst[i] = rs * 64;
        }
        STAMP(17);
        issue(0);
        issue(1);
        issue(2);
        STAMP(18);
    }
    f32x16 acc0 = {}, acc1 = {}, acc2 = {};
    unsigned wa[2][3], xk[2][4];
    {
        const int i = lane & 15, sl = 2 * (g4 & 1) + ((i & 3) >> 1);
        const unsigned base = WOFF + (8 * (g4 >> 1) + (i >> 2)) * 320 + 32 * (i & 1), pk = 8 * (tj & 1), pq = 8 * (tq & 1);
        if (ISB) {
            wa[0][0] = base + 240 + 8 * sl + pq;
            wa[0][1] = base + (sl == 0 ? 240 + 32 + pq : 160 + 8 * (sl - 1) + pk);
            wa[0][2] = 0;
        } else {
            wa[0][0] = base + 8 * sl + pk;
            wa[0][1] = base + 80 + 8 * sl + pq;
            wa[0][2] = base + (sl == 0 ? 32 + pk : (sl == 1 ? 80 + 32 + pq : 160 + 24 + 8 * (sl - 2) + pk));
        }
        const unsigned xa = (32 * tb + n) * 128 + ((hi ^ ((n >> 1) & 7)) * 16);
#pragma unroll
        for (int k = 0; k < 4; ++k) { xk[0][k] = xa ^ (k * 32); xk[1][k] = xk[0][k] + 2 * SLOT; PIN(xk[0][k]); PIN(xk[1][k]); }
#pragma unroll
        for (int m = 0; m < (ISB ? 2 : 3); ++m) { wa[1][m] = wa[0][m] + 2 * SLOT; PIN(wa[0][m]); PIN(wa[1][m]); }
    }
#define WPHI_AT 2
    constexpr bool NODMA = false;
#define HM(acc, a, b) acc = MFMA(a, b, acc)
    constexpr bool HRD = true;
    u32x4 FX[4], FW0[4], FW1[4], FW2[4];
    auto rdx = [&](int c, int ks) { return LA(u32x4, xk[(c >> 1) & 1][ks] + (c & 1) * SLOT); };
    auto rdw = [&](int c, int ks, int m) { const unsigned a = wa[(c >> 1) & 1][m] + (c & 1) * SLOT + ks * 5120; return trd2(a, a + 1280); };
    if (!ISB) BAR_L();
    else asm volatile("s_waitcnt vmcnt(18)\n\ts_barrier" : "+v"(Ev), "+v"(pf) :: "memory");
    const u64 tag = mix64(((u64)(unsigned)uni(Ev[1]) << 32) | (unsigned)uni(Ev[0]));
    STAMP(3);
    if (!ISB) {
        if (tb == 2) {
            dma_s(L_WPR, P.wphi + tj * 1296, lane * 16); dma_s(L_WPQ, P.wphi + tq * 1296, lane * 16);
            if (lane < 17) { dma_s(L_WPR + 1024, P.wphi + tj * 1296 + 1024, lane * 16); dma_s(L_WPQ + 1024, P.wphi + tq * 1296 + 1024, lane * 16); }
        }
    }
    auto kv_format = [&]() {
        {
            const int t = lane & 7, bi = tj >> 2, pc = tj & 3;
            const int rho = 16 * (pc >> 1) + 8 * (t >> 2) + 4 * (pc & 1) + (t & 3);
            const int d = lane - 16;
            unsigned base, strd;
            if (lane < 16) { base = t * 40 + 8 * (lane >> 3); strd = 1; }
            else if (lane < 40) { base = d < 16 ? 16 + d : (d < 18 ? 18 + d : 0); strd = 40; }
            else if (lane < 48) { base = t * 40 + 32; strd = 1; }
            else { base = t * 40 + 36; strd = 0; }
            float v[8];
#pragma unroll
            for (int i = 0; i < 8; ++i) {
                const unsigned ad = L_RED + (base + i * strd) * 4;
                v[i] = (LA(float, ad) + LA(float, ad + 1280)) + (LA(float, ad + 2560) + LA(float, ad + 3840));
            }
            float ss = v[0] * v[0] + v[1] * v[1];
            float s6 = 0.f;
#pragma unroll
            for (int i = 2; i < 8; ++i) s6 += v[i] * v[i];
            if (lane < 16) ss += s6;
            u32x4 o = {cvtpk(v[0], v[1]), cvtpk(v[2], v[3]), cvtpk(v[4], v[5]), cvtpk(v[6], v[7])};
            if (lane == 34) o = (u32x4){0x3f803f80u, 0x3f803f80u, 0x3f803f80u, 0x3f803f80u};
            const unsigned img = WS_KVIMG + ti * STEP_BYTES;
            const unsigned o16 = lane < 16 ? img + bi * KB_BYTES + ((lane >> 3) * 32 + rho) * 16 : img + bi * KB_BYTES + 1024 + pc * 304 + d * 16;
            if (lane < 35) __builtin_amdgcn_raw_buffer_store_b128(o, rws, o16, 0, 16);
            float tot = __shfl(ss, t) + __shfl(ss, 8 + t) + __shfl(ss, 40 + t);
            tot = __builtin_fmaxf(tot, dppf<0xb1>(tot)); tot = __builtin_fmaxf(tot, dppf<0x4e>(tot)); tot = __builtin_fmaxf(tot, dppf<0x141>(tot));
            const float gv = __builtin_amdgcn_rcpf(1.f + EX(-1.4426950408889634f * v[0]));
            unsigned o4, d4;
            if (lane < 48) { o4 = img + KR_OFF + (bi >> 1) * 256 + rho * 8 + (bi & 1) * 4; d4 = o[0]; }
            else if (lane < 56) { o4 = WS_G + (128 * ti + 8 * tj + t) * 4; d4 = __float_as_uint(gv); }
            else { o4 = WS_KMAX + (16 * ti + tj) * 4; d4 = __float_as_uint(tot); }
            if (lane >= 40 && lane <= 56) __builtin_amdgcn_raw_buffer_store_b32(d4, rws, o4, 0, 16);
        }
    };
    auto build_wphi = [&](unsigned raw) {
            const int hm = (n >> 2) & 1;
            auto wval = [&](int mb, int pos) {
                const int f = 16 * mb + 4 * (n >> 3) + (n & 3), d = pos - 2 * hi;
                const bool ok = hm == hi && f < 18 && d >= 0 && d < 18;
                const unsigned e = LA(unsigned short, raw + (ok ? (hi * 324 + d * 18 + f) * 2 : 0));
                return ok ? e : 0u;
            };
            if (tb < 2) {
                const int mb = tb;
                unsigned v[16];
#pragma unroll
                for (int pos = 0; pos < 16; ++pos) v[pos] = wval(mb, pos);
                LW(u32x4, L_WPHI + mb * 2560 + lane * 16) = (u32x4){v[0] | (v[1] << 16), v[2] | (v[3] << 16), v[4] | (v[5] << 16), v[6] | (v[7] << 16)};
                LW(u32x4, L_WPHI + mb * 2560 + 1024 + lane * 16) = (u32x4){v[8] | (v[9] << 16), v[10] | (v[11] << 16), v[12] | (v[13] << 16), v[14] | (v[15] << 16)};
            } else {
#pragma unroll
                for (int mb = 0; mb < 2; ++mb)
                    LW(u32x2, L_WPHI + mb * 2560 + 2048 + lane * 8) = (u32x2){wval(mb, 16) | (wval(mb, 17) << 16), wval(mb, 18) | (wval(mb, 19) << 16)};
            }
    };
    SBAR();
#pragma unroll
    for (int ks = 0; ks < 2; ++ks) { FX[ks] = rdx(0, ks); FW0[ks] = rdw(0, ks, 0); FW1[ks] = rdw(0, ks, 1); if (!ISB) FW2[ks] = rdw(0, ks, 2); }
    u32x2 fl0, fl1, fl2, fl3;
    SBAR();
#pragma unroll
    for (int c = 0; c < 9; ++c) {
#pragma unroll
        for (int ks = 0; ks < 4; ++ks) {
            if (ks == 0 && c == 0 && ISB) { lds_wait<1>(C_WKVF, 4, ws); STAMP(28); }
            if (ks == 0 && c == 5 && !ISB) {
                if (wid == 7) {
                    const unsigned fo = WS_FKV + lane * 8;
                    asm volatile("global_load_dwordx2 %0, %4, %5 sc1\n\tglobal_load_dwordx2 %1, %4, %5 offset:512 sc1\n\t"
                                 "global_load_dwordx2 %2, %4, %5 offset:1024 sc1\n\tglobal_load_dwordx2 %3, %4, %5 offset:1536 sc1"
                                 : "=&v"(fl0), "=&v"(fl1), "=&v"(fl2), "=&v"(fl3) : "v"(fo), "s"(ws) : "memory");
                }
            }
            if (ks == 2) {
                if (ISB) {
                    if (c == 0) bar_vl<14>(); else if (c == 1) bar_vl<12>(); else if (c <= 5) bar_vl<8>(); else if (c == 6) bar_vl<5>(); else if (c == 7) bar_vl<10>(); else BAR_L();
                } else if (c == 2) {
                    bar_vl<6>();
                    PIN(pf);
                    if (tb == 3 && lane == 0) put_flag(ws, WS_FKV + (16 * ti + tj) * 8, tag);
                } else if (c == 3 || c == 4) bar_vl<6>();
                else if (c == 5) { if (tb == 3) WAITV(10); else WAITV(6); BAR_L(); }
                else if (c == 6) {
                    if (tb == 3) {
                        asm volatile("s_waitcnt vmcnt(4)" : "+v"(fl0), "+v"(fl1), "+v"(fl2), "+v"(fl3) :: "memory");
                        const unsigned t0 = (unsigned)tag, t1 = (unsigned)(tag >> 32);
                        const bool seen = fl0[0] == t0 && fl0[1] == t1 && fl1[0] == t0 && fl1[1] == t1 && fl2[0] == t0 && fl2[1] == t1 && fl3[0] == t0 && fl3[1] == t1;
                        if (!__all(seen)) poll4(ws, tag, lane);
                        bar_vl<4>();
                        if (b == 0 && lane == 0) __hip_atomic_store((gu64*)ws, tag, __ATOMIC_RELAXED, __HIP_MEMORY_SCOPE_AGENT);
                    } else bar_vl<4>();
                } else if (c == 7) bar_vl<0>();
                else BAR_L();
                if (!ISB && c == 0 && tb == 3) { __builtin_amdgcn_s_setprio(3); kv_format(); __builtin_amdgcn_s_setprio(0); }
                if (!ISB && c == WPHI_AT) { if (tb < 3) build_wphi(L_WPR); }
                if (c == 0) STAMP(20);
                if (c == 1) STAMP(21);
                if (c == 3) STAMP(4);
                if (c == 5) STAMP(22);
                if (c == 7) STAMP(23);
                if (c == 8) STAMP(6);
            }
            const int lc = ks < 2 ? c : c + 1, lk = (ks + 2) & 3;
            const bool ld = lc < 9;
            const int dc = c + 3;
            const int p0 = dc == 3 ? (ks == 0 ? 0 : 2 * ks + 1) : (ISB ? (ks == 0 ? 4 : 5 + ks) : ks);
            const int p1 = NODMA || dc >= 9 || (dc == 3 && !ISB) ? p0 : (dc == 3 ? 2 * ks + 3 : (ISB ? 6 + ks : ks + 1));
            const int pm = p0 + (p1 - p0 + 1) / 2;
            const int ei = 4 * (c - 6) + ks - 2;
            auto early = [&](int e) {
                if (e < 9) { const unsigned p = 37 + tb + 4 * e; dma_s_sc1(p * 1024, ws + WS_KVIMG + p * 1024, lane * 16); }
                else dma_s_sc1(tb == 1 ? L_KMAX : 73 * 1024, ws + (tb == 1 ? WS_KMAX : WS_KVIMG + 73 * 1024), lane * 16);
            };
            HM(acc0, FW0[ks], FX[ks]); SBAR();
            if (ld && HRD) { FX[lk] = rdx(lc, lk); FW0[lk] = rdw(lc, lk, 0); } SBAR();
#pragma unroll
            for (int p = p0; p < pm; ++p) dma1(dc, p);
            if (ISB && ei >= 0 && ei < 4) { early(ei < 2 ? 3 * ei : 2 * ei + 2); if (ei < 2) early(3 * ei + 1); }
            SBAR();
            HM(acc1, FW1[ks], FX[ks]); SBAR();
            if (ld && HRD) FW1[lk] = rdw(lc, lk, 1); SBAR();
#pragma unroll
            for (int p = pm; p < p1; ++p) dma1(dc, p);
            if (ISB && ei >= 0 && ei < 4) early(ei < 2 ? 3 * ei + 2 : 2 * ei + 3);
            SBAR();
            if (!ISB) { HM(acc2, FW2[ks], FX[ks]); SBAR(); if (ld && HRD) FW2[lk] = rdw(lc, lk, 2); SBAR(); }
        }
    }
    STAMP(5);
    const unsigned vrow = L_VB + (32 * tb + n) * 112 + hi * 52 + 8;

    if (ISB) {
#pragma unroll
        for (int u = 0; u < 6; ++u) LW(unsigned, vrow + 4 * u) = cvtpk(acc1[4 + 2 * u], acc1[5 + 2 * u]);
        asm volatile("s_waitcnt vmcnt(0) lgkmcnt(0)" ::: "memory");
        lds_inc(C_PHI, lane); lds_inc(C_LAND0, lane);
        const float gate = __uint_as_float(__builtin_amdgcn_raw_buffer_load_b32(rws, WS_G + (128 * ti + 32 * tb + n) * 4, 0, 16));
        u32x4 QA, QB; u32x2 QRA0, QRA1, QRB0, QRB1; f32x16 MA, MB;
        {
            const float sc = 0.23570226039551584f * 1.4426950408889634f;
            float q[20];
#pragma unroll
            for (int i = 0; i < 16; ++i) q[i] = acc0[i] * sc;
#pragma unroll
            for (int i = 0; i < 4; ++i) q[16 + i] = acc1[i] * sc;
            float n2 = hi ? q[18] * q[18] + q[19] * q[19] : q[0] * q[0] + q[1] * q[1];
#pragma unroll
            for (int i = 2; i < 18; ++i) n2 = __builtin_fmaf(q[i], q[i], n2);
            unsigned cc[10], D[9];
#pragma unroll
            for (int u = 0; u < 10; ++u) cc[u] = cvtpk(q[2 * u], q[2 * u + 1]);
#pragma unroll
            for (int u = 0; u < 9; ++u) D[u] = hi ? cc[u + 1] : cc[u];
#pragma unroll
            for (int u = 0; u < 4; ++u) { const auto sw = __builtin_amdgcn_permlane32_swap(D[u], D[u + 4], false, false); QA[u] = sw[0]; QB[u] = sw[1]; }
            QRA0 = (u32x2){hi ? 0u : D[8], 0u}; QRA1 = (u32x2){0u, hi ? 0u : D[8]};
            QRB0 = (u32x2){hi ? D[8] : 0u, 0u}; QRB1 = (u32x2){0u, hi ? D[8] : 0u};
            SBAR();
            lds_wait<1>(C_LAND0, 4, ws);
            const f32x4 km4 = LA(f32x4, L_KMAX + lane * 16);
            const float km = wave_max(__builtin_fmaxf(__builtin_fmaxf(km4[0], km4[1]), __builtin_fmaxf(km4[2], km4[3])));
            const float m = -1.01f * __builtin_amdgcn_sqrtf(n2 * km);
            const auto sw = __builtin_amdgcn_permlane32_swap(__float_as_uint(m), __float_as_uint(m), false, false);
            const float ma = __uint_as_float(sw[0]), mb = __uint_as_float(sw[1]);
#pragma unroll
            for (int i = 0; i < 16; ++i) { MA[i] = ma; MB[i] = mb; }
            PIN(MA); PIN(MB); PIN(QA); PIN(QB); PIN(QRA0); PIN(QRA1); PIN(QRB0); PIN(QRB1);
        }
        STAMP(7);
        unsigned kp = 4 * STEP_BYTES + lane * 16, rp = 4 * STEP_BYTES + KR_OFF + n * 8, vp = 4 * STEP_BYTES + 1024 + hi * 304 + (n < 18 ? n : 18) * 16;
        if (n > 18) vp = 0x80000;
        PIN(kp); PIN(rp); PIN(vp);
        f32x16 OA = {}, OB = {}, S0A, S0B, S1A, S1B;
        u32x4 KMa, KMb, V0a, V0b, V1a, V1b = {}, P0 = {}, P1 = {}; u32x2 KRX, KRY;
        float e0, e1, e2, e3, f0 = 0.f, f1 = 0.f, f2 = 0.f, f3 = 0.f;
        KRX = LA(u32x2, rp); KMa = LA(u32x4, kp);
        S0A = MFMA(KMa, QA, MA); S0B = MFMA(KMa, QB, MB); S0A = MFMA8(KRX, QRA0, S0A); S0B = MFMA8(KRX, QRB0, S0B);
        SBAR();
        KMb = LA(u32x4, kp + KB_BYTES); V0a = LA(u32x4, vp); V1a = LA(u32x4, vp + 608);
        __builtin_amdgcn_s_waitcnt(0xC07F);
        SBAR();
#define LD(dst, T, p) dst = LA(T, p)
#define NOP_ (void)0
#define SLOTB(MF, POST, d0, d1, d2, d3, S, g, Pq, i, a0, a1, a2, a3) do { MF; SBAR(); POST; SBAR(); \
        d0 = EX(S[4 * (g)]); d1 = EX(S[4 * (g) + 1]); PIN(d0); PIN(d1); SBAR(); \
        Pq[i] = cvtpk(a0, a1); Pq[i + 1] = cvtpk(a2, a3); PIN(Pq); SBAR(); \
        d2 = EX(S[4 * (g) + 2]); d3 = EX(S[4 * (g) + 3]); PIN(d2); PIN(d3); SBAR(); } while (0)
#define E_ e0, e1, e2, e3
#define F_ f0, f1, f2, f3
#define SLOT_(...) SLOTB(__VA_ARGS__)
#define WAITL __builtin_amdgcn_s_waitcnt(0xC07F)
#define PERIOD(SA, SB, NA, NB, KMu, KMl, KRu, QRAx, QRBx, V0u, V1u, V0l, V1l, va, ka, PA3) do { \
        SLOT_(NA = MFMA(KMu, QA, MA),     LD(KMl, u32x4, ka),              E_, SA, 0, P1, 2, F_); \
        SLOT_(OB = MFMA(V1l, P1, OB),     LD(V1l, u32x4, (va) + 608),      F_, SA, 1, P0, 0, E_); \
        SLOT_(NA = MFMA8(KRu, QRAx, NA),  LD(V0l, u32x4, va),              E_, SA, 2, P0, 2, F_); \
        SLOT_(OA = MFMA(V0u, P0, OA),     PA3,                             F_, SA, 3, P1, 0, E_); \
        SLOT_(NB = MFMA(KMu, QB, MB),     NOP_,                            E_, SB, 0, P1, 2, F_); \
        SLOT_(OA = MFMA(V1u, P1, OA),     NOP_,                            F_, SB, 1, P0, 0, E_); \
        SLOT_(NB = MFMA8(KRu, QRBx, NB),  WAITL,                           E_, SB, 2, P0, 2, F_); \
        SLOT_(OB = MFMA(V0u, P0, OB),     NOP_,                            F_, SB, 3, P1, 0, E_); \
    } while (0)
#pragma unroll 1
        for (int seg = 0; seg < 4; ++seg) {
            if (seg == 1) lds_wait<1>(C_LAND1, 4, ws);
            if (seg == 2) { lds_inc(C_BPROG, lane); STAMP(8); }
            if (seg == 3) lds_wait<1>(C_LAND2, 4, ws);
            const int ns = seg == 0 ? 3 : (seg == 1 ? 6 : (seg == 2 ? 2 : 5));
            unsigned dl = seg == 3 ? 0u - 15 * STEP_BYTES : STEP_BYTES;
#pragma unroll 1
            for (int s = 0; s < ns; ++s) {
                unsigned kn = kp + dl, vn = vp + dl; PIN(kn); PIN(vn); SBAR();
                PERIOD(S0A, S0B, S1A, S1B, KMb, KMa, KRX, QRA1, QRB1, V0a, V1a, V0b, V1b, vp + KB_BYTES, kp + 2 * KB_BYTES, LD(KRY, u32x2, rp + 256));
                PERIOD(S1A, S1B, S0A, S0B, KMa, KMb, KRY, QRA0, QRB0, V0b, V1b, V0a, V1a, vp + 2 * KB_BYTES, kp + 3 * KB_BYTES, NOP_);
                rp += dl; PIN(rp); SBAR();
                PERIOD(S0A, S0B, S1A, S1B, KMb, KMa, KRY, QRA1, QRB1, V0a, V1a, V0b, V1b, vp + 3 * KB_BYTES, kn, LD(KRX, u32x2, rp));
                PERIOD(S1A, S1B, S0A, S0B, KMa, KMb, KRX, QRA0, QRB0, V0b, V1b, V0a, V1a, vn, kn + KB_BYTES, NOP_);
                kp = kn; vp = vn; dl = STEP_BYTES;
            }
        }
        P1[2] = cvtpk(f0, f1); P1[3] = cvtpk(f2, f3); PIN(P1); SBAR();
        OB = MFMA(V1b, P1, OB);
        STAMP(10);
        auto rowsum = [&](const f32x16& O) { const auto sw = __builtin_amdgcn_permlane32_swap(__float_as_uint(O[10]), __float_as_uint(O[10]), false, false); return __uint_as_float(sw[0]); };
        float lA = rowsum(OA), lB = rowsum(OB);
        if (__any(!(lA >= 0x1p-80f) || !(lB >= 0x1p-80f))) {
            const unsigned vo = WS_KVIMG + 1024 + hi * 304 + (n < 18 ? n : 18) * 16;
#pragma unroll 1
            for (int which = 0; which < 2; ++which) {
                const u32x4 Q = which ? QB : QA; const u32x2 R0 = which ? QRB0 : QRA0, R1 = which ? QRB1 : QRA1;
                float mx = -__builtin_inff();
#pragma unroll 1
                for (int kb = 0; kb < 64; ++kb) {
                    const unsigned bo = (kb >> 2) * STEP_BYTES;
                    const u32x4 km = __builtin_bit_cast(u32x4, __builtin_amdgcn_raw_buffer_load_b128(rws, WS_KVIMG + bo + (kb & 3) * KB_BYTES + lane * 16, 0, 16));
                    const u32x2 kr = __builtin_bit_cast(u32x2, __builtin_amdgcn_raw_buffer_load_b64(rws, WS_KVIMG + bo + KR_OFF + ((kb >> 1) & 1) * 256 + n * 8, 0, 16));
                    f32x16 S = {}; S = MFMA(km, Q, S); S = MFMA8(kr, (kb & 1) ? R1 : R0, S);
#pragma unroll
                    for (int i = 0; i < 16; ++i) mx = __builtin_fmaxf(mx, S[i]);
                }
                { const auto sw = __builtin_amdgcn_permlane32_swap(__float_as_uint(mx), __float_as_uint(mx), false, false); mx = __builtin_fmaxf(__uint_as_float(sw[0]), __uint_as_float(sw[1])); }
                f32x16 O = {}, C;
#pragma unroll
                for (int i = 0; i < 16; ++i) C[i] = -mx;
#pragma unroll 1
                for (int kb = 0; kb < 64; ++kb) {
                    const unsigned bo = (kb >> 2) * STEP_BYTES, bb = bo + (kb & 3) * KB_BYTES;
                    const u32x4 km = __builtin_bit_cast(u32x4, __builtin_amdgcn_raw_buffer_load_b128(rws, WS_KVIMG + bb + lane * 16, 0, 16));
                    const u32x2 kr = __builtin_bit_cast(u32x2, __builtin_amdgcn_raw_buffer_load_b64(rws, WS_KVIMG + bo + KR_OFF + ((kb >> 1) & 1) * 256 + n * 8, 0, 16));
                    const u32x4 v0 = __builtin_bit_cast(u32x4, __builtin_amdgcn_raw_buffer_load_b128(rws, vo + bb, 0, 16));
                    const u32x4 v1 = __builtin_bit_cast(u32x4, __builtin_amdgcn_raw_buffer_load_b128(rws, vo + bb + 608, 0, 16));
                    f32x16 S = MFMA(km, Q, C); S = MFMA8(kr, (kb & 1) ? R1 : R0, S);
                    float p[16];
#pragma unroll
                    for (int i = 0; i < 16; ++i) p[i] = EX(S[i]);
                    const u32x4 p0 = {cvtpk(p[0], p[1]), cvtpk(p[2], p[3]), cvtpk(p[4], p[5]), cvtpk(p[6], p[7])};
                    const u32x4 p1 = {cvtpk(p[8], p[9]), cvtpk(p[10], p[11]), cvtpk(p[12], p[13]), cvtpk(p[14], p[15])};
                    O = MFMA(v0, p0, O); O = MFMA(v1, p1, O);
                }
                const float lo = rowsum(O);
                if (which) { OB = O; lB = lo; } else { OA = O; lA = lo; }
            }
        }
        {
            const float ga = (1.f - gate) * __builtin_amdgcn_rcpf(lA), gb = (1.f - gate) * __builtin_amdgcn_rcpf(lB);
            auto word = [&](float lo, float hi_) { const unsigned w = cvtpk(lo, hi_); return w < POISON ? w : POISON - 1; };
            const u32x4 fa = {word(OA[0] * ga, OA[1] * ga), word(OA[2] * ga, OA[3] * ga), word(OA[4] * ga, OA[5] * ga), word(OA[6] * ga, OA[7] * ga)};
            const u32x4 fb = {word(OB[0] * gb, OB[1] * gb), word(OB[2] * gb, OB[3] * gb), word(OB[4] * gb, OB[5] * gb), word(OB[6] * gb, OB[7] * gb)};
            const u32x2 rr = {word(OA[8] * ga, OA[9] * ga), word(OB[8] * gb, OB[9] * gb)};
            __builtin_amdgcn_raw_buffer_store_b128(fa, rws, obm, 0, 16);
            __builtin_amdgcn_raw_buffer_store_b128(fb, rws, obm + 1024, 0, 16);
            if (!hi) __builtin_amdgcn_raw_buffer_store_b64(rr, rws, obr, 0, 16);
            lds_inc(C_BDONE, lane);
        }
        STAMP(11);
    } else {
        __builtin_amdgcn_s_setprio(1);
        {
            unsigned ph[18];
            features(acc0, acc2[0], acc2[1], acc2[2], acc2[3], lane, ph);
            const unsigned pa = L_PHIK + (32 * tb + n) * 160 + hi * 80;
#pragma unroll
            for (int u = 0; u < 4; ++u) LW(u32x4, pa + 16 * u) = (u32x4){ph[4 * u], ph[4 * u + 1], ph[4 * u + 2], ph[4 * u + 3]};
            LW(u32x2, pa + 64) = (u32x2){ph[16], ph[17]};
            LW(unsigned, vrow + 24) = cvtpk(acc2[8], acc2[9]); LW(unsigned, vrow + 28) = cvtpk(acc2[10], acc2[11]);
            LW(unsigned, vrow + 32) = cvtpk(acc2[12], acc2[13]); LW(unsigned, vrow + 36) = cvtpk(acc2[14], acc2[15]);
            LW(unsigned, vrow + 36 + 4 * hi) = 0x00003f80u;
            asm volatile("s_waitcnt lgkmcnt(0)" ::: "memory");
            lds_inc(C_PHI, lane);
        }
#pragma unroll 1
        for (int i = 0; i < 19; ++i) { const int p = 74 + tb + 4 * i < 147 ? 74 + tb + 4 * i : 147; dma_s_sc1(p * 1024, ws + WS_KVIMG + p * 1024, lane * 16); }
        const float gate = __uint_as_float(__builtin_amdgcn_raw_buffer_load_b32(rws, WS_G + (128 * ti + 32 * tb + n) * 4, 0, 16));
        STAMP(7);
        lds_wait<2>(C_PHI, 8, ws);
        __builtin_amdgcn_s_setprio(2);
        if (tb < 3) build_wphi(L_WPQ);
        unsigned gen = 0;
        {
            const int hh = tb & 1, i = lane & 15;
            const int tok = 8 * g4 + (i >> 2), gr = i & 3;
            const unsigned va = L_VB + tok * 112 + hh * 56 + 8 + gr * 8;
            const int ntask = tb < 2 ? 2 : 1;
#pragma unroll 1
            for (int t = 0; t < ntask; ++t) {
                const int mbf = tb < 2 ? 2 * t : 1;
                const unsigned pa = L_PHIK + tok * 160 + hh * 80 + mbf * 32 + gr * 8;
                f32x4 d0 = {}, d1 = {};
#pragma unroll
                for (int ks = 0; ks < 4; ++ks) {
                    const u32x4 a = trd2(pa + ks * 32 * 160, pa + ks * 32 * 160 + 4 * 160);
                    const u32x4 b0 = trd2(va + ks * 32 * 112, va + ks * 32 * 112 + 4 * 112);
                    const u32x4 b1 = trd2(va + ks * 32 * 112 + 32, va + ks * 32 * 112 + 4 * 112 + 32);
                    d0 = MFMA16(a, b0, d0); d1 = MFMA16(a, b1, d1);
                }
                const unsigned ko = WS_KVP + (unsigned)(16 * tj + ti) * 5472 + hh * 2736 + (16 * mbf + 4 * g4) * 4;
                if (mbf < 2 || g4 == 0) {
                    __builtin_amdgcn_raw_buffer_store_b128(__builtin_bit_cast(u32x4, d0), rws, ko + n16 * 144, 0, 16);
                    if (n16 < 3) __builtin_amdgcn_raw_buffer_store_b128(__builtin_bit_cast(u32x4, d1), rws, ko + (16 + n16) * 144, 0, 16);
                }
            }
        }
        WAITV(0);
        lds_inc(C_LAND1, lane);
        async_bar(gen, lane, ws);
        if (tb == 0 && lane == 0) put_flag(ws, WS_FKVP + (16 * tj + ti) * 8, tag);
        STAMP(8);
        if (tb == 3) {
            const unsigned l0 = b * 21 + lane % 21;
            u64 p2 = (u64)P.woA, p3 = (u64)P.woB;
            asm volatile("" : "+s"(p2), "+s"(p3));
            const u64 ad = (lane < 21 ? p2 : p3) + (l0 < 5183 ? l0 : 5183) * 128;
            asm volatile("global_load_dword %0, %1, off" : "=&v"(pf) : "v"(ad) : "memory");
        }
        for (int p = tb; p < 37; p += 4) dma_s_sc1(p * 1024, ws + WS_KVIMG + p * 1024, lane * 16);
        unsigned pq[18];
        features(acc1, acc2[4], acc2[5], acc2[6], acc2[7], lane, pq);
        WAITV(0);
        PIN(pf);
        lds_inc(C_LAND2, lane);
        poll_flags(ws, WS_FKVP + 16 * tq * 8, 16, tag, lane, 1);
        STAMP(9);
        {
            const int at = tb * 64 + lane;
#pragma unroll 1
            for (int rd = 0; rd < 2; ++rd) {
                const int it = rd == 0 ? at : 256 + tb * 22 + lane;
                const bool ok = rd == 0 || (lane < 22 && it < 342);
                const unsigned ko = WS_KVP + (unsigned)(16 * tq) * 5472 + (ok ? it : 0) * 16;
                f32x4 v[16];
#pragma unroll
                for (int i = 0; i < 16; ++i) v[i] = __builtin_bit_cast(f32x4, __builtin_amdgcn_raw_buffer_load_b128(rws, ko + i * 5472, 0, 16));
                f32x4 s = v[0];
#pragma unroll
                for (int i = 1; i < 16; ++i) s += v[i];
                const int hh = it >= 171, rem = it - 171 * hh, d = rem / 9, fq = rem - 9 * d;
                if (ok) LW(u32x2, L_KVT + hh * 1600 + d * 80 + fq * 8) = (u32x2){cvtpk(s[0], s[1]), cvtpk(s[2], s[3])};
            }
            if (tb == 3 && lane < 10) LW(u32x4, L_KVT + (lane >= 5) * 1600 + 19 * 80 + (lane % 5) * 16) = (u32x4){0u, 0u, 0u, 0u};
        }
        async_bar(gen, lane, ws);
        {
            const int hm = (n >> 2) & 1, d0 = 4 * (n >> 3) + (n & 3);
            const unsigned a0 = L_KVT + (hm == hi ? hm * 1600 + d0 * 80 : 19 * 80);
            const unsigned a1 = L_KVT + (hm == hi && d0 < 3 ? hm * 1600 + (16 + d0) * 80 : 19 * 80);
            f32x16 N0 = {}, N1 = {};
#pragma unroll
            for (int s = 0; s < 4; ++s) {
                const u32x4 bq = {pq[4 * s], pq[4 * s + 1], pq[4 * s + 2], pq[4 * s + 3]};
                N0 = MFMA(LA(u32x4, a0 + 16 * s), bq, N0); N1 = MFMA(LA(u32x4, a1 + 16 * s), bq, N1);
            }
            const u32x2 br = {pq[16], pq[17]};
            N0 = MFMA8(LA(u32x2, a0 + 64), br, N0); N1 = MFMA8(LA(u32x2, a1 + 64), br, N1);
            const float sc = gate / (N1[2] + 1e-6f);
            const unsigned fo = WS_OAF + (unsigned)(4 * ti + tb) * 36 * 1024;
            const u32x4 f0 = {cvtpk(N0[0] * sc, N0[1] * sc), cvtpk(N0[2] * sc, N0[3] * sc), cvtpk(N0[4] * sc, N0[5] * sc), cvtpk(N0[6] * sc, N0[7] * sc)};
            const u32x4 f1 = {cvtpk(N0[8] * sc, N0[9] * sc), cvtpk(N0[10] * sc, N0[11] * sc), cvtpk(N0[12] * sc, N0[13] * sc), cvtpk(N0[14] * sc, N0[15] * sc)};
            __builtin_amdgcn_raw_buffer_store_b128(f0, rws, fo + (2 * tq) * 1024 + lane * 16, 0, 16);
            __builtin_amdgcn_raw_buffer_store_b128(f1, rws, fo + (2 * tq + 1) * 1024 + lane * 16, 0, 16);
            __builtin_amdgcn_raw_buffer_store_b32(cvtpk(N1[0] * sc, N1[1] * sc), rws, fo + (32 + (tq >> 2)) * 1024 + (((tq >> 1) & 1) * 32 + n) * 16 + (2 * (tq & 1) + hi) * 4, 0, 16);
        }
        WAITV(0);
        async_bar(gen, lane, ws);
        if (tb == 0 && lane == 0) put_flag(ws, WS_FOA + (16 * ti + tq) * 8, tag);
        STAMP(10);
        const int tt = 4 * x8 + (r & 3), cg = r >> 2;
        unsigned wo[9];
#pragma unroll
        for (int i = 0; i < 9; ++i) { const unsigned q = (tb + 4 * i) * 64 + lane, row = q / 9; wo[i] = row * 1152 + (q - row * 9) * 16; }
        auto stage = [&](const char* w) {
            const char* wsl = w + cg * 144;
#pragma unroll
            for (int i = 0; i < 21; ++i) {
                const int p = tb + 4 * i;
                if (i < 20 || tb == 0) dma_s(L_WOS + p * 1024, wsl + (i / 9) * 256 * 1152, wo[i % 9]);
            }
        };
        const int i16 = lane & 15, gr = i16 & 3, hk = g4 & 1, fh = g4 >> 1;
        const unsigned fro = (unsigned)(2 * tt + (tb >> 1)) * 36 * 1024 + fh * 1024 + (hk * 32 + n16 + 16 * (tb & 1)) * 16;
        const unsigned remo = L_WOS + (18 * (8 * fh + 4 * hk + (i16 >> 3)) + 16 + ((i16 >> 2) & 1)) * 144 + gr * 8;
        f32x4 T[5] = {};
        auto wfrag = [&](int ks, int cb, unsigned maino, unsigned r4s) {
            const unsigned a = (ks < 16 ? maino + ks * 36 * 144 : remo + (ks - 16) * 16 * 18 * 144) + cb * 32;
            return trd2(a, a + (ks < 16 ? r4s : 2 * 18 * 144));
        };
        auto gemm = [&](const u32x4 (&bf)[18], unsigned maino, unsigned r4s, auto ord) {
            u32x4 wf[2][5];
#pragma unroll
            for (int cb = 0; cb < 5; ++cb) wf[0][cb] = wfrag(ord(0), cb, maino, r4s);
            SBAR();
#pragma unroll
            for (int q = 0; q < 18; ++q) {
#pragma unroll
                for (int cb = 0; cb < 5; ++cb) {
                    T[cb] = MFMA16(wf[q & 1][cb], bf[q], T[cb]); SBAR();
                    if (q < 17) wf[(q + 1) & 1][cb] = wfrag(ord(q + 1), cb, maino, r4s);
                    SBAR();
                }
            }
        };
        u32x4 bf[18];
        poll_flags(ws, WS_FOA + 16 * (tt >> 1) * 8, 16, tag, lane, 1);
#pragma unroll
        for (int ks = 0; ks < 18; ++ks) bf[ks] = __builtin_bit_cast(u32x4, __builtin_amdgcn_raw_buffer_load_b128(rws, WS_OAF + fro + ks * 2048, 0, 16));
        SBAR();
        lds_wait<16>(C_BPROG, 4, ws);
        stage(P.woA);
        WAITV(0);
        async_bar(gen, lane, ws);
        STAMP(11);
        gemm(bf, L_WOS + (18 * hk + 8 * fh + (i16 >> 2)) * 144 + gr * 8, 4 * 144, [](int q) { return q; });
        async_bar(gen, lane, ws);
        STAMP(12);
        stage(P.woB);
        WAITV(0);
        async_bar(gen, lane, ws);
        STAMP(13);
        {
            const unsigned maino = L_WOS + (18 * fh + 4 * hk + (i16 >> 2)) * 144 + gr * 8;
            const unsigned remg = L_WOS + (36 * g4 + 16 + 18 * (i16 >> 3) + ((i16 >> 2) & 1)) * 144 + gr * 8;
            const unsigned rro = WS_OBF + (unsigned)(2 * tt + (tb >> 1)) * 36 * 1024 + 32 * 1024 + ((g4 >> 1) * 32 + 16 * (tb & 1) + n16) * 16 + (g4 & 1) * 8;
            f32x4 G[4][5];
#pragma unroll
            for (int cb = 0; cb < 5; ++cb) { G[0][cb] = T[cb]; G[1][cb] = (f32x4){}; G[2][cb] = (f32x4){}; G[3][cb] = (f32x4){}; }
            u32x2 br[4];
            unsigned have = 0, done = 0;
            bool bdone = false;
#pragma unroll 1
            for (unsigned s = 0;; ++s) {
#pragma unroll
                for (int g = 0; g < 4; ++g) {
                    if (!((have >> g) & 1)) {
#pragma unroll
                        for (int u = 0; u < 4; ++u) bf[4 * g + u] = __builtin_bit_cast(u32x4, __builtin_amdgcn_raw_buffer_load_b128(rws, WS_OBF + fro + (4 * g + u) * 2048, 0, 16));
                        br[g] = __builtin_bit_cast(u32x2, __builtin_amdgcn_raw_buffer_load_b64(rws, rro + g * 1024, 0, 16));
                    }
                }
#pragma unroll
                for (int g = 0; g < 4; ++g) {
                    if (!((have >> g) & 1)) {
                        unsigned m = br[g][0] > br[g][1] ? br[g][0] : br[g][1];
#pragma unroll
                        for (int u = 0; u < 4; ++u) {
                            const u32x4 w = bf[4 * g + u];
                            const unsigned m0 = w[0] > w[1] ? w[0] : w[1], m1 = w[2] > w[3] ? w[2] : w[3];
                            m = m > m0 ? m : m0; m = m > m1 ? m : m1;
                        }
                        if (__all(m != POISON)) have |= 1u << g;
                    }
                }
                if (!bdone) { bdone = uni(__hip_atomic_load((AS3 unsigned*)(uintptr_t)C_BDONE, __ATOMIC_RELAXED, __HIP_MEMORY_SCOPE_WORKGROUP)) >= 4; if (bdone) STAMP(14); }
                if (bdone) {
#pragma unroll
                    for (int g = 0; g < 4; ++g) {
                        if (((have & ~done) >> g) & 1) {
                            u32x4 wf[2][5]; u32x2 wr[5];
#pragma unroll
                            for (int cb = 0; cb < 5; ++cb) wf[0][cb] = wfrag(4 * g, cb, maino, 8 * 144);
                            SBAR();
#pragma unroll
                            for (int u = 0; u < 4; ++u) {
#pragma unroll
                                for (int cb = 0; cb < 5; ++cb) {
                                    G[g][cb] = MFMA16(wf[u & 1][cb], bf[4 * g + u], G[g][cb]); SBAR();
                                    if (u < 3) wf[(u + 1) & 1][cb] = wfrag(4 * g + u + 1, cb, maino, 8 * 144);
                                    else wr[cb] = trd(remg + g * 144 * 144 + cb * 32);
                                    SBAR();
                                }
                            }
#pragma unroll
                            for (int cb = 0; cb < 5; ++cb) G[g][cb] = MFMA1616(wr[cb], br[g], G[g][cb]);
                            done |= 1u << g;
                        }
                    }
                    if (done == 15) {
                        break;
                    }
                }
                if (s > SPIN_MAX) { if (lane == 0) __hip_atomic_store((gu32*)(ws + WS_TMO), 0x300u, __ATOMIC_RELAXED, __HIP_MEMORY_SCOPE_AGENT); break; }
                if (bdone) __builtin_amdgcn_s_sleep(1); else __builtin_amdgcn_s_sleep(16);
            }
#pragma unroll
            for (int cb = 0; cb < 5; ++cb) T[cb] = (G[0][cb] + G[1][cb]) + (G[2][cb] + G[3][cb]);
        }
        STAMP(19);
#define OUTAUX 0
        const __amdgpu_buffer_rsrc_t rout = __builtin_amdgcn_make_buffer_rsrc((void*)P.out, (short)0, 2048 * 1152, 0x00020000);
        const unsigned op = (unsigned)(64 * tt + 16 * tb + n16) * 1152 + cg * 144 + g4 * 8;
#pragma unroll
        for (int cb = 0; cb < 5; ++cb) {
            const u32x2 o = {cvtpk(T[cb][0], T[cb][1]), cvtpk(T[cb][2], T[cb][3])};
            if (cb < 4 || g4 < 2) __builtin_amdgcn_raw_buffer_store_b64(o, rout, op + cb * 32, 0, OUTAUX);
        }
    }
}

__global__ __launch_bounds__(512, 2) void gated_mix_kernel(const void* x, const void* wqkv, const void* wphi, const void* woA, const void* wq, const void* wk,
                                                           const void* wv, const void* woB, const void* gate, void* out, void* ws) {
    extern __shared__ __attribute__((aligned(16))) char lds[];
    const int lane = threadIdx.x & 63, wid = uni(threadIdx.x >> 6);
    const Ptrs P = {(const char*)x, (const char*)wqkv, (const char*)wphi, (const char*)woA, (const char*)wq, (const char*)wk, (const char*)wv, (const char*)woB, (const char*)gate, (char*)out, (char*)ws};
    if (wid < 4) role_main<true>(P, wid, lane); else role_main<false>(P, wid, lane);
}
}

extern "C" void kernel_launch(void* const* d_in, const int* in_sizes, int n_in, void* d_out, int out_size, void* d_ws, size_t ws_size, hipStream_t stream) {
    (void)in_sizes; (void)n_in; (void)out_size; (void)ws_size;
    (void)hipFuncSetAttribute((const void*)fk::gated_mix_kernel, hipFuncAttributeMaxDynamicSharedMemorySize, fk::LDS_BYTES);
    hipLaunchKernelGGL(fk::gated_mix_kernel, dim3(256), dim3(512), fk::LDS_BYTES, stream,
                       (const void*)d_in[0], (const void*)d_in[1], (const void*)d_in[2], (const void*)d_in[3], (const void*)d_in[5], (const void*)d_in[6],
                       (const void*)d_in[7], (const void*)d_in[8], (const void*)d_in[9], d_out, d_ws);
}
```

```cpp
#include <hip/hip_runtime.h>
#include <hip/hip_bf16.h>
#include <stdint.h>

namespace fk {
typedef __bf16 bf16x2 __attribute__((ext_vector_type(2)));
typedef __bf16 bf16x8 __attribute__((ext_vector_type(8)));
typedef short s16x4 __attribute__((ext_vector_type(4)));
typedef float f32x2 __attribute__((ext_vector_type(2)));
typedef float f32x4 __attribute__((ext_vector_type(4)));
typedef float f32x16 __attribute__((ext_vector_type(16)));
typedef unsigned u32x2 __attribute__((ext_vector_type(2)));
typedef unsigned u32x4 __attribute__((ext_vector_type(4)));
typedef unsigned long long u64;
typedef __attribute__((address_space(1))) u64 gu64;
typedef __attribute__((address_space(1))) unsigned gu32;

#define AS3 __attribute__((address_space(3)))
#define SBAR() __builtin_amdgcn_sched_barrier(0)
#define PIN(x) asm volatile("" : "+v"(x))
#define MFMA(a, b, c) __builtin_amdgcn_mfma_f32_32x32x16_bf16(__builtin_bit_cast(bf16x8, a), __builtin_bit_cast(bf16x8, b), c, 0, 0, 0)
#define MFMA8(a, b, c) __builtin_amdgcn_mfma_f32_32x32x8bf16_1k(__builtin_bit_cast(s16x4, a), __builtin_bit_cast(s16x4, b), c, 0, 0, 0)
#define MFMA1616(a, b, c) __builtin_amdgcn_mfma_f32_16x16x16bf16_1k(__builtin_bit_cast(s16x4, a), __builtin_bit_cast(s16x4, b), c, 0, 0, 0)
#define MFMA16(a, b, c) __builtin_amdgcn_mfma_f32_16x16x32_bf16(__builtin_bit_cast(bf16x8, a), __builtin_bit_cast(bf16x8, b), c, 0, 0, 0)
#define LA(T, a) (*(const AS3 T*)(uintptr_t)(a))
#define LW(T, a) (*(AS3 T*)(uintptr_t)(a))
#define PFV 0
#define APRIO 3
#define FENCE() asm volatile("" ::: "memory")
#define EX(x) __builtin_amdgcn_exp2f(x)

#define WS_TMO 64
constexpr unsigned WS_FKV = 4096, WS_FKVP = 8192, WS_FOA = 12288, WS_FOB = 16384;
#define WS_G 32768
#define WS_KMAX 40960
#define WS_KVIMG 65536
#define WS_KVP 262144
#define WS_OAF (2 << 20)
#define WS_OBF (5 << 20)

constexpr unsigned SLOT = 36864, WOFF = 16384;
constexpr unsigned L_WKV = 110592;
constexpr unsigned L_XR = 152064, L_RED = 152064;
constexpr unsigned L_CNT = 163792, L_WPR = 157184, L_WPQ = 158480;
constexpr unsigned L_WPHI = 152064, L_KVT = 157184;
constexpr unsigned L_PHIK = 0, L_VB = 20480;
constexpr unsigned L_KMAX = 161152, L_WOS = 37888;
constexpr unsigned KB_BYTES = 2240, STEP_BYTES = 9472, KR_OFF = 8960;
constexpr unsigned LDS_BYTES = 163840;
constexpr unsigned C_LAND0 = L_CNT, C_LAND1 = L_CNT + 4, C_LAND2 = L_CNT + 8, C_PHI = L_CNT + 12, C_ASY = L_CNT + 16, C_BPROG = L_CNT + 20, C_BDONE = L_CNT + 24, C_RAW = L_CNT + 28, C_WKVF = L_CNT + 32;
constexpr unsigned POISON = 0xffffffffu;

#define STAMP(k) do { } while (0)

__device__ __forceinline__ unsigned cvtpk(float lo, float hi) { f32x2 v = {lo, hi}; return __builtin_bit_cast(unsigned, __builtin_convertvector(v, bf16x2)); }
__device__ __forceinline__ int uni(int v) { return __builtin_amdgcn_readfirstlane(v); }

__device__ __forceinline__ void dma_s(unsigned lds, const void* sbase, unsigned voff) {
    asm volatile("s_mov_b32 m0, %0\n\ts_nop 0\n\tglobal_load_lds_dwordx4 %1, %2" :: "s"(lds), "v"(voff), "s"(sbase) : "memory", "m0");
}
__device__ __forceinline__ void dma_s_sc1(unsigned lds, const void* sbase, unsigned voff) {
    asm volatile("s_mov_b32 m0, %0\n\ts_nop 0\n\tglobal_load_lds_dwordx4 %1, %2 sc1" :: "s"(lds), "v"(voff), "s"(sbase) : "memory", "m0");
}
__device__ __forceinline__ void dma_v(unsigned lds, const void* vaddr) {
    asm volatile("s_mov_b32 m0, %0\n\ts_nop 0\n\tglobal_load_lds_dwordx4 %1, off" :: "s"(lds), "v"(vaddr) : "memory", "m0");
}
#define WAITV(n) asm volatile("s_waitcnt vmcnt(" #n ")" ::: "memory")
#define BAR_L() asm volatile("s_waitcnt lgkmcnt(0)\n\ts_barrier" ::: "memory")
template <int N> __device__ __forceinline__ void waitv() { asm volatile("s_waitcnt vmcnt(%0)" :: "n"(N) : "memory"); }
template <int N> __device__ __forceinline__ void bar_vl() { asm volatile("s_waitcnt vmcnt(%0) lgkmcnt(0)\n\ts_barrier" :: "n"(N) : "memory"); }

__device__ __forceinline__ u32x2 trd(unsigned a) { return __builtin_bit_cast(u32x2, __builtin_amdgcn_ds_read_tr16_b64_v4i16((AS3 s16x4*)(uintptr_t)a)); }
__device__ __forceinline__ u32x4 trd2(unsigned a0, unsigned a1) { const u32x2 p = trd(a0), q = trd(a1); return (u32x4){p[0], p[1], q[0], q[1]}; }

template <int CTRL> __device__ __forceinline__ float dppf(float v) { return __builtin_bit_cast(float, __builtin_amdgcn_update_dpp(0, __builtin_bit_cast(int, v), CTRL, 0xf, 0xf, true)); }
__device__ __forceinline__ float wave_max(float v) {
    v = __builtin_fmaxf(v, dppf<0x121>(v)); v = __builtin_fmaxf(v, dppf<0x122>(v)); v = __builtin_fmaxf(v, dppf<0x124>(v)); v = __builtin_fmaxf(v, dppf<0x128>(v));
    { const auto t = __builtin_amdgcn_permlane16_swap(__float_as_uint(v), __float_as_uint(v), false, false); v = __builtin_fmaxf(__uint_as_float(t[0]), __uint_as_float(t[1])); }
    { const auto t = __builtin_amdgcn_permlane32_swap(__float_as_uint(v), __float_as_uint(v), false, false); v = __builtin_fmaxf(__uint_as_float(t[0]), __uint_as_float(t[1])); }
    return v;
}
__device__ __forceinline__ u64 mix64(u64 z) { z += 0x9E3779B97F4A7C15ull; z = (z ^ (z >> 30)) * 0xBF58476D1CE4E5B9ull; z = (z ^ (z >> 27)) * 0x94D049BB133111EBull; return z ^ (z >> 31); }

constexpr unsigned SPIN_MAX = 1u << 20;
__device__ __forceinline__ void poll_flags(const char* ws, unsigned off, int n, u64 tag, int lane, int slp) {
    const gu64* p = (const gu64*)(ws + off) + (lane < n ? lane : 0);
    for (unsigned s = 0;; ++s) {
        const u64 v = __hip_atomic_load(p, __ATOMIC_RELAXED, __HIP_MEMORY_SCOPE_AGENT);
        if (__all(v == tag)) break;
        if (s > SPIN_MAX) { if (lane == 0) __hip_atomic_store((gu32*)(ws + WS_TMO), 0x100u + off, __ATOMIC_RELAXED, __HIP_MEMORY_SCOPE_AGENT); break; }
        if (slp) __builtin_amdgcn_s_sleep(8); else __builtin_amdgcn_s_sleep(1);
    }
    FENCE();
}
__device__ __forceinline__ void poll4(const char* ws, u64 tag, int lane) {
    const gu64* p = (const gu64*)(ws + WS_FKV) + lane;
    for (unsigned s = 0;; ++s) {
        const u64 v0 = __hip_atomic_load(p, __ATOMIC_RELAXED, __HIP_MEMORY_SCOPE_AGENT), v1 = __hip_atomic_load(p + 64, __ATOMIC_RELAXED, __HIP_MEMORY_SCOPE_AGENT);
        const u64 v2 = __hip_atomic_load(p + 128, __ATOMIC_RELAXED, __HIP_MEMORY_SCOPE_AGENT), v3 = __hip_atomic_load(p + 192, __ATOMIC_RELAXED, __HIP_MEMORY_SCOPE_AGENT);
        if (__all(v0 == tag && v1 == tag && v2 == tag && v3 == tag)) break;
        if (s > SPIN_MAX) { if (lane == 0) __hip_atomic_store((gu32*)(ws + WS_TMO), 0x100u, __ATOMIC_RELAXED, __HIP_MEMORY_SCOPE_AGENT); break; }
        __builtin_amdgcn_s_sleep(1);
    }
    FENCE();
}
__device__ __forceinline__ void put_flag(char* ws, unsigned off, u64 tag) { __hip_atomic_store((gu64*)(ws + off), tag, __ATOMIC_RELAXED, __HIP_MEMORY_SCOPE_AGENT); }
__device__ __forceinline__ void lds_inc(unsigned a, int lane) { if (lane == 0) __hip_atomic_fetch_add((AS3 unsigned*)(uintptr_t)a, 1u, __ATOMIC_RELAXED, __HIP_MEMORY_SCOPE_WORKGROUP); }
template <int SLP> __device__ __forceinline__ void lds_wait(unsigned a, unsigned target, const char* ws) {
    for (unsigned s = 0;; ++s) {
        const unsigned v = uni(__hip_atomic_load((AS3 unsigned*)(uintptr_t)a, __ATOMIC_RELAXED, __HIP_MEMORY_SCOPE_WORKGROUP));
        if (v >= target) break;
        if (s > SPIN_MAX) { __hip_atomic_store((gu32*)(ws + WS_TMO), 0x10u + (a & 15), __ATOMIC_RELAXED, __HIP_MEMORY_SCOPE_AGENT); break; }
        __builtin_amdgcn_s_sleep(SLP);
    }
    FENCE();
}
__device__ __forceinline__ void async_bar(unsigned& gen, int lane, const char* ws) {
    asm volatile("s_waitcnt lgkmcnt(0)" ::: "memory");
    gen += 4; lds_inc(C_ASY, lane); lds_wait<2>(C_ASY, gen, ws);
}

__device__ __forceinline__ void features(const f32x16& a0, float p16, float p17, float p18, float p19, int lane, unsigned (&out)[18]) {
    const u32x4 b0 = {cvtpk(a0[0], a0[1]), cvtpk(a0[2], a0[3]), cvtpk(a0[4], a0[5]), cvtpk(a0[6], a0[7])};
    const u32x4 b1 = {cvtpk(a0[8], a0[9]), cvtpk(a0[10], a0[11]), cvtpk(a0[12], a0[13]), cvtpk(a0[14], a0[15])};
    const u32x2 b2 = {cvtpk(p16, p17), cvtpk(p18, p19)};
    const unsigned wa = L_WPHI + lane * 16, wr = L_WPHI + 2048 + lane * 8;
    f32x16 P0 = {}, P1 = {};
    P0 = MFMA(LA(u32x4, wa), b0, P0); P1 = MFMA(LA(u32x4, wa + 2560), b0, P1);
    P0 = MFMA(LA(u32x4, wa + 1024), b1, P0); P1 = MFMA(LA(u32x4, wa + 2560 + 1024), b1, P1);
    P0 = MFMA8(LA(u32x2, wr), b2, P0); P1 = MFMA8(LA(u32x2, wr + 2560), b2, P1);
    float p[18];
#pragma unroll
    for (int i = 0; i < 16; ++i) p[i] = P0[i];
    p[16] = P1[0]; p[17] = P1[1];
    float mx = p[0], mn = p[0];
#pragma unroll
    for (int i = 1; i < 18; ++i) { mx = __builtin_fmaxf(mx, p[i]); mn = __builtin_fminf(mn, p[i]); }
    const float L = 1.4426950408889634f, mxl = -mx * L, mnl = mn * L;
    float ep[18], en[18], sp = 0.f, sn = 0.f;
#pragma unroll
    for (int i = 0; i < 18; ++i) { ep[i] = EX(__builtin_fmaf(p[i], L, mxl)); en[i] = EX(__builtin_fmaf(p[i], -L, mnl)); sp += ep[i]; sn += en[i]; }
    const float rp = __builtin_amdgcn_rcpf(sp), rn = __builtin_amdgcn_rcpf(sn);
#pragma unroll
    for (int u = 0; u < 9; ++u) { out[u] = cvtpk(ep[2 * u] * rp, ep[2 * u + 1] * rp); out[9 + u] = cvtpk(en[2 * u] * rn, en[2 * u + 1] * rn); }
}

struct Ptrs { const char *x, *wqkv, *wphi, *woA, *wq, *wk, *wv, *woB, *gate; char *out, *ws; };

template <bool ISB>
__device__ __forceinline__ void role_main(const Ptrs& P, const int wid, const int lane) {
    const int b = blockIdx.x, x8 = b & 7, r = b >> 3;
    const int ti = 8 * (x8 & 1) + (r & 7), tj = 4 * (x8 >> 1) + (r >> 3);
    const int tq = tj ^ 1;
    const int tb = wid & 3, n = lane & 31, hi = lane >> 5, n16 = lane & 15, g4 = lane >> 4;
    const int tid = wid * 64 + lane;
    char* const ws = P.ws;
    const __amdgpu_buffer_rsrc_t rws = __builtin_amdgcn_make_buffer_rsrc((void*)ws, (short)0, 0x1000000, 0x00020000);
    STAMP(0);
    if (tid == 0) { LW(u32x4, L_CNT) = (u32x4){0u, 0u, 0u, 0u}; LW(u32x4, L_CNT + 16) = (u32x4){0u, 0u, 0u, 0u}; LW(u32x4, L_CNT + 32) = (u32x4){0u, 0u, 0u, 0u}; }
    BAR_L();
    u32x2 Ev;
    unsigned pf = 0;
    const unsigned obf = WS_OBF + (unsigned)(4 * ti + tb) * 36 * 1024, obm = obf + 2 * tq * 1024 + lane * 16;
    const unsigned obr = obf + (32 + (tq >> 2)) * 1024 + (((tq >> 1) & 1) * 32 + n) * 16 + (tq & 1) * 8;
    auto touch_w = [&](int s) {
        const unsigned l = lane < 36 ? lane : 35, cc = l / 12, ty = (l - 12 * cc) / 3, ln = l - 12 * cc - 3 * ty;
        const unsigned row = 64 * (3 * s + cc) + 32 * (x8 & 1) + r;
        u64 p0 = (u64)P.wqkv, p1 = (u64)P.wq;
        asm volatile("" : "+s"(p0), "+s"(p1));
        const u64 ad = (ty < 3 ? p0 + (row * 3456 + ty * 1152) : p1 + row * 1152) + (2 * (x8 >> 1) + ln) * 128;
        asm volatile("global_load_dword %0, %1, off" : "=&v"(pf) : "v"(ad) : "memory");
    };
    unsigned xo[4]; u64 wb[5]; unsigned wst[5];
    auto dma1 = [&](int c, int i) {
        const unsigned slot = (c & 3) * SLOT;
        if (i < 4) dma_s(slot + (4 * tb + i) * 1024, P.x + c * 128, xo[i]);
        else { dma_v(slot + WOFF + (5 * tb + i - 4) * 1024, (const char*)wb[i - 4]); wb[i - 4] += wst[i - 4]; }
    };
    auto issue = [&](int c) {
#pragma unroll
        for (int i = 0; i < 9; ++i) dma1(c, i);
    };
#pragma unroll
    for (int i = 0; i < 4; ++i) xo[i] = (unsigned)(128 * ti + 32 * tb + 8 * i + (lane >> 3)) * 1152u + (((lane & 7) ^ (4 * (i & 1) + (lane >> 4))) * 16);
    if (!ISB) {
        u32x4 xf[5], gf[5];
        __builtin_amdgcn_s_setprio(APRIO);
        const unsigned go = (32 * tb + 8 * g4) * 2, g4o = tb < 2 ? go + 1024 : go;
        {
            const unsigned zo = 0;
            asm volatile("s_mov_b32 exec_lo, 1\n\ts_mov_b32 exec_hi, 0\n\tglobal_load_dwordx2 %0, %6, %9 sc1\n\t"
                         "s_mov_b32 exec_lo, 0x00100010\n\ts_mov_b32 exec_hi, 0x00100010\n\t"
                         "global_load_dwordx4 %1, %7, %10\n\tglobal_load_dwordx4 %2, %7, %10 offset:256\n\tglobal_load_dwordx4 %3, %7, %10 offset:512\n\t"
                         "global_load_dwordx4 %4, %7, %10 offset:768\n\tglobal_load_dwordx4 %5, %8, %10\n\t"
                         "s_mov_b32 exec_lo, -1\n\ts_mov_b32 exec_hi, -1"
                         : "=&v"(Ev), "=&v"(gf[0]), "=&v"(gf[1]), "=&v"(gf[2]), "=&v"(gf[3]), "=&v"(gf[4])
                         : "v"(zo), "v"(go), "v"(g4o), "s"(ws), "s"(P.gate) : "memory");
        }
        {
            const char* xr = P.x + (unsigned)(128 * ti + 8 * tj) * 1152u;
            dma_s(L_XR + tb * 1024, xr + tb * 1024, lane * 16); dma_s(L_XR + (4 + tb) * 1024, xr + (4 + tb) * 1024, lane * 16);
            if (tb == 3) dma_s(L_XR + 8 * 1024, xr + 8 * 1024, lane * 16);
        }
#pragma unroll
        for (int t = 0; t < 10; ++t) {
            const int p = tb + 4 * t;
            if (t != 5 || tb != 0) dma_s(L_WKV + p * 1024, p < 20 ? P.wk + p * 1024 : P.wv + (p * 1024 - 20736), lane * 16);
            else { if (lane < 16) dma_s(L_WKV + 20 * 1024, P.wk + 20 * 1024, lane * 16); else dma_s(L_WKV + 20 * 1024, P.wv - 256, lane * 16); }
        }
        if (tb == 1) { if (lane < 32) dma_s(L_WKV + 40 * 1024, P.wv + (40 * 1024 - 20736), lane * 16); }
        {
            const u32x4 pz = {POISON, POISON, POISON, POISON};
            __builtin_amdgcn_raw_buffer_store_b128(pz, rws, obm, 0, 16);
            __builtin_amdgcn_raw_buffer_store_b128(pz, rws, obm + 1024, 0, 16);
            __builtin_amdgcn_raw_buffer_store_b64((u32x2){POISON, POISON}, rws, obr, 0, 16);
        }
        STAMP(16);
        WAITV(3);
        asm volatile("" : "+v"(Ev), "+v"(gf[0]), "+v"(gf[1]), "+v"(gf[2]), "+v"(gf[3]), "+v"(gf[4]) :: "memory");
        lds_inc(C_RAW, lane); lds_wait<1>(C_RAW, 4, ws);
        STAMP(1);
        if (tb < 2) touch_w(1 + tb);
#pragma unroll
        for (int s = 0; s < 5; ++s) xf[s] = LA(u32x4, L_XR + (n16 & 7) * 1152 + (s < 4 ? go + 256 * s : g4o));
        const int i = lane & 15;
        const unsigned rowa = L_WKV + (32 * tb + 8 * g4 + 2 * (i >> 2)) * 36;
        const unsigned am = rowa + 8 * (i & 3), ar = rowa + (i & 1) * 20736 + 32;
        u32x2 we0[5], we1[5], wr[5], wo0[5], wo1[5];
#pragma unroll
        for (int s = 0; s < 5; ++s) {
            const unsigned o = (s < 4 || tb < 2 ? s : 0) * 128 * 36;
            we0[s] = trd(am + o); we1[s] = trd(am + o + 20736); wr[s] = trd(ar + o); wo0[s] = trd(am + o + 40); wo1[s] = trd(am + o + 40 + 20736);
        }
        SBAR();
#pragma unroll
        for (int s = 0; s < 5; ++s) { PIN(we0[s]); PIN(we1[s]); PIN(wr[s]); PIN(wo0[s]); PIN(wo1[s]); PIN(xf[s]); }
        lds_inc(C_WKVF, lane);
        f32x4 E0 = {}, E1 = {}, E2 = {}, O0 = {}, O1 = {}, O2 = {}, G = {};
#pragma unroll
        for (int s = 0; s < 5; ++s) {
            if (s < 4 || tb < 2) {
                const u32x2 be = {__builtin_amdgcn_perm(xf[s][1], xf[s][0], 0x05040100u), __builtin_amdgcn_perm(xf[s][3], xf[s][2], 0x05040100u)};
                const u32x2 bo = {__builtin_amdgcn_perm(xf[s][1], xf[s][0], 0x07060302u), __builtin_amdgcn_perm(xf[s][3], xf[s][2], 0x07060302u)};
                E0 = MFMA1616(we0[s], be, E0); E1 = MFMA1616(we1[s], be, E1); E2 = MFMA1616(wr[s], be, E2);
                O0 = MFMA1616(wo0[s], bo, O0); O1 = MFMA1616(wo1[s], bo, O1); O2 = MFMA1616(wr[s], bo, O2);
                G = MFMA16(gf[s], xf[s], G);
            }
        }
        const int ra_ = ((lane - 16) & 63) * 4;
        auto rot16 = [&](float v) { return __int_as_float(__builtin_amdgcn_ds_bpermute(ra_, __float_as_int(v))); };
        const float k0 = rot16(O0[2]), k1 = rot16(O0[3]), v0 = rot16(O1[2]), v1 = rot16(O1[3]), w0 = rot16(O2[2]), w1 = rot16(O2[3]);
        const bool g0 = g4 == 0, up = n16 >= 8;
        const f32x4 K = {E0[0] + (g0 ? O2[2] : k0), E0[1] + (g0 ? O2[3] : k1), E0[2] + O0[0], E0[3] + O0[1]};
        const f32x4 V = {E1[0] + (g0 ? w0 : v0), E1[1] + (g0 ? w1 : v1), E1[2] + O1[0], E1[3] + O1[1]};
        lds_wait<1>(C_WKVF, 4, ws);
        const unsigned ra = L_RED + tb * 1280 + (n16 & 7) * 160;
        LW(f32x4, ra + (up ? 64 : 0) + g4 * 16) = up ? V : K;
        const f32x2 rm = g0 ? (f32x2){E2[0] + k0, E2[1] + k1} : (g4 == 3 ? (f32x2){E2[0] + O1[2], E2[1] + O1[3]} : (f32x2){G[0], G[0]});
        if (!up && g4 != 2) LW(f32x2, ra + (g0 ? 128 : (g4 == 3 ? 136 : 144))) = rm;
        __builtin_amdgcn_s_setprio(0);
        STAMP(2);
    } else {
        { const unsigned zo = 0; asm volatile("s_mov_b32 exec_lo, 1\n\ts_mov_b32 exec_hi, 0\n\tglobal_load_dwordx2 %0, %1, %2 sc1\n\ts_mov_b32 exec_lo, -1\n\ts_mov_b32 exec_hi, -1" : "=&v"(Ev) : "v"(zo), "s"(ws) : "memory"); }
        if (tb == 0) touch_w(0);
        const int c0 = 72 * tj - 8 * (tj & 1), c0q = 72 * tq - 8 * (tq & 1);
#pragma unroll
        for (int i = 0; i < 5; ++i) {
            const unsigned q = (5 * tb + i) * 64 + lane, row = q / 20, wi = q - row * 20, ty = wi / 5, pc = wi - ty * 5;
            const bool isq = ty == 3;
            const unsigned rs = isq ? 1152u : 3456u, to = ty == 0 ? 1152u : (ty == 2 ? 2304u : 0u);
            wb[i] = (isq ? (u64)P.wq : (u64)P.wqkv) + (row * rs + to + ((ty & 1) ? c0q : c0) + pc * 16);
            wst[i] = rs * 64;
        }
        STAMP(17);
        issue(0);
        issue(1);
        issue(2);
        STAMP(18);
    }
    f32x16 acc0 = {}, acc1 = {}, acc2 = {};
    unsigned wa[2][3], xk[2][4];
    {
        const int i = lane & 15, sl = 2 * (g4 & 1) + ((i & 3) >> 1);
        const unsigned base = WOFF + (8 * (g4 >> 1) + (i >> 2)) * 320 + 32 * (i & 1), pk = 8 * (tj & 1), pq = 8 * (tq & 1);
        if (ISB) {
            wa[0][0] = base + 240 + 8 * sl + pq;
            wa[0][1] = base + (sl == 0 ? 240 + 32 + pq : 160 + 8 * (sl - 1) + pk);
            wa[0][2] = 0;
        } else {
            wa[0][0] = base + 8 * sl + pk;
            wa[0][1] = base + 80 + 8 * sl + pq;
            wa[0][2] = base + (sl == 0 ? 32 + pk : (sl == 1 ? 80 + 32 + pq : 160 + 24 + 8 * (sl - 2) + pk));
        }
        const unsigned xa = (32 * tb + n) * 128 + ((hi ^ ((n >> 1) & 7)) * 16);
#pragma unroll
        for (int k = 0; k < 4; ++k) { xk[0][k] = xa ^ (k * 32); xk[1][k] = xk[0][k] + 2 * SLOT; PIN(xk[0][k]); PIN(xk[1][k]); }
#pragma unroll
        for (int m = 0; m < (ISB ? 2 : 3); ++m) { wa[1][m] = wa[0][m] + 2 * SLOT; PIN(wa[0][m]); PIN(wa[1][m]); }
    }
#define WPHI_AT 2
    constexpr bool NODMA = false;
#define HM(acc, a, b) acc = MFMA(a, b, acc)
    constexpr bool HRD = true;
    u32x4 FX[4], FW0[4], FW1[4], FW2[4];
    auto rdx = [&](int c, int ks) { return LA(u32x4, xk[(c >> 1) & 1][ks] + (c & 1) * SLOT); };
    auto rdw = [&](int c, int ks, int m) { const unsigned a = wa[(c >> 1) & 1][m] + (c & 1) * SLOT + ks * 5120; return trd2(a, a + 1280); };
    if (!ISB) BAR_L();
    else asm volatile("s_waitcnt vmcnt(18)\n\ts_barrier" : "+v"(Ev), "+v"(pf) :: "memory");
    const u64 tag = mix64(((u64)(unsigned)uni(Ev[1]) << 32) | (unsigned)uni(Ev[0]));
    STAMP(3);
    if (!ISB) {
        if (tb == 2) {
            dma_s(L_WPR, P.wphi + tj * 1296, lane * 16); dma_s(L_WPQ, P.wphi + tq * 1296, lane * 16);
            if (lane < 17) { dma_s(L_WPR + 1024, P.wphi + tj * 1296 + 1024, lane * 16); dma_s(L_WPQ + 1024, P.wphi + tq * 1296 + 1024, lane * 16); }
        }
    }
    auto kv_format = [&]() {
        {
            const int t = lane & 7, bi = tj >> 2, pc = tj & 3;
            const int rho = 16 * (pc >> 1) + 8 * (t >> 2) + 4 * (pc & 1) + (t & 3);
            const int d = lane - 16;
            unsigned base, strd;
            if (lane < 16) { base = t * 40 + 8 * (lane >> 3); strd = 1; }
            else if (lane < 40) { base = d < 16 ? 16 + d : (d < 18 ? 18 + d : 0); strd = 40; }
            else if (lane < 48) { base = t * 40 + 32; strd = 1; }
            else { base = t * 40 + 36; strd = 0; }
            float v[8];
#pragma unroll
            for (int i = 0; i < 8; ++i) {
                const unsigned ad = L_RED + (base + i * strd) * 4;
                v[i] = (LA(float, ad) + LA(float, ad + 1280)) + (LA(float, ad + 2560) + LA(float, ad + 3840));
            }
            float ss = v[0] * v[0] + v[1] * v[1];
            float s6 = 0.f;
#pragma unroll
            for (int i = 2; i < 8; ++i) s6 += v[i] * v[i];
            if (lane < 16) ss += s6;
            u32x4 o = {cvtpk(v[0], v[1]), cvtpk(v[2], v[3]), cvtpk(v[4], v[5]), cvtpk(v[6], v[7])};
            if (lane == 34) o = (u32x4){0x3f803f80u, 0x3f803f80u, 0x3f803f80u, 0x3f803f80u};
            const unsigned img = WS_KVIMG + ti * STEP_BYTES;
            const unsigned o16 = lane < 16 ? img + bi * KB_BYTES + ((lane >> 3) * 32 + rho) * 16 : img + bi * KB_BYTES + 1024 + pc * 304 + d * 16;
            if (lane < 35) __builtin_amdgcn_raw_buffer_store_b128(o, rws, o16, 0, 16);
            float tot = __shfl(ss, t) + __shfl(ss, 8 + t) + __shfl(ss, 40 + t);
            tot = __builtin_fmaxf(tot, dppf<0xb1>(tot)); tot = __builtin_fmaxf(tot, dppf<0x4e>(tot)); tot = __builtin_fmaxf(tot, dppf<0x141>(tot));
            const float gv = __builtin_amdgcn_rcpf(1.f + EX(-1.4426950408889634f * v[0]));
            unsigned o4, d4;
            if (lane < 48) { o4 = img + KR_OFF + (bi >> 1) * 256 + rho * 8 + (bi & 1) * 4; d4 = o[0]; }
            else if (lane < 56) { o4 = WS_G + (128 * ti + 8 * tj + t) * 4; d4 = __float_as_uint(gv); }
            else { o4 = WS_KMAX + (16 * ti + tj) * 4; d4 = __float_as_uint(tot); }
            if (lane >= 40 && lane <= 56) __builtin_amdgcn_raw_buffer_store_b32(d4, rws, o4, 0, 16);
        }
    };
    auto build_wphi = [&](unsigned raw) {
            const int hm = (n >> 2) & 1;
            auto wval = [&](int mb, int pos) {
                const int f = 16 * mb + 4 * (n >> 3) + (n & 3), d = pos - 2 * hi;
                const bool ok = hm == hi && f < 18 && d >= 0 && d < 18;
                const unsigned e = LA(unsigned short, raw + (ok ? (hi * 324 + d * 18 + f) * 2 : 0));
                return ok ? e : 0u;
            };
            if (tb < 2) {
                const int mb = tb;
                unsigned v[16];
#pragma unroll
                for (int pos = 0; pos < 16; ++pos) v[pos] = wval(mb, pos);
                LW(u32x4, L_WPHI + mb * 2560 + lane * 16) = (u32x4){v[0] | (v[1] << 16), v[2] | (v[3] << 16), v[4] | (v[5] << 16), v[6] | (v[7] << 16)};
                LW(u32x4, L_WPHI + mb * 2560 + 1024 + lane * 16) = (u32x4){v[8] | (v[9] << 16), v[10] | (v[11] << 16), v[12] | (v[13] << 16), v[14] | (v[15] << 16)};
            } else {
#pragma unroll
                for (int mb = 0; mb < 2; ++mb)
                    LW(u32x2, L_WPHI + mb * 2560 + 2048 + lane * 8) = (u32x2){wval(mb, 16) | (wval(mb, 17) << 16), wval(mb, 18) | (wval(mb, 19) << 16)};
            }
    };
    SBAR();
#pragma unroll
    for (int ks = 0; ks < 2; ++ks) { FX[ks] = rdx(0, ks); FW0[ks] = rdw(0, ks, 0); FW1[ks] = rdw(0, ks, 1); if (!ISB) FW2[ks] = rdw(0, ks, 2); }
    u32x2 fl0, fl1, fl2, fl3;
    SBAR();
#pragma unroll
    for (int c = 0; c < 9; ++c) {
#pragma unroll
        for (int ks = 0; ks < 4; ++ks) {
            if (ks == 0 && c == 0 && ISB) { lds_wait<1>(C_WKVF, 4, ws); STAMP(28); }
            if (ks == 0 && c == 5 && !ISB) {
                if (wid == 7) {
                    const unsigned fo = WS_FKV + lane * 8;
                    asm volatile("global_load_dwordx2 %0, %4, %5 sc1\n\tglobal_load_dwordx2 %1, %4, %5 offset:512 sc1\n\t"
                                 "global_load_dwordx2 %2, %4, %5 offset:1024 sc1\n\tglobal_load_dwordx2 %3, %4, %5 offset:1536 sc1"
                                 : "=&v"(fl0), "=&v"(fl1), "=&v"(fl2), "=&v"(fl3) : "v"(fo), "s"(ws) : "memory");
                }
            }
            if (ks == 2) {
                if (ISB) {
                    if (c == 0) bar_vl<14>(); else if (c == 1) bar_vl<12>(); else if (c <= 5) bar_vl<8>(); else if (c == 6) bar_vl<5>(); else if (c == 7) bar_vl<10>(); else BAR_L();
                } else if (c == 2) {
                    bar_vl<6>();
                    PIN(pf);
                    if (tb == 3 && lane == 0) put_flag(ws, WS_FKV + (16 * ti + tj) * 8, tag);
                } else if (c == 3 || c == 4) bar_vl<6>();
                else if (c == 5) { if (tb == 3) WAITV(10); else WAITV(6); BAR_L(); }
                else if (c == 6) {
                    if (tb == 3) {
                        asm volatile("s_waitcnt vmcnt(4)" : "+v"(fl0), "+v"(fl1), "+v"(fl2), "+v"(fl3) :: "memory");
                        const unsigned t0 = (unsigned)tag, t1 = (unsigned)(tag >> 32);
                        const bool seen = fl0[0] == t0 && fl0[1] == t1 && fl1[0] == t0 && fl1[1] == t1 && fl2[0] == t0 && fl2[1] == t1 && fl3[0] == t0 && fl3[1] == t1;
                        if (!__all(seen)) poll4(ws, tag, lane);
                        bar_vl<4>();
                        if (b == 0 && lane == 0) __hip_atomic_store((gu64*)ws, tag, __ATOMIC_RELAXED, __HIP_MEMORY_SCOPE_AGENT);
                    } else bar_vl<4>();
                } else if (c == 7) bar_vl<0>();
                else BAR_L();
                if (!ISB && c == 0 && tb == 3) { __builtin_amdgcn_s_setprio(3); kv_format(); __builtin_amdgcn_s_setprio(0); }
                if (!ISB && c == WPHI_AT) { if (tb < 3) build_wphi(L_WPR); }
                if (c == 0) STAMP(20);
                if (c == 1) STAMP(21);
                if (c == 3) STAMP(4);
                if (c == 5) STAMP(22);
                if (c == 7) STAMP(23);
                if (c == 8) STAMP(6);
            }
            const int lc = ks < 2 ? c : c + 1, lk = (ks + 2) & 3;
            const bool ld = lc < 9;
            const int dc = c + 3;
            const int p0 = dc == 3 ? (ks == 0 ? 0 : 2 * ks + 1) : (ISB ? (ks == 0 ? 4 : 5 + ks) : ks);
            const int p1 = NODMA || dc >= 9 || (dc == 3 && !ISB) ? p0 : (dc == 3 ? 2 * ks + 3 : (ISB ? 6 + ks : ks + 1));
            const int pm = p0 + (p1 - p0 + 1) / 2;
            const int ei = 4 * (c - 6) + ks - 2;
            auto early = [&](int e) {
                if (e < 9) { const unsigned p = 37 + tb + 4 * e; dma_s_sc1(p * 1024, ws + WS_KVIMG + p * 1024, lane * 16); }
                else dma_s_sc1(tb == 1 ? L_KMAX : 73 * 1024, ws + (tb == 1 ? WS_KMAX : WS_KVIMG + 73 * 1024), lane * 16);
            };
            HM(acc0, FW0[ks], FX[ks]); SBAR();
            if (ld && HRD) { FX[lk] = rdx(lc, lk); FW0[lk] = rdw(lc, lk, 0); } SBAR();
#pragma unroll
            for (int p = p0; p < pm; ++p) dma1(dc, p);
            if (ISB && ei >= 0 && ei < 4) { early(ei < 2 ? 3 * ei : 2 * ei + 2); if (ei < 2) early(3 * ei + 1); }
            SBAR();
            HM(acc1, FW1[ks], FX[ks]); SBAR();
            if (ld && HRD) FW1[lk] = rdw(lc, lk, 1); SBAR();
#pragma unroll
            for (int p = pm; p < p1; ++p) dma1(dc, p);
            if (ISB && ei >= 0 && ei < 4) early(ei < 2 ? 3 * ei + 2 : 2 * ei + 3);
            SBAR();
            if (!ISB) { HM(acc2, FW2[ks], FX[ks]); SBAR(); if (ld && HRD) FW2[lk] = rdw(lc, lk, 2); SBAR(); }
        }
    }
    STAMP(5);
    const unsigned vrow = L_VB + (32 * tb + n) * 112 + hi * 52 + 8;

    if (ISB) {
#pragma unroll
        for (int u = 0; u < 6; ++u) LW(unsigned, vrow + 4 * u) = cvtpk(acc1[4 + 2 * u], acc1[5 + 2 * u]);
        asm volatile("s_waitcnt vmcnt(0) lgkmcnt(0)" ::: "memory");
        lds_inc(C_PHI, lane); lds_inc(C_LAND0, lane);
        const float gate = __uint_as_float(__builtin_amdgcn_raw_buffer_load_b32(rws, WS_G + (128 * ti + 32 * tb + n) * 4, 0, 16));
        u32x4 QA, QB; u32x2 QRA0, QRA1, QRB0, QRB1; f32x16 MA, MB;
        {
            const float sc = 0.23570226039551584f * 1.4426950408889634f;
            float q[20];
#pragma unroll
            for (int i = 0; i < 16; ++i) q[i] = acc0[i] * sc;
#pragma unroll
            for (int i = 0; i < 4; ++i) q[16 + i] = acc1[i] * sc;
            float n2 = hi ? q[18] * q[18] + q[19] * q[19] : q[0] * q[0] + q[1] * q[1];
#pragma unroll
            for (int i = 2; i < 18; ++i) n2 = __builtin_fmaf(q[i], q[i], n2);
            unsigned cc[10], D[9];
#pragma unroll
            for (int u = 0; u < 10; ++u) cc[u] = cvtpk(q[2 * u], q[2 * u + 1]);
#pragma unroll
            for (int u = 0; u < 9; ++u) D[u] = hi ? cc[u + 1] : cc[u];
#pragma unroll
            for (int u = 0; u < 4; ++u) { const auto sw = __builtin_amdgcn_permlane32_swap(D[u], D[u + 4], false, false); QA[u] = sw[0]; QB[u] = sw[1]; }
            QRA0 = (u32x2){hi ? 0u : D[8], 0u}; QRA1 = (u32x2){0u, hi ? 0u : D[8]};
            QRB0 = (u32x2){hi ? D[8] : 0u, 0u}; QRB1 = (u32x2){0u, hi ? D[8] : 0u};
            SBAR();
            lds_wait<1>(C_LAND0, 4, ws);
            const f32x4 km4 = LA(f32x4, L_KMAX + lane * 16);
            const float km = wave_max(__builtin_fmaxf(__builtin_fmaxf(km4[0], km4[1]), __builtin_fmaxf(km4[2], km4[3])));
            const float m = -1.01f * __builtin_amdgcn_sqrtf(n2 * km);
            const auto sw = __builtin_amdgcn_permlane32_swap(__float_as_uint(m), __float_as_uint(m), false, false);
            const float ma = __uint_as_float(sw[0]), mb = __uint_as_float(sw[1]);
#pragma unroll
            for (int i = 0; i < 16; ++i) { MA[i] = ma; MB[i] = mb; }
            PIN(MA); PIN(MB); PIN(QA); PIN(QB); PIN(QRA0); PIN(QRA1); PIN(QRB0); PIN(QRB1);
        }
        STAMP(7);
        unsigned kp = 4 * STEP_BYTES + lane * 16, rp = 4 * STEP_BYTES + KR_OFF + n * 8, vp = 4 * STEP_BYTES + 1024 + hi * 304 + (n < 18 ? n : 18) * 16;
        if (n > 18) vp = 0x80000;
        PIN(kp); PIN(rp); PIN(vp);
        f32x16 OA = {}, OB = {}, S0A, S0B, S1A, S1B;
        u32x4 KMa, KMb, V0a, V0b, V1a, V1b = {}, P0 = {}, P1 = {}; u32x2 KRX, KRY;
        float e0, e1, e2, e3, f0 = 0.f, f1 = 0.f, f2 = 0.f, f3 = 0.f;
        KRX = LA(u32x2, rp); KMa = LA(u32x4, kp);
        S0A = MFMA(KMa, QA, MA); S0B = MFMA(KMa, QB, MB); S0A = MFMA8(KRX, QRA0, S0A); S0B = MFMA8(KRX, QRB0, S0B);
        SBAR();
        KMb = LA(u32x4, kp + KB_BYTES); V0a = LA(u32x4, vp); V1a = LA(u32x4, vp + 608);
        __builtin_amdgcn_s_waitcnt(0xC07F);
        SBAR();
#define LD(dst, T, p) dst = LA(T, p)
#define NOP_ (void)0
#define SLOTB(MF, POST, d0, d1, d2, d3, S, g, Pq, i, a0, a1, a2, a3) do { MF; SBAR(); POST; SBAR(); \
        d0 = EX(S[4 * (g)]); d1 = EX(S[4 * (g) + 1]); PIN(d0); PIN(d1); SBAR(); \
        Pq[i] = cvtpk(a0, a1); Pq[i + 1] = cvtpk(a2, a3); PIN(Pq); SBAR(); \
        d2 = EX(S[4 * (g) + 2]); d3 = EX(S[4 * (g) + 3]); PIN(d2); PIN(d3); SBAR(); } while (0)
#define E_ e0, e1, e2, e3
#define F_ f0, f1, f2, f3
#define SLOT_(...) SLOTB(__VA_ARGS__)
#define WAITL __builtin_amdgcn_s_waitcnt(0xC07F)
#define PERIOD(SA, SB, NA, NB, KMu, KMl, KRu, QRAx, QRBx, V0u, V1u, V0l, V1l, va, ka, PA3) do { \
        SLOT_(NA = MFMA(KMu, QA, MA),     LD(KMl, u32x4, ka),              E_, SA, 0, P1, 2, F_); \
        SLOT_(OB = MFMA(V1l, P1, OB),     LD(V1l, u32x4, (va) + 608),      F_, SA, 1, P0, 0, E_); \
        SLOT_(NA = MFMA8(KRu, QRAx, NA),  LD(V0l, u32x4, va),              E_, SA, 2, P0, 2, F_); \
        SLOT_(OA = MFMA(V0u, P0, OA),     PA3,                             F_, SA, 3, P1, 0, E_); \
        SLOT_(NB = MFMA(KMu, QB, MB),     NOP_,                            E_, SB, 0, P1, 2, F_); \
        SLOT_(OA = MFMA(V1u, P1, OA),     NOP_,                            F_, SB, 1, P0, 0, E_); \
        SLOT_(NB = MFMA8(KRu, QRBx, NB),  WAITL,                           E_, SB, 2, P0, 2, F_); \
        SLOT_(OB = MFMA(V0u, P0, OB),     NOP_,                            F_, SB, 3, P1, 0, E_); \
    } while (0)
#pragma unroll 1
        for (int seg = 0; seg < 4; ++seg) {
            if (seg == 1) lds_wait<1>(C_LAND1, 4, ws);
            if (seg == 2) { lds_inc(C_BPROG, lane); STAMP(8); }
            if (seg == 3) lds_wait<1>(C_LAND2, 4, ws);
            const int ns = seg == 0 ? 3 : (seg == 1 ? 6 : (seg == 2 ? 2 : 5));
            unsigned dl = seg == 3 ? 0u - 15 * STEP_BYTES : STEP_BYTES;
#pragma unroll 1
            for (int s = 0; s < ns; ++s) {
                unsigned kn = kp + dl, vn = vp + dl; PIN(kn); PIN(vn); SBAR();
                PERIOD(S0A, S0B, S1A, S1B, KMb, KMa, KRX, QRA1, QRB1, V0a, V1a, V0b, V1b, vp + KB_BYTES, kp + 2 * KB_BYTES, LD(KRY, u32x2, rp + 256));
                PERIOD(S1A, S1B, S0A, S0B, KMa, KMb, KRY, QRA0, QRB0, V0b, V1b, V0a, V1a, vp + 2 * KB_BYTES, kp + 3 * KB_BYTES, NOP_);
                rp += dl; PIN(rp); SBAR();
                PERIOD(S0A, S0B, S1A, S1B, KMb, KMa, KRY, QRA1, QRB1, V0a, V1a, V0b, V1b, vp + 3 * KB_BYTES, kn, LD(KRX, u32x2, rp));
                PERIOD(S1A, S1B, S0A, S0B, KMa, KMb, KRX, QRA0, QRB0, V0b, V1b, V0a, V1a, vn, kn + KB_BYTES, NOP_);
                kp = kn; vp = vn; dl = STEP_BYTES;
            }
        }
        P1[2] = cvtpk(f0, f1); P1[3] = cvtpk(f2, f3); PIN(P1); SBAR();
        OB = MFMA(V1b, P1, OB);
        STAMP(10);
        auto rowsum = [&](const f32x16& O) { const auto sw = __builtin_amdgcn_permlane32_swap(__float_as_uint(O[10]), __float_as_uint(O[10]), false, false); return __uint_as_float(sw[0]); };
        float lA = rowsum(OA), lB = rowsum(OB);
        if (__any(!(lA >= 0x1p-80f) || !(lB >= 0x1p-80f))) {
            const unsigned vo = WS_KVIMG + 1024 + hi * 304 + (n < 18 ? n : 18) * 16;
#pragma unroll 1
            for (int which = 0; which < 2; ++which) {
                const u32x4 Q = which ? QB : QA; const u32x2 R0 = which ? QRB0 : QRA0, R1 = which ? QRB1 : QRA1;
                float mx = -__builtin_inff();
#pragma unroll 1
                for (int kb = 0; kb < 64; ++kb) {
                    const unsigned bo = (kb >> 2) * STEP_BYTES;
                    const u32x4 km = __builtin_bit_cast(u32x4, __builtin_amdgcn_raw_buffer_load_b128(rws, WS_KVIMG + bo + (kb & 3) * KB_BYTES + lane * 16, 0, 16));
                    const u32x2 kr = __builtin_bit_cast(u32x2, __builtin_amdgcn_raw_buffer_load_b64(rws, WS_KVIMG + bo + KR_OFF + ((kb >> 1) & 1) * 256 + n * 8, 0, 16));
                    f32x16 S = {}; S = MFMA(km, Q, S); S = MFMA8(kr, (kb & 1) ? R1 : R0, S);
#pragma unroll
                    for (int i = 0; i < 16; ++i) mx = __builtin_fmaxf(mx, S[i]);
                }
                { const auto sw = __builtin_amdgcn_permlane32_swap(__float_as_uint(mx), __float_as_uint(mx), false, false); mx = __builtin_fmaxf(__uint_as_float(sw[0]), __uint_as_float(sw[1])); }
                f32x16 O = {}, C;
#pragma unroll
                for (int i = 0; i < 16; ++i) C[i] = -mx;
#pragma unroll 1
                for (int kb = 0; kb < 64; ++kb) {
                    const unsigned bo = (kb >> 2) * STEP_BYTES, bb = bo + (kb & 3) * KB_BYTES;
                    const u32x4 km = __builtin_bit_cast(u32x4, __builtin_amdgcn_raw_buffer_load_b128(rws, WS_KVIMG + bb + lane * 16, 0, 16));
                    const u32x2 kr = __builtin_bit_cast(u32x2, __builtin_amdgcn_raw_buffer_load_b64(rws, WS_KVIMG + bo + KR_OFF + ((kb >> 1) & 1) * 256 + n * 8, 0, 16));
                    const u32x4 v0 = __builtin_bit_cast(u32x4, __builtin_amdgcn_raw_buffer_load_b128(rws, vo + bb, 0, 16));
                    const u32x4 v1 = __builtin_bit_cast(u32x4, __builtin_amdgcn_raw_buffer_load_b128(rws, vo + bb + 608, 0, 16));
                    f32x16 S = MFMA(km, Q, C); S = MFMA8(kr, (kb & 1) ? R1 : R0, S);
                    float p[16];
#pragma unroll
                    for (int i = 0; i < 16; ++i) p[i] = EX(S[i]);
                    const u32x4 p0 = {cvtpk(p[0], p[1]), cvtpk(p[2], p[3]), cvtpk(p[4], p[5]), cvtpk(p[6], p[7])};
                    const u32x4 p1 = {cvtpk(p[8], p[9]), cvtpk(p[10], p[11]), cvtpk(p[12], p[13]), cvtpk(p[14], p[15])};
                    O = MFMA(v0, p0, O); O = MFMA(v1, p1, O);
                }
                const float lo = rowsum(O);
                if (which) { OB = O; lB = lo; } else { OA = O; lA = lo; }
            }
        }
        {
            const float ga = (1.f - gate) * __builtin_amdgcn_rcpf(lA), gb = (1.f - gate) * __builtin_amdgcn_rcpf(lB);
            auto word = [&](float lo, float hi_) { const unsigned w = cvtpk(lo, hi_); return w < POISON ? w : POISON - 1; };
            const u32x4 fa = {word(OA[0] * ga, OA[1] * ga), word(OA[2] * ga, OA[3] * ga), word(OA[4] * ga, OA[5] * ga), word(OA[6] * ga, OA[7] * ga)};
            const u32x4 fb = {word(OB[0] * gb, OB[1] * gb), word(OB[2] * gb, OB[3] * gb), word(OB[4] * gb, OB[5] * gb), word(OB[6] * gb, OB[7] * gb)};
            const u32x2 rr = {word(OA[8] * ga, OA[9] * ga), word(OB[8] * gb, OB[9] * gb)};
            __builtin_amdgcn_raw_buffer_store_b128(fa, rws, obm, 0, 16);
            __builtin_amdgcn_raw_buffer_store_b128(fb, rws, obm + 1024, 0, 16);
            if (!hi) __builtin_amdgcn_raw_buffer_store_b64(rr, rws, obr, 0, 16);
            lds_inc(C_BDONE, lane);
        }
        STAMP(11);
    } else {
        __builtin_amdgcn_s_setprio(1);
        {
            unsigned ph[18];
            features(acc0, acc2[0], acc2[1], acc2[2], acc2[3], lane, ph);
            const unsigned pa = L_PHIK + (32 * tb + n) * 160 + hi * 80;
#pragma unroll
            for (int u = 0; u < 4; ++u) LW(u32x4, pa + 16 * u) = (u32x4){ph[4 * u], ph[4 * u + 1], ph[4 * u + 2], ph[4 * u + 3]};
            LW(u32x2, pa + 64) = (u32x2){ph[16], ph[17]};
            LW(unsigned, vrow + 24) = cvtpk(acc2[8], acc2[9]); LW(unsigned, vrow + 28) = cvtpk(acc2[10], acc2[11]);
            LW(unsigned, vrow + 32) = cvtpk(acc2[12], acc2[13]); LW(unsigned, vrow + 36) = cvtpk(acc2[14], acc2[15]);
            LW(unsigned, vrow + 36 + 4 * hi) = 0x00003f80u;
            asm volatile("s_waitcnt lgkmcnt(0)" ::: "memory");
            lds_inc(C_PHI, lane);
        }
#pragma unroll 1
        for (int i = 0; i < 19; ++i) { const int p = 74 + tb + 4 * i < 147 ? 74 + tb + 4 * i : 147; dma_s_sc1(p * 1024, ws + WS_KVIMG + p * 1024, lane * 16); }
        const float gate = __uint_as_float(__builtin_amdgcn_raw_buffer_load_b32(rws, WS_G + (128 * ti + 32 * tb + n) * 4, 0, 16));
        STAMP(7);
        lds_wait<2>(C_PHI, 8, ws);
        __builtin_amdgcn_s_setprio(2);
        if (tb < 3) build_wphi(L_WPQ);
        unsigned gen = 0;
        {
            const int hh = tb & 1, i = lane & 15;
            const int tok = 8 * g4 + (i >> 2), gr = i & 3;
            const unsigned va = L_VB + tok * 112 + hh * 56 + 8 + gr * 8;
            const int ntask = tb < 2 ? 2 : 1;
#pragma unroll 1
            for (int t = 0; t < ntask; ++t) {
                const int mbf = tb < 2 ? 2 * t : 1;
                const unsigned pa = L_PHIK + tok * 160 + hh * 80 + mbf * 32 + gr * 8;
                f32x4 d0 = {}, d1 = {};
#pragma unroll
                for (int ks = 0; ks < 4; ++ks) {
                    const u32x4 a = trd2(pa + ks * 32 * 160, pa + ks * 32 * 160 + 4 * 160);
                    const u32x4 b0 = trd2(va + ks * 32 * 112, va + ks * 32 * 112 + 4 * 112);
                    const u32x4 b1 = trd2(va + ks * 32 * 112 + 32, va + ks * 32 * 112 + 4 * 112 + 32);
                    d0 = MFMA16(a, b0, d0); d1 = MFMA16(a, b1, d1);
                }
                const unsigned ko = WS_KVP + (unsigned)(16 * tj + ti) * 5472 + hh * 2736 + (16 * mbf + 4 * g4) * 4;
                if (mbf < 2 || g4 == 0) {
                    __builtin_amdgcn_raw_buffer_store_b128(__builtin_bit_cast(u32x4, d0), rws, ko + n16 * 144, 0, 16);
                    if (n16 < 3) __builtin_amdgcn_raw_buffer_store_b128(__builtin_bit_cast(u32x4, d1), rws, ko + (16 + n16) * 144, 0, 16);
                }
            }
        }
        WAITV(0);
        lds_inc(C_LAND1, lane);
        async_bar(gen, lane, ws);
        if (tb == 0 && lane == 0) put_flag(ws, WS_FKVP + (16 * tj + ti) * 8, tag);
        STAMP(8);
        if (tb == 3) {
            const unsigned l0 = b * 21 + lane % 21;
            u64 p2 = (u64)P.woA, p3 = (u64)P.woB;
            asm volatile("" : "+s"(p2), "+s"(p3));
            const u64 ad = (lane < 21 ? p2 : p3) + (l0 < 5183 ? l0 : 5183) * 128;
            asm volatile("global_load_dword %0, %1, off" : "=&v"(pf) : "v"(ad) : "memory");
        }
#define OTOUCH 2
        unsigned pg = 0;
        if (OTOUCH == 1 && tb == 2) {
            const unsigned ro = (unsigned)(64 * (4 * x8 + (r & 3)) + (lane & 7) * 9) * 1152 + (r >> 2) * 144;
            if (lane < 8) asm volatile("global_load_dword %0, %1, %2" : "=&v"(pf) : "v"(ro), "s"(P.out) : "memory");
        }
        if (OTOUCH == 2 && tb == 2) {
            const unsigned ro = (unsigned)(64 * (4 * x8 + (r & 3)) + lane) * 1152 + (r >> 2) * 144;
            asm volatile("global_load_dword %0, %2, %3\n\tglobal_load_dword %1, %2, %3 offset:140" : "=&v"(pf), "=&v"(pg) : "v"(ro), "s"(P.out) : "memory");
        }
        for (int p = tb; p < 37; p += 4) dma_s_sc1(p * 1024, ws + WS_KVIMG + p * 1024, lane * 16);
        unsigned pq[18];
        features(acc1, acc2[4], acc2[5], acc2[6], acc2[7], lane, pq);
        WAITV(0);
        PIN(pf); PIN(pg);
        lds_inc(C_LAND2, lane);
        poll_flags(ws, WS_FKVP + 16 * tq * 8, 16, tag, lane, 1);
        STAMP(9);
        {
            const int at = tb * 64 + lane;
#pragma unroll 1
            for (int rd = 0; rd < 2; ++rd) {
                const int it = rd == 0 ? at : 256 + tb * 22 + lane;
                const bool ok = rd == 0 || (lane < 22 && it < 342);
                const unsigned ko = WS_KVP + (unsigned)(16 * tq) * 5472 + (ok ? it : 0) * 16;
                f32x4 v[16];
#pragma unroll
                for (int i = 0; i < 16; ++i) v[i] = __builtin_bit_cast(f32x4, __builtin_amdgcn_raw_buffer_load_b128(rws, ko + i * 5472, 0, 16));
                f32x4 s = v[0];
#pragma unroll
                for (int i = 1; i < 16; ++i) s += v[i];
                const int hh = it >= 171, rem = it - 171 * hh, d = rem / 9, fq = rem - 9 * d;
                if (ok) LW(u32x2, L_KVT + hh * 1600 + d * 80 + fq * 8) = (u32x2){cvtpk(s[0], s[1]), cvtpk(s[2], s[3])};
            }
            if (tb == 3 && lane < 10) LW(u32x4, L_KVT + (lane >= 5) * 1600 + 19 * 80 + (lane % 5) * 16) = (u32x4){0u, 0u, 0u, 0u};
        }
        async_bar(gen, lane, ws);
        {
            const int hm = (n >> 2) & 1, d0 = 4 * (n >> 3) + (n & 3);
            const unsigned a0 = L_KVT + (hm == hi ? hm * 1600 + d0 * 80 : 19 * 80);
            const unsigned a1 = L_KVT + (hm == hi && d0 < 3 ? hm * 1600 + (16 + d0) * 80 : 19 * 80);
            f32x16 N0 = {}, N1 = {};
#pragma unroll
            for (int s = 0; s < 4; ++s) {
                const u32x4 bq = {pq[4 * s], pq[4 * s + 1], pq[4 * s + 2], pq[4 * s + 3]};
                N0 = MFMA(LA(u32x4, a0 + 16 * s), bq, N0); N1 = MFMA(LA(u32x4, a1 + 16 * s), bq, N1);
            }
            const u32x2 br = {pq[16], pq[17]};
            N0 = MFMA8(LA(u32x2, a0 + 64), br, N0); N1 = MFMA8(LA(u32x2, a1 + 64), br, N1);
            const float sc = gate / (N1[2] + 1e-6f);
            const unsigned fo = WS_OAF + (unsigned)(4 * ti + tb) * 36 * 1024;
            const u32x4 f0 = {cvtpk(N0[0] * sc, N0[1] * sc), cvtpk(N0[2] * sc, N0[3] * sc), cvtpk(N0[4] * sc, N0[5] * sc), cvtpk(N0[6] * sc, N0[7] * sc)};
            const u32x4 f1 = {cvtpk(N0[8] * sc, N0[9] * sc), cvtpk(N0[10] * sc, N0[11] * sc), cvtpk(N0[12] * sc, N0[13] * sc), cvtpk(N0[14] * sc, N0[15] * sc)};
            __builtin_amdgcn_raw_buffer_store_b128(f0, rws, fo + (2 * tq) * 1024 + lane * 16, 0, 16);
            __builtin_amdgcn_raw_buffer_store_b128(f1, rws, fo + (2 * tq + 1) * 1024 + lane * 16, 0, 16);
            __builtin_amdgcn_raw_buffer_store_b32(cvtpk(N1[0] * sc, N1[1] * sc), rws, fo + (32 + (tq >> 2)) * 1024 + (((tq >> 1) & 1) * 32 + n) * 16 + (2 * (tq & 1) + hi) * 4, 0, 16);
        }
        WAITV(0);
        async_bar(gen, lane, ws);
        if (tb == 0 && lane == 0) put_flag(ws, WS_FOA + (16 * ti + tq) * 8, tag);
        STAMP(10);
        const int tt = 4 * x8 + (r & 3), cg = r >> 2;
        unsigned wo[9];
#pragma unroll
        for (int i = 0; i < 9; ++i) { const unsigned q = (tb + 4 * i) * 64 + lane, row = q / 9; wo[i] = row * 1152 + (q - row * 9) * 16; }
        auto stage = [&](const char* w) {
            const char* wsl = w + cg * 144;
#pragma unroll
            for (int i = 0; i < 21; ++i) {
                const int p = tb + 4 * i;
                if (i < 20 || tb == 0) dma_s(L_WOS + p * 1024, wsl + (i / 9) * 256 * 1152, wo[i % 9]);
            }
        };
        const int i16 = lane & 15, gr = i16 & 3, hk = g4 & 1, fh = g4 >> 1;
        const unsigned fro = (unsigned)(2 * tt + (tb >> 1)) * 36 * 1024 + fh * 1024 + (hk * 32 + n16 + 16 * (tb & 1)) * 16;
        const unsigned remo = L_WOS + (18 * (8 * fh + 4 * hk + (i16 >> 3)) + 16 + ((i16 >> 2) & 1)) * 144 + gr * 8;
        f32x4 T[5] = {};
        auto wfrag = [&](int ks, int cb, unsigned maino, unsigned r4s) {
            const unsigned a = (ks < 16 ? maino + ks * 36 * 144 : remo + (ks - 16) * 16 * 18 * 144) + cb * 32;
            return trd2(a, a + (ks < 16 ? r4s : 2 * 18 * 144));
        };
        auto gemm = [&](const u32x4 (&bf)[18], unsigned maino, unsigned r4s, auto ord) {
            u32x4 wf[2][5];
#pragma unroll
            for (int cb = 0; cb < 5; ++cb) wf[0][cb] = wfrag(ord(0), cb, maino, r4s);
            SBAR();
#pragma unroll
            for (int q = 0; q < 18; ++q) {
#pragma unroll
                for (int cb = 0; cb < 5; ++cb) {
                    T[cb] = MFMA16(wf[q & 1][cb], bf[q], T[cb]); SBAR();
                    if (q < 17) wf[(q + 1) & 1][cb] = wfrag(ord(q + 1), cb, maino, r4s);
                    SBAR();
                }
            }
        };
        u32x4 bf[18];
        poll_flags(ws, WS_FOA + 16 * (tt >> 1) * 8, 16, tag, lane, 1);
#pragma unroll
        for (int ks = 0; ks < 18; ++ks) bf[ks] = __builtin_bit_cast(u32x4, __builtin_amdgcn_raw_buffer_load_b128(rws, WS_OAF + fro + ks * 2048, 0, 16));
        SBAR();
        lds_wait<16>(C_BPROG, 4, ws);
        stage(P.woA);
        WAITV(0);
        async_bar(gen, lane, ws);
        STAMP(11);
        gemm(bf, L_WOS + (18 * hk + 8 * fh + (i16 >> 2)) * 144 + gr * 8, 4 * 144, [](int q) { return q; });
        async_bar(gen, lane, ws);
        STAMP(12);
        stage(P.woB);
        WAITV(0);
        async_bar(gen, lane, ws);
        STAMP(13);
        {
            const unsigned maino = L_WOS + (18 * fh + 4 * hk + (i16 >> 2)) * 144 + gr * 8;
            const unsigned remg = L_WOS + (36 * g4 + 16 + 18 * (i16 >> 3) + ((i16 >> 2) & 1)) * 144 + gr * 8;
            const unsigned rro = WS_OBF + (unsigned)(2 * tt + (tb >> 1)) * 36 * 1024 + 32 * 1024 + ((g4 >> 1) * 32 + 16 * (tb & 1) + n16) * 16 + (g4 & 1) * 8;
            f32x4 G[4][5];
#pragma unroll
            for (int cb = 0; cb < 5; ++cb) { G[0][cb] = T[cb]; G[1][cb] = (f32x4){}; G[2][cb] = (f32x4){}; G[3][cb] = (f32x4){}; }
            u32x2 br[4];
            unsigned have = 0, done = 0;
            bool bdone = false;
#pragma unroll 1
            for (unsigned s = 0;; ++s) {
#pragma unroll
                for (int g = 0; g < 4; ++g) {
                    if (!((have >> g) & 1)) {
#pragma unroll
                        for (int u = 0; u < 4; ++u) bf[4 * g + u] = __builtin_bit_cast(u32x4, __builtin_amdgcn_raw_buffer_load_b128(rws, WS_OBF + fro + (4 * g + u) * 2048, 0, 16));
                        br[g] = __builtin_bit_cast(u32x2, __builtin_amdgcn_raw_buffer_load_b64(rws, rro + g * 1024, 0, 16));
                    }
                }
#pragma unroll
                for (int g = 0; g < 4; ++g) {
                    if (!((have >> g) & 1)) {
                        unsigned m = br[g][0] > br[g][1] ? br[g][0] : br[g][1];
#pragma unroll
                        for (int u = 0; u < 4; ++u) {
                            const u32x4 w = bf[4 * g + u];
                            const unsigned m0 = w[0] > w[1] ? w[0] : w[1], m1 = w[2] > w[3] ? w[2] : w[3];
                            m = m > m0 ? m : m0; m = m > m1 ? m : m1;
                        }
                        if (__all(m != POISON)) have |= 1u << g;
                    }
                }
                if (!bdone) { bdone = uni(__hip_atomic_load((AS3 unsigned*)(uintptr_t)C_BDONE, __ATOMIC_RELAXED, __HIP_MEMORY_SCOPE_WORKGROUP)) >= 4; if (bdone) STAMP(14); }
                if (bdone) {
#pragma unroll
                    for (int g = 0; g < 4; ++g) {
                        if (((have & ~done) >> g) & 1) {
                            u32x4 wf[2][5]; u32x2 wr[5];
#pragma unroll
                            for (int cb = 0; cb < 5; ++cb) wf[0][cb] = wfrag(4 * g, cb, maino, 8 * 144);
                            SBAR();
#pragma unroll
                            for (int u = 0; u < 4; ++u) {
#pragma unroll
                                for (int cb = 0; cb < 5; ++cb) {
                                    G[g][cb] = MFMA16(wf[u & 1][cb], bf[4 * g + u], G[g][cb]); SBAR();
                                    if (u < 3) wf[(u + 1) & 1][cb] = wfrag(4 * g + u + 1, cb, maino, 8 * 144);
                                    else wr[cb] = trd(remg + g * 144 * 144 + cb * 32);
                                    SBAR();
                                }
                            }
#pragma unroll
                            for (int cb = 0; cb < 5; ++cb) G[g][cb] = MFMA1616(wr[cb], br[g], G[g][cb]);
                            done |= 1u << g;
                        }
                    }
                    if (done == 15) {
                        break;
                    }
                }
                if (s > SPIN_MAX) { if (lane == 0) __hip_atomic_store((gu32*)(ws + WS_TMO), 0x300u, __ATOMIC_RELAXED, __HIP_MEMORY_SCOPE_AGENT); break; }
                if (bdone) __builtin_amdgcn_s_sleep(1); else __builtin_amdgcn_s_sleep(16);
            }
#pragma unroll
            for (int cb = 0; cb < 5; ++cb) T[cb] = (G[0][cb] + G[1][cb]) + (G[2][cb] + G[3][cb]);
        }
        STAMP(19);
#define OUTAUX 0
        const __amdgpu_buffer_rsrc_t rout = __builtin_amdgcn_make_buffer_rsrc((void*)P.out, (short)0, 2048 * 1152, 0x00020000);
        const unsigned op = (unsigned)(64 * tt + 16 * tb + n16) * 1152 + cg * 144 + g4 * 8;
#pragma unroll
        for (int cb = 0; cb < 5; ++cb) {
            const u32x2 o = {cvtpk(T[cb][0], T[cb][1]), cvtpk(T[cb][2], T[cb][3])};
            if (cb < 4 || g4 < 2) __builtin_amdgcn_raw_buffer_store_b64(o, rout, op + cb * 32, 0, OUTAUX);
        }
    }
}

__global__ __launch_bounds__(512, 2) void gated_mix_kernel(const void* x, const void* wqkv, const void* wphi, const void* woA, const void* wq, const void* wk,
                                                           const void* wv, const void* woB, const void* gate, void* out, void* ws) {
    extern __shared__ __attribute__((aligned(16))) char lds[];
    const int lane = threadIdx.x & 63, wid = uni(threadIdx.x >> 6);
    const Ptrs P = {(const char*)x, (const char*)wqkv, (const char*)wphi, (const char*)woA, (const char*)wq, (const char*)wk, (const char*)wv, (const char*)woB, (const char*)gate, (char*)out, (char*)ws};
    if (wid < 4) role_main<true>(P, wid, lane); else role_main<false>(P, wid, lane);
}
}

extern "C" void kernel_launch(void* const* d_in, const int* in_sizes, int n_in, void* d_out, int out_size, void* d_ws, size_t ws_size, hipStream_t stream) {
    (void)in_sizes; (void)n_in; (void)out_size; (void)ws_size;
    (void)hipFuncSetAttribute((const void*)fk::gated_mix_kernel, hipFuncAttributeMaxDynamicSharedMemorySize, fk::LDS_BYTES);
    hipLaunchKernelGGL(fk::gated_mix_kernel, dim3(256), dim3(512), fk::LDS_BYTES, stream,
                       (const void*)d_in[0], (const void*)d_in[1], (const void*)d_in[2], (const void*)d_in[3], (const void*)d_in[5], (const void*)d_in[6],
                       (const void*)d_in[7], (const void*)d_in[8], (const void*)d_in[9], d_out, d_ws);
}
```

```cpp
#include <hip/hip_runtime.h>
#include <hip/hip_bf16.h>
#include <stdint.h>

namespace fk {
typedef __bf16 bf16x2 __attribute__((ext_vector_type(2)));
typedef __bf16 bf16x8 __attribute__((ext_vector_type(8)));
typedef short s16x4 __attribute__((ext_vector_type(4)));
typedef float f32x2 __attribute__((ext_vector_type(2)));
typedef float f32x4 __attribute__((ext_vector_type(4)));
typedef float f32x16 __attribute__((ext_vector_type(16)));
typedef unsigned u32x2 __attribute__((ext_vector_type(2)));
typedef unsigned u32x4 __attribute__((ext_vector_type(4)));
typedef unsigned long long u64;
typedef __attribute__((address_space(1))) u64 gu64;
typedef __attribute__((address_space(1))) unsigned gu32;

#define AS3 __attribute__((address_space(3)))
#define SBAR() __builtin_amdgcn_sched_barrier(0)
#define PIN(x) asm volatile("" : "+v"(x))
#define MFMA(a, b, c) __builtin_amdgcn_mfma_f32_32x32x16_bf16(__builtin_bit_cast(bf16x8, a), __builtin_bit_cast(bf16x8, b), c, 0, 0, 0)
#define MFMA8(a, b, c) __builtin_amdgcn_mfma_f32_32x32x8bf16_1k(__builtin_bit_cast(s16x4, a), __builtin_bit_cast(s16x4, b), c, 0, 0, 0)
#define MFMA1616(a, b, c) __builtin_amdgcn_mfma_f32_16x16x16bf16_1k(__builtin_bit_cast(s16x4, a), __builtin_bit_cast(s16x4, b), c, 0, 0, 0)
#define MFMA16(a, b, c) __builtin_amdgcn_mfma_f32_16x16x32_bf16(__builtin_bit_cast(bf16x8, a), __builtin_bit_cast(bf16x8, b), c, 0, 0, 0)
#define LA(T, a) (*(const AS3 T*)(uintptr_t)(a))
#define LW(T, a) (*(AS3 T*)(uintptr_t)(a))
#define PFV 0
#define CT 8
#define TT 0
#define SR 0
#define PW 1
#define BC 1
#define AB 1
#define QP 3
#define QI 1
#define QH 1
#define WH 1
#define SG 1
#define EX1 1
#define CK1 1
#define PF 1
#define PG 1
#define FI 1
#define HA 1
#define GL 0
#define CTB 1
#define RO 6
#define RS2 0
#define RS0 0
#define RS1 20
#define BTOUCH 0
#define OVL 1
#define PKF 1
#define HS 0
#define TE 1
#define KF 0
#define GP 1
#define EF 0
#define BPS 16
#define SPL 1
#define PK 2
#define PQ 0
#define PR 2
#define PO 2
#define LE 1
#define MR 1
#define NF 0
#define NFS 8
#define TW 0
#define BW 1
#define ONEP 1
#define RF 1
#define BD 10
#define HINTS 0
#define HPS 0
#define TIGHT 12
#define OLDS 1
#define BPRIO 3
#define APRIO 3
#define FENCE() asm volatile("" ::: "memory")
#define EX(x) __builtin_amdgcn_exp2f(x)

#define WS_TMO 64
#define FLS 128
constexpr unsigned WS_FKV = 4096, WS_FKVP = FLS == 8 ? 8192 : 0x1A0000, WS_FOA = FLS == 8 ? 12288 : 0x1B0000;
#define WS_HINT 0x1C0000
#define WS_G 32768
#define WS_KMAX 40960
#define WS_KVIMG 65536
#define WS_KVP 262144
#define WS_OAF (2 << 20)
#define WS_OBF (5 << 20)

constexpr unsigned SLOT = 36864, WOFF = 16384;
constexpr unsigned L_WKV = 110592;
constexpr unsigned L_XR = 152064, L_RED = 152064;
constexpr unsigned L_CNT = 163792, L_WPR = 157184, L_WPQ = 158480;
constexpr unsigned L_WPHI = 152064, L_ZERO = 160384, L_KVT = 157184;
constexpr unsigned L_PHIK = 0, L_VB = 20480;
constexpr unsigned L_KMAX = 161152, L_WOS = 37888;
constexpr unsigned KB_BYTES = 2240, STEP_BYTES = 9472, KR_OFF = 8960;
constexpr unsigned LDS_BYTES = 163840;
constexpr unsigned C_LAND0 = L_CNT, C_LAND1 = L_CNT + 4, C_LAND2 = L_CNT + 8, C_PHI = L_CNT + 12, C_ASY = L_CNT + 16, C_BPROG = L_CNT + 20, C_BDONE = L_CNT + 24, C_RAW = L_CNT + 28, C_WKVF = L_CNT + 32, C_BPROG1 = L_CNT + 36, C_F1 = L_CNT + 40, C_F2 = L_CNT + 44;
constexpr unsigned POISON = 0xffffffffu;

#define STAMP(k) do { } while (0)

__device__ __forceinline__ unsigned cvtpk(float lo, float hi) { f32x2 v = {lo, hi}; return __builtin_bit_cast(unsigned, __builtin_convertvector(v, bf16x2)); }
__device__ __forceinline__ int uni(int v) { return __builtin_amdgcn_readfirstlane(v); }

__device__ __forceinline__ void dma_s(unsigned lds, const void* sbase, unsigned voff) {
    asm volatile("s_mov_b32 m0, %0\n\ts_nop 0\n\tglobal_load_lds_dwordx4 %1, %2" :: "s"(lds), "v"(voff), "s"(sbase) : "memory", "m0");
}
__device__ __forceinline__ void dma_s_sc1(unsigned lds, const void* sbase, unsigned voff) {
    asm volatile("s_mov_b32 m0, %0\n\ts_nop 0\n\tglobal_load_lds_dwordx4 %1, %2 sc1" :: "s"(lds), "v"(voff), "s"(sbase) : "memory", "m0");
}
__device__ __forceinline__ void dma_v(unsigned lds, const void* vaddr) {
    asm volatile("s_mov_b32 m0, %0\n\ts_nop 0\n\tglobal_load_lds_dwordx4 %1, off" :: "s"(lds), "v"(vaddr) : "memory", "m0");
}
#define WAITV(n) asm volatile("s_waitcnt vmcnt(" #n ")" ::: "memory")
#define BAR_L() asm volatile("s_waitcnt lgkmcnt(0)\n\ts_barrier" ::: "memory")
template <int N> __device__ __forceinline__ void waitv() { asm volatile("s_waitcnt vmcnt(%0)" :: "n"(N) : "memory"); }
template <int N> __device__ __forceinline__ void bar_vl() { asm volatile("s_waitcnt vmcnt(%0) lgkmcnt(0)\n\ts_barrier" :: "n"(N) : "memory"); }

__device__ __forceinline__ u32x2 trd(unsigned a) { return __builtin_bit_cast(u32x2, __builtin_amdgcn_ds_read_tr16_b64_v4i16((AS3 s16x4*)(uintptr_t)a)); }
__device__ __forceinline__ u32x4 trd2(unsigned a0, unsigned a1) { const u32x2 p = trd(a0), q = trd(a1); return (u32x4){p[0], p[1], q[0], q[1]}; }

template <int CTRL> __device__ __forceinline__ float dppf(float v) { return __builtin_bit_cast(float, __builtin_amdgcn_update_dpp(0, __builtin_bit_cast(int, v), CTRL, 0xf, 0xf, true)); }
template <int CTRL> __device__ __forceinline__ unsigned dppmax(unsigned v) { return __builtin_elementwise_max(v, (unsigned)__builtin_amdgcn_update_dpp(0, (int)v, CTRL, 0xf, 0xf, true)); }
__device__ __forceinline__ unsigned wave_umax(unsigned v) {
    v = dppmax<0x121>(v); v = dppmax<0x122>(v); v = dppmax<0x124>(v); v = dppmax<0x128>(v);
    { const auto t = __builtin_amdgcn_permlane16_swap(v, v, false, false); v = __builtin_elementwise_max((unsigned)t[0], (unsigned)t[1]); }
    { const auto t = __builtin_amdgcn_permlane32_swap(v, v, false, false); v = __builtin_elementwise_max((unsigned)t[0], (unsigned)t[1]); }
    return v;
}
__device__ __forceinline__ float wave_max(float v) {
    v = __builtin_fmaxf(v, dppf<0x121>(v)); v = __builtin_fmaxf(v, dppf<0x122>(v)); v = __builtin_fmaxf(v, dppf<0x124>(v)); v = __builtin_fmaxf(v, dppf<0x128>(v));
    { const auto t = __builtin_amdgcn_permlane16_swap(__float_as_uint(v), __float_as_uint(v), false, false); v = __builtin_fmaxf(__uint_as_float(t[0]), __uint_as_float(t[1])); }
    { const auto t = __builtin_amdgcn_permlane32_swap(__float_as_uint(v), __float_as_uint(v), false, false); v = __builtin_fmaxf(__uint_as_float(t[0]), __uint_as_float(t[1])); }
    return v;
}
__device__ __forceinline__ u64 mix64(u64 z) { z += 0x9E3779B97F4A7C15ull; z = (z ^ (z >> 30)) * 0xBF58476D1CE4E5B9ull; z = (z ^ (z >> 27)) * 0x94D049BB133111EBull; return z ^ (z >> 31); }

constexpr unsigned SPIN_MAX = 1u << 20;
__device__ __forceinline__ void poll_flags(const char* ws, unsigned off, int n, u64 tag, int lane, int slp) {
    const gu64* p = (const gu64*)(ws + off + (lane < n ? lane : 0) * FLS);
    for (unsigned s = 0;; ++s) {
        const u64 v = __hip_atomic_load(p, __ATOMIC_RELAXED, __HIP_MEMORY_SCOPE_AGENT);
        if (__all(v == tag)) break;
        if (s > SPIN_MAX) { if (lane == 0) __hip_atomic_store((gu32*)(ws + WS_TMO), 0x100u + off, __ATOMIC_RELAXED, __HIP_MEMORY_SCOPE_AGENT); break; }
        if (slp) __builtin_amdgcn_s_sleep(8); else __builtin_amdgcn_s_sleep(1);
    }
    FENCE();
}
__device__ __forceinline__ void poll_at(const char* ws, unsigned voff, u64 tag) {
    u64 v; unsigned cnt, tmp;
    asm volatile("s_mov_b32 %1, %6\n.Lgm_p%=:\n\tglobal_load_dwordx2 %0, %3, %4 sc1\n\ts_waitcnt vmcnt(0)\n\tv_cmp_ne_u64 vcc, %5, %0\n\t"
                 "s_cmp_eq_u64 vcc, 0\n\ts_cbranch_scc1 .Lgm_q%=\n\ts_sleep 1\n\ts_sub_u32 %1, %1, 1\n\ts_cbranch_scc0 .Lgm_p%=\n\t"
                 "v_mov_b32 %2, %7\n\tglobal_store_dword %2, %2, %4 sc1\n.Lgm_q%=:"
                 : "=&v"(v), "=&s"(cnt), "=&v"(tmp) : "v"(voff), "s"(ws), "s"(tag), "n"(SPIN_MAX), "n"(WS_TMO) : "scc", "vcc", "memory");
}
__device__ __forceinline__ void poll4(const char* ws, u64 tag, int lane) {
    const gu64* p = (const gu64*)(ws + WS_FKV) + lane;
    for (unsigned s = 0;; ++s) {
        const u64 v0 = __hip_atomic_load(p, __ATOMIC_RELAXED, __HIP_MEMORY_SCOPE_AGENT), v1 = __hip_atomic_load(p + 64, __ATOMIC_RELAXED, __HIP_MEMORY_SCOPE_AGENT);
        const u64 v2 = __hip_atomic_load(p + 128, __ATOMIC_RELAXED, __HIP_MEMORY_SCOPE_AGENT), v3 = __hip_atomic_load(p + 192, __ATOMIC_RELAXED, __HIP_MEMORY_SCOPE_AGENT);
        if (__all(v0 == tag && v1 == tag && v2 == tag && v3 == tag)) break;
        if (s > SPIN_MAX) { if (lane == 0) __hip_atomic_store((gu32*)(ws + WS_TMO), 0x100u, __ATOMIC_RELAXED, __HIP_MEMORY_SCOPE_AGENT); break; }
        __builtin_amdgcn_s_sleep(1);
    }
    FENCE();
}
__device__ __forceinline__ void put_flag(char* ws, unsigned off, u64 tag) { __hip_atomic_store((gu64*)(ws + off), tag, __ATOMIC_RELAXED, __HIP_MEMORY_SCOPE_AGENT); }
__device__ __forceinline__ void lds_inc(unsigned a, int) {
    asm volatile("s_mov_b64 exec, 1\n\tds_add_u32 %0, %1\n\ts_mov_b64 exec, -1" :: "v"(a), "v"(1u) : "memory");
}
template <int SLP> __device__ __forceinline__ void lds_wait(unsigned a, unsigned target, const char* ws) {
    unsigned v, sv, cnt;
    asm volatile("s_mov_b32 %2, %5\n.Lgm_l%=:\n\tds_read_b32 %0, %3\n\ts_waitcnt lgkmcnt(0)\n\tv_readfirstlane_b32 %1, %0\n\ts_cmp_ge_u32 %1, %4\n\t"
                 "s_cbranch_scc1 .Lgm_d%=\n\ts_sleep %6\n\ts_sub_u32 %2, %2, 1\n\ts_cbranch_scc0 .Lgm_l%=\n\t"
                 "v_mov_b32 %0, %7\n\tglobal_store_dword %0, %0, %8 sc1\n.Lgm_d%=:"
                 : "=&v"(v), "=&s"(sv), "=&s"(cnt) : "v"(a), "s"(target), "n"(SPIN_MAX), "n"(SLP), "n"(WS_TMO), "s"(ws) : "scc", "memory");
}
__device__ __forceinline__ void async_bar(unsigned& gen, int lane, const char* ws) {
    asm volatile("s_waitcnt lgkmcnt(0)" ::: "memory");
    gen += 4; lds_inc(C_ASY, lane); lds_wait<2>(C_ASY, gen, ws);
}

__device__ __forceinline__ void features(const f32x16& a0, float p16, float p17, float p18, float p19, unsigned wa, unsigned (&out)[18]) {
    const u32x4 b0 = {cvtpk(a0[0], a0[1]), cvtpk(a0[2], a0[3]), cvtpk(a0[4], a0[5]), cvtpk(a0[6], a0[7])};
    const u32x4 b1 = {cvtpk(a0[8], a0[9]), cvtpk(a0[10], a0[11]), cvtpk(a0[12], a0[13]), cvtpk(a0[14], a0[15])};
    const u32x2 b2 = {cvtpk(p16, p17), cvtpk(p18, p19)};
    const u32x4 wt = LA(u32x4, wa + 64);
    f32x16 P0 = {}, P1 = {};
    P0 = MFMA(LA(u32x4, wa), b0, P0); P1 = MFMA(LA(u32x4, wa + 32), b0, P1);
    P0 = MFMA(LA(u32x4, wa + 16), b1, P0); P1 = MFMA(LA(u32x4, wa + 48), b1, P1);
    P0 = MFMA8(((u32x2){wt[0], wt[1]}), b2, P0); P1 = MFMA8(((u32x2){wt[2], wt[3]}), b2, P1);
    float p[18];
#pragma unroll
    for (int i = 0; i < 16; ++i) p[i] = P0[i];
    p[16] = P1[0]; p[17] = P1[1];
    float mx = p[0], mn = p[0];
#pragma unroll
    for (int i = 1; i < 18; ++i) { mx = __builtin_fmaxf(mx, p[i]); mn = __builtin_fminf(mn, p[i]); }
    const float L = 1.4426950408889634f, mxl = -mx * L, mnl = mn * L;
    const f32x2 cp = {L, L}, cn = {-L, -L}, bp = {mxl, mxl}, bn = {mnl, mnl};
    f32x2 ep[9], en[9], sp2 = {0.f, 0.f}, sn2 = {0.f, 0.f};
#pragma unroll
    for (int u = 0; u < 9; ++u) {
        const f32x2 pp = {p[2 * u], p[2 * u + 1]};
        const f32x2 ap = __builtin_elementwise_fma(pp, cp, bp), an = __builtin_elementwise_fma(pp, cn, bn);
        ep[u] = (f32x2){EX(ap[0]), EX(ap[1])}; en[u] = (f32x2){EX(an[0]), EX(an[1])};
        sp2 += ep[u]; sn2 += en[u];
    }
    const float rp = __builtin_amdgcn_rcpf(sp2[0] + sp2[1]), rn = __builtin_amdgcn_rcpf(sn2[0] + sn2[1]);
    const f32x2 rp2 = {rp, rp}, rn2 = {rn, rn};
#pragma unroll
    for (int u = 0; u < 9; ++u) { const f32x2 a = ep[u] * rp2, c = en[u] * rn2; out[u] = cvtpk(a[0], a[1]); out[9 + u] = cvtpk(c[0], c[1]); }
}

struct Ptrs { const char *x, *wqkv, *wphi, *woA, *wq, *wk, *wv, *woB, *gate; char *out, *ws; };

template <bool ISB>
__device__ __forceinline__ void role_main(const Ptrs& P, const int wid, const int lane, unsigned pf) {
    const int b = blockIdx.x, x8 = b & 7, r = b >> 3;
    const int ti = 8 * (x8 & 1) + (r & 7), tj = 4 * (x8 >> 1) + (r >> 3);
    const int tq = tj ^ 1;
    const int tb = wid & 3, n = lane & 31, hi = lane >> 5, n16 = lane & 15, g4 = lane >> 4;
    const int tid = wid * 64 + lane;
    char* const ws = P.ws;
    const __amdgpu_buffer_rsrc_t rws = __builtin_amdgcn_make_buffer_rsrc((void*)ws, (short)0, 0x1000000, 0x00020000);
    STAMP(0);
    if (tid == 0) { LW(u32x4, L_CNT) = (u32x4){0u, 0u, 0u, 0u}; LW(u32x4, L_CNT + 16) = (u32x4){0u, 0u, 0u, 0u}; LW(u32x4, L_CNT + 32) = (u32x4){0u, 0u, 0u, 0u}; }
    if (!AB) BAR_L();
    u32x2 Ev;
    const unsigned obf = WS_OBF + (unsigned)(4 * ti + tb) * 36 * 1024, obm = obf + 2 * tq * 1024 + lane * 16;
    const unsigned obr = obf + (32 + (tq >> 2)) * 1024 + (((tq >> 1) & 1) * 32 + n) * 16 + (tq & 1) * 8;
    const unsigned oam = WS_OAF + (unsigned)(4 * ti + tb) * 36 * 1024 + 2 * tq * 1024 + lane * 16;
    const unsigned oar = WS_OAF + (unsigned)(4 * ti + tb) * 36 * 1024 + (32 + (tq >> 2)) * 1024 + (((tq >> 1) & 1) * 32 + n) * 16 + (2 * (tq & 1) + hi) * 4;
    auto touch_w = [&](int s) {
        const unsigned l = lane < 36 ? lane : 35, cc = l / 12, ty = (l - 12 * cc) / 3, ln = l - 12 * cc - 3 * ty;
        const unsigned row = 64 * (3 * s + cc) + 32 * (x8 & 1) + r;
        u64 p0 = (u64)P.wqkv, p1 = (u64)P.wq;
        asm volatile("" : "+s"(p0), "+s"(p1));
        const u64 ad = (ty < 3 ? p0 + (row * 3456 + ty * 1152) : p1 + row * 1152) + (2 * (x8 >> 1) + ln) * 128;
        asm volatile("global_load_dword %0, %1, off" : "+v"(pf) : "v"(ad) : "memory");
    };
    unsigned xo[4]; u64 wb[5]; unsigned wst[5];
    auto dma1 = [&](int c, int i) {
        const unsigned slot = (c & 3) * SLOT;
        if (i < 4) dma_s(slot + (4 * tb + i) * 1024, P.x + c * 128, xo[i]);
        else { dma_v(slot + WOFF + (5 * tb + i - 4) * 1024, (const char*)wb[i - 4]); wb[i - 4] += wst[i - 4]; }
    };
    auto issue = [&](int c) {
#pragma unroll
        for (int i = 0; i < 9; ++i) dma1(c, i);
    };
#pragma unroll
    for (int i = 0; i < 4; ++i) xo[i] = (unsigned)(128 * ti + 32 * tb + 8 * i + (lane >> 3)) * 1152u + (((lane & 7) ^ (4 * (i & 1) + (lane >> 4))) * 16);
    unsigned wa[2][3], xk[2][4];
    auto haddr = [&]() {
        const int i = lane & 15, sl = 2 * (g4 & 1) + ((i & 3) >> 1);
        const unsigned base = WOFF + (8 * (g4 >> 1) + (i >> 2)) * 320 + 32 * (i & 1), pk = 8 * (tj & 1), pq = 8 * (tq & 1);
        if (ISB) {
            wa[0][0] = base + 240 + 8 * sl + pq;
            wa[0][1] = base + (sl == 0 ? 240 + 32 + pq : 160 + 8 * (sl - 1) + pk);
            wa[0][2] = 0;
        } else {
            wa[0][0] = base + 8 * sl + pk;
            wa[0][1] = base + 80 + 8 * sl + pq;
            wa[0][2] = base + (sl == 0 ? 32 + pk : (sl == 1 ? 80 + 32 + pq : 160 + 24 + 8 * (sl - 2) + pk));
        }
        const unsigned xa = (32 * tb + n) * 128 + ((hi ^ ((n >> 1) & 7)) * 16);
#pragma unroll
        for (int k = 0; k < 4; ++k) { xk[0][k] = xa ^ (k * 32); xk[1][k] = xk[0][k] + 2 * SLOT; PIN(xk[0][k]); PIN(xk[1][k]); }
#pragma unroll
        for (int m = 0; m < (ISB ? 2 : 3); ++m) { wa[1][m] = wa[0][m] + 2 * SLOT; PIN(wa[0][m]); PIN(wa[1][m]); }
    };
    if (!ISB) {
        u32x4 xf[5], gf[5];
        __builtin_amdgcn_s_setprio(APRIO);
        const unsigned go = (32 * tb + 8 * g4) * 2, g4o = tb < 2 ? go + 1024 : go;
        {
            const unsigned zo = 0;
            asm volatile("s_mov_b32 exec_lo, 1\n\ts_mov_b32 exec_hi, 0\n\tglobal_load_dwordx2 %0, %6, %9 sc1\n\t"
                         "s_mov_b32 exec_lo, 0x00100010\n\ts_mov_b32 exec_hi, 0x00100010\n\t"
                         "global_load_dwordx4 %1, %7, %10\n\tglobal_load_dwordx4 %2, %7, %10 offset:256\n\tglobal_load_dwordx4 %3, %7, %10 offset:512\n\t"
                         "global_load_dwordx4 %4, %7, %10 offset:768\n\tglobal_load_dwordx4 %5, %8, %10\n\t"
                         "s_mov_b32 exec_lo, -1\n\ts_mov_b32 exec_hi, -1"
                         : "=&v"(Ev), "=&v"(gf[0]), "=&v"(gf[1]), "=&v"(gf[2]), "=&v"(gf[3]), "=&v"(gf[4])
                         : "v"(zo), "v"(go), "v"(g4o), "s"(ws), "s"(P.gate) : "memory");
        }
        {
            const char* xr = P.x + (unsigned)(128 * ti + 8 * tj) * 1152u;
            dma_s(L_XR + tb * 1024, xr + tb * 1024, lane * 16); dma_s(L_XR + (4 + tb) * 1024, xr + (4 + tb) * 1024, lane * 16);
            if (tb == 3) dma_s(L_XR + 8 * 1024, xr + 8 * 1024, lane * 16);
        }
#pragma unroll
        for (int t = 0; t < 10; ++t) {
            const int p = tb + 4 * t;
            if (t != 5 || tb != 0) dma_s(L_WKV + p * 1024, p < 20 ? P.wk + p * 1024 : P.wv + (p * 1024 - 20736), lane * 16);
            else { if (lane < 16) dma_s(L_WKV + 20 * 1024, P.wk + 20 * 1024, lane * 16); else dma_s(L_WKV + 20 * 1024, P.wv - 256, lane * 16); }
        }
        if (tb == 1) { if (lane < 32) dma_s(L_WKV + 40 * 1024, P.wv + (40 * 1024 - 20736), lane * 16); }
        if (AB) BAR_L();
        if (RO) lds_inc(C_RAW, lane);
        {
            const u32x4 pz = {POISON, POISON, POISON, POISON};
            __builtin_amdgcn_raw_buffer_store_b128(pz, rws, obm, 0, 16);
            __builtin_amdgcn_raw_buffer_store_b128(pz, rws, obm + 1024, 0, 16);
            __builtin_amdgcn_raw_buffer_store_b64((u32x2){POISON, POISON}, rws, obr, 0, 16);
        }
        STAMP(16);
        const int i = lane & 15;
        const unsigned rowa = L_WKV + (32 * tb + 8 * g4 + 2 * (i >> 2)) * 36;
        unsigned am = rowa + 8 * (i & 3), ar = rowa + (i & 1) * 20736 + 32;
        unsigned xa0 = L_XR + (n16 & 7) * 1152 + go, xa4 = L_XR + (n16 & 7) * 1152 + g4o;
        int ra_ = ((lane - 16) & 63) * 4;
        const bool g0 = g4 == 0, up = n16 >= 8;
        unsigned ra = L_RED + tb * 1280 + (n16 & 7) * 160, rav = ra + (up ? 64 : 0) + g4 * 16, rar = ra + (g0 ? 128 : (g4 == 3 ? 136 : 144));
        if (HA) { PIN(am); PIN(ar); PIN(xa0); PIN(xa4); PIN(ra_); PIN(rav); PIN(rar); haddr(); }
        if (tb < 2) { touch_w(1 + tb); WAITV(4); } else WAITV(3);
        asm volatile("" : "+v"(Ev), "+v"(gf[0]), "+v"(gf[1]), "+v"(gf[2]), "+v"(gf[3]), "+v"(gf[4]), "+v"(pf) :: "memory");
        lds_inc(C_RAW, lane); lds_wait<1>(C_RAW, RO ? 8 : 4, ws);
        STAMP(1);
#pragma unroll
        for (int s = 0; s < 5; ++s) xf[s] = LA(u32x4, s < 4 ? xa0 + 256 * s : xa4);
        u32x2 we0[5], we1[5], wr[5], wo0[5], wo1[5];
#pragma unroll
        for (int s = 0; s < 5; ++s) {
            const unsigned o = (s < 4 || tb < 2 ? s : 0) * 128 * 36;
            we0[s] = trd(am + o); we1[s] = trd(am + o + 20736); wr[s] = trd(ar + o); wo0[s] = trd(am + o + 40); wo1[s] = trd(am + o + 40 + 20736);
        }
        SBAR();
#pragma unroll
        for (int s = 0; s < 5; ++s) { PIN(we0[s]); PIN(we1[s]); PIN(wr[s]); PIN(wo0[s]); PIN(wo1[s]); PIN(xf[s]); }
        lds_inc(C_WKVF, lane);
        f32x4 E0 = {}, E1 = {}, E2 = {}, O0 = {}, O1 = {}, O2 = {}, G = {};
#pragma unroll
        for (int s = 0; s < 5; ++s) {
            if (s < 4 || tb < 2) {
                const u32x2 be = {__builtin_amdgcn_perm(xf[s][1], xf[s][0], 0x05040100u), __builtin_amdgcn_perm(xf[s][3], xf[s][2], 0x05040100u)};
                const u32x2 bo = {__builtin_amdgcn_perm(xf[s][1], xf[s][0], 0x07060302u), __builtin_amdgcn_perm(xf[s][3], xf[s][2], 0x07060302u)};
                E0 = MFMA1616(we0[s], be, E0); E1 = MFMA1616(we1[s], be, E1); E2 = MFMA1616(wr[s], be, E2);
                O0 = MFMA1616(wo0[s], bo, O0); O1 = MFMA1616(wo1[s], bo, O1); O2 = MFMA1616(wr[s], bo, O2);
                G = MFMA16(gf[s], xf[s], G);
            }
        }
        auto rot16 = [&](float v) { return __int_as_float(__builtin_amdgcn_ds_bpermute(ra_, __float_as_int(v))); };
        const float k0 = rot16(O0[2]), k1 = rot16(O0[3]), v0 = rot16(O1[2]), v1 = rot16(O1[3]), w0 = rot16(O2[2]), w1 = rot16(O2[3]);
        const f32x4 K = {E0[0] + (g0 ? O2[2] : k0), E0[1] + (g0 ? O2[3] : k1), E0[2] + O0[0], E0[3] + O0[1]};
        const f32x4 V = {E1[0] + (g0 ? w0 : v0), E1[1] + (g0 ? w1 : v1), E1[2] + O1[0], E1[3] + O1[1]};
        lds_wait<1>(C_WKVF, 4, ws);
        LW(f32x4, rav) = up ? V : K;
        const f32x2 rm = g0 ? (f32x2){E2[0] + k0, E2[1] + k1} : (g4 == 3 ? (f32x2){E2[0] + O1[2], E2[1] + O1[3]} : (f32x2){G[0], G[0]});
        if (!up && g4 != 2) LW(f32x2, rar) = rm;
        __builtin_amdgcn_s_setprio(0);
        STAMP(2);
    } else {
        { const unsigned zo = 0; asm volatile("s_mov_b32 exec_lo, 1\n\ts_mov_b32 exec_hi, 0\n\tglobal_load_dwordx2 %0, %1, %2 sc1\n\ts_mov_b32 exec_lo, -1\n\ts_mov_b32 exec_hi, -1" : "=&v"(Ev) : "v"(zo), "s"(ws) : "memory"); }
        if (tb == 0) touch_w(0);
        const int c0 = 72 * tj - 8 * (tj & 1), c0q = 72 * tq - 8 * (tq & 1);
#pragma unroll
        for (int i = 0; i < 5; ++i) {
            const unsigned q = (5 * tb + i) * 64 + lane, row = q / 20, wi = q - row * 20, ty = wi / 5, pc = wi - ty * 5;
            const bool isq = ty == 3;
            const unsigned rs = isq ? 1152u : 3456u, to = ty == 0 ? 1152u : (ty == 2 ? 2304u : 0u);
            wb[i] = (isq ? (u64)P.wq : (u64)P.wqkv) + (row * rs + to + ((ty & 1) ? c0q : c0) + pc * 16);
            wst[i] = rs * 64;
        }
        STAMP(17);
        if (AB) BAR_L();
        if (RS0) __builtin_amdgcn_s_sleep(RS0);
        if (RO >= 4) lds_wait<1>(C_RAW, 4, ws);
        issue(0);
        if (RO == 1 || RO == 3) lds_wait<1>(C_RAW, 4, ws);
        if (RO == 2 || RO == 4) lds_wait<1>(C_RAW, 8, ws);
        if (RS1) __builtin_amdgcn_s_sleep(RS1);
        issue(1);
        if (RO == 3 || RO == 5) lds_wait<1>(C_RAW, 8, ws);
        if (RS2) __builtin_amdgcn_s_sleep(RS2);
        issue(2);
        if (HS == 2) { lds_wait<1>(C_WKVF, 4, ws); STAMP(28); issue(3); }
        STAMP(18);
    }
    f32x16 acc0 = {}, acc1 = {}, acc2 = {};
    if (ISB || !HA) haddr();
#define WPHI_AT 4
    constexpr bool NODMA = false;
#define HM(acc, a, b) acc = MFMA(a, b, acc)
    constexpr bool HRD = true;
    u32x4 FX[4], FW0[4], FW1[4], FW2[4];
    auto rdx = [&](int c, int ks) { return LA(u32x4, xk[(c >> 1) & 1][ks] + (c & 1) * SLOT); };
    auto rdw = [&](int c, int ks, int m) { const unsigned a = wa[(c >> 1) & 1][m] + (c & 1) * SLOT + ks * 5120; return trd2(a, a + 1280); };
    if (!ISB) BAR_L();
    else if (HS == 2) asm volatile("s_waitcnt vmcnt(27)\n\ts_barrier" : "+v"(Ev), "+v"(pf) :: "memory");
    else asm volatile("s_waitcnt vmcnt(18)\n\ts_barrier" : "+v"(Ev), "+v"(pf) :: "memory");
    const u64 tag = mix64(((u64)(unsigned)uni(Ev[1]) << 32) | (unsigned)uni(Ev[0]));
    STAMP(3);
    if (!ISB) {
        if (tb == 2) {
            dma_s(L_WPR, P.wphi + tj * 1296, lane * 16); dma_s(L_WPQ, P.wphi + tq * 1296, lane * 16);
            if (lane < 17) { dma_s(L_WPR + 1024, P.wphi + tj * 1296 + 1024, lane * 16); dma_s(L_WPQ + 1024, P.wphi + tq * 1296 + 1024, lane * 16); }
        }
    }
    auto kv_format = [&]() {
        {
            const int t = lane & 7, bi = tj >> 2, pc = tj & 3;
            const int rho = 16 * (pc >> 1) + 8 * (t >> 2) + 4 * (pc & 1) + (t & 3);
            const int d = lane - 16;
            unsigned base, strd;
            if (lane < 16) { base = t * 40 + 8 * (lane >> 3); strd = 1; }
            else if (lane < 40) { base = d < 16 ? 16 + d : (d < 18 ? 18 + d : 0); strd = 40; }
            else if (lane < 48) { base = t * 40 + 32; strd = 1; }
            else { base = t * 40 + 36; strd = 0; }
            float v[8];
#pragma unroll
            for (int i = 0; i < 8; ++i) {
                const unsigned ad = L_RED + (base + i * strd) * 4;
                v[i] = (LA(float, ad) + LA(float, ad + 1280)) + (LA(float, ad + 2560) + LA(float, ad + 3840));
            }
            float ss = v[0] * v[0] + v[1] * v[1];
            float s6 = 0.f;
#pragma unroll
            for (int i = 2; i < 8; ++i) s6 += v[i] * v[i];
            if (lane < 16) ss += s6;
            u32x4 o = {cvtpk(v[0], v[1]), cvtpk(v[2], v[3]), cvtpk(v[4], v[5]), cvtpk(v[6], v[7])};
            if (lane == 34) o = (u32x4){0x3f803f80u, 0x3f803f80u, 0x3f803f80u, 0x3f803f80u};
            const unsigned img = WS_KVIMG + ti * STEP_BYTES;
            const unsigned o16 = lane < 16 ? img + bi * KB_BYTES + ((lane >> 3) * 32 + rho) * 16 : img + bi * KB_BYTES + 1024 + pc * 304 + d * 16;
            if (lane < 35) __builtin_amdgcn_raw_buffer_store_b128(o, rws, o16, 0, 16);
            float tot = __shfl(ss, t) + __shfl(ss, 8 + t) + __shfl(ss, 40 + t);
            tot = __builtin_fmaxf(tot, dppf<0xb1>(tot)); tot = __builtin_fmaxf(tot, dppf<0x4e>(tot)); tot = __builtin_fmaxf(tot, dppf<0x141>(tot));
            const float gv = __builtin_amdgcn_rcpf(1.f + EX(-1.4426950408889634f * v[0]));
            unsigned o4, d4;
            if (lane < 48) { o4 = img + KR_OFF + (bi >> 1) * 256 + rho * 8 + (bi & 1) * 4; d4 = o[0]; }
            else if (lane < 56) { o4 = WS_G + (128 * ti + 8 * tj + t) * 4; d4 = __float_as_uint(gv); }
            else { o4 = WS_KMAX + (16 * ti + tj) * 4; d4 = __float_as_uint(tot); }
            if (lane >= 40 && lane <= 56) __builtin_amdgcn_raw_buffer_store_b32(d4, rws, o4, 0, 16);
        }
    };
    auto clear_wphi = [&]() {
        const u32x4 z = {0u, 0u, 0u, 0u};
        LW(u32x4, L_WPHI + (tb * 64 + lane) * 16) = z;
        if (lane < 16) LW(u32x4, L_WPHI + 4096 + (tb * 16 + lane) * 16) = z;
        if (lane < 5) LW(u32x4, L_ZERO + lane * 16) = z;
    };
    auto fill_wphi = [&]() {
#pragma unroll
        for (int rd = 0; rd < 3; ++rd) {
            const unsigned idx = rd * 256 + tb * 64 + lane;
            if (rd < 2 || idx < 648) {
                const unsigned v = LA(unsigned, L_WPR + idx * 4);
                const unsigned q = (idx * 1619u) >> 18, r2 = idx - 162 * q;
                const unsigned d = (r2 * 7282u) >> 16, fp = r2 - 9 * d, mb = fp >> 3, pos = d + 2 * (q & 1);
                const unsigned dst = L_WPHI + q * 1280 + (fp & 7) * 160 + (pos < 16 ? mb * 32 + 2 * pos : 32 + 8 * mb + 2 * pos);
                LW(unsigned short, dst) = (unsigned short)v; LW(unsigned short, dst + 80) = (unsigned short)(v >> 16);
            }
        }
    };
    const unsigned wrow = ((n >> 2) & 1) == hi ? L_WPHI + (hi * 16 + 4 * (n >> 3) + (n & 3)) * 80 : L_ZERO;
    unsigned wo[9];
    auto wo_init = [&]() {
#pragma unroll
        for (int i = 0; i < 9; ++i) {
            const unsigned q = (tb + 4 * i) * 64 + lane;
            const unsigned pr = q / 18, t = q - pr * 18, odd = t >= 8 && t < 17;
            wo[i] = (2 * pr + odd) * 1152 + (t < 8 ? t : (t < 17 ? t - 8 : 8)) * 16;
        }
#pragma unroll
        for (int i = 0; i < 9; ++i) PIN(wo[i]);
    };
    unsigned obm_ = obm, obr_ = obr;
    unsigned kp, rp, vp; f32x16 OA, OB; u32x4 V1b, P0, P1; float f0, f1, f2, f3;
    auto att_init = [&]() {
        kp = 4 * STEP_BYTES + lane * 16; rp = 4 * STEP_BYTES + KR_OFF + n * 8; vp = 4 * STEP_BYTES + 1024 + hi * 304 + (n < 18 ? n : 18) * 16;
        if (n > 18) vp = 0x80000;
        OA = (f32x16){}; OB = (f32x16){}; V1b = (u32x4){}; P0 = (u32x4){}; P1 = (u32x4){}; f0 = f1 = f2 = f3 = 0.f;
        PIN(kp); PIN(rp); PIN(vp);
        if (EX1) { obm_ = obm; obr_ = obr; PIN(obm_); PIN(obr_); }
        if (QH) { PIN(OA); PIN(OB); PIN(V1b); PIN(P0); PIN(P1); PIN(f0); PIN(f1); PIN(f2); PIN(f3); }
    };
    if (KF && !ISB && tb == 3) kv_format();
    SBAR();
#pragma unroll
    for (int ks = 0; ks < 2; ++ks) { FX[ks] = rdx(0, ks); FW0[ks] = rdw(0, ks, 0); FW1[ks] = rdw(0, ks, 1); if (!ISB) FW2[ks] = rdw(0, ks, 2); }
    u32x2 fl0, fl1, fl2, fl3;
    SBAR();
#pragma unroll
    for (int c = 0; c < 9; ++c) {
#pragma unroll
        for (int ks = 0; ks < 4; ++ks) {
            if (HS != 2 && ks == 0 && c == 0 && ISB) { lds_wait<1>(C_WKVF, 4, ws); STAMP(28); }
            if (ks == 0 && c == 5 && !ISB) {
                if (wid == 7) {
                    const unsigned fo = WS_FKV + lane * 8;
                    asm volatile("global_load_dwordx2 %0, %4, %5 sc1\n\tglobal_load_dwordx2 %1, %4, %5 offset:512 sc1\n\t"
                                 "global_load_dwordx2 %2, %4, %5 offset:1024 sc1\n\tglobal_load_dwordx2 %3, %4, %5 offset:1536 sc1"
                                 : "=&v"(fl0), "=&v"(fl1), "=&v"(fl2), "=&v"(fl3) : "v"(fo), "s"(ws) : "memory");
                }
            }
            if (ks == 2) {
                if (ISB) {
                    if (c == 0) bar_vl<14>(); else if (c == 1) bar_vl<12>(); else if (c <= 5) bar_vl<8>(); else if (c == 6) bar_vl<5>(); else if (c == 7) bar_vl<10>(); else BAR_L();
                } else if (KF && c == 1) {
                    bar_vl<2>();
                    PIN(pf);
                    if (tb == 3 && lane == 0) put_flag(ws, WS_FKV + (16 * ti + tj) * 8, tag);
                } else if (!KF && c == 2) {
                    bar_vl<6>();
                    PIN(pf);
                    if (tb == 3 && lane == 0) put_flag(ws, WS_FKV + (16 * ti + tj) * 8, tag);
                } else if (c == 2) { bar_vl<6>(); } else if (c == 3 || c == 4) bar_vl<6>();
                else if (c == 5) { if (tb == 3) WAITV(10); else WAITV(6); BAR_L(); }
                else if (c == 6) {
                    if (tb == 3) {
                        asm volatile("s_waitcnt vmcnt(4)" : "+v"(fl0), "+v"(fl1), "+v"(fl2), "+v"(fl3) :: "memory");
                        const unsigned t0 = (unsigned)tag, t1 = (unsigned)(tag >> 32);
                        const bool seen = fl0[0] == t0 && fl0[1] == t1 && fl1[0] == t0 && fl1[1] == t1 && fl2[0] == t0 && fl2[1] == t1 && fl3[0] == t0 && fl3[1] == t1;
                        if (!__all(seen)) poll4(ws, tag, lane);
                        bar_vl<4>();
                        if (b == 0 && lane == 0) __hip_atomic_store((gu64*)ws, tag, __ATOMIC_RELAXED, __HIP_MEMORY_SCOPE_AGENT);
                    } else bar_vl<4>();
                } else if (c == 7) bar_vl<0>();
                else BAR_L();
                if (!KF && !ISB && c == 0 && tb == 3) { __builtin_amdgcn_s_setprio(3); kv_format(); __builtin_amdgcn_s_setprio(0); }
                if (!ISB && c == 1) clear_wphi();
                if (!ISB && c == WPHI_AT) fill_wphi();
                if (c == 0) STAMP(20);
                if (c == 1) STAMP(21);
                if (c == 3) STAMP(4);
                if (c == 5) STAMP(22);
                if (c == 7) STAMP(23);
                if (c == 8) STAMP(6);
            }
            if (QH && ISB && c == 5 && ks == 3) att_init();
            if (WH && !ISB && c == 7 && ks == 3) wo_init();
            const int lc = ks < 2 ? c : c + 1, lk = (ks + 2) & 3;
            const bool ld = lc < 9;
            const int dc = c + 3;
            const int p0 = dc == 3 ? (ks == 0 ? 0 : 2 * ks + 1) : (ISB ? (ks == 0 ? 4 : 5 + ks) : ks);
            const int p1 = NODMA || dc >= 9 || (dc == 3 && !ISB) ? p0 : (dc == 3 ? 2 * ks + 3 : (ISB ? 6 + ks : ks + 1));
            const int pm = p0 + (p1 - p0 + 1) / 2;
            const int ei = 4 * (c - 6) + ks - 2;
            auto early = [&](int e) {
                if (e < 9) { const unsigned p = 37 + tb + 4 * e; dma_s_sc1(p * 1024, ws + WS_KVIMG + p * 1024, lane * 16); }
                else dma_s_sc1(tb == 1 ? L_KMAX : 73 * 1024, ws + (tb == 1 ? WS_KMAX : WS_KVIMG + 73 * 1024), lane * 16);
            };
            HM(acc0, FW0[ks], FX[ks]); SBAR();
            if (ld && HRD) { FX[lk] = rdx(lc, lk); FW0[lk] = rdw(lc, lk, 0); } SBAR();
#pragma unroll
            for (int p = p0; p < pm; ++p) dma1(dc, p);
            if (ISB && ei >= 0 && ei < 4) { early(ei < 2 ? 3 * ei : 2 * ei + 2); if (ei < 2) early(3 * ei + 1); }
            SBAR();
            if (LE && !ISB && 4 * c + ks >= 30) {
                const int e0 = 4 * c + ks == 30 ? 0 : 3 * (4 * c + ks - 30) + 1, e1 = 3 * (4 * c + ks - 30) + 4;
#pragma unroll
                for (int i = e0; i < e1; ++i) { const int p = 74 + tb + 4 * i < 147 ? 74 + tb + 4 * i : 147; dma_s_sc1(p * 1024, ws + WS_KVIMG + p * 1024, lane * 16); }
                SBAR();
            }
            HM(acc1, FW1[ks], FX[ks]); SBAR();
            if (ld && HRD) FW1[lk] = rdw(lc, lk, 1); SBAR();
#pragma unroll
            for (int p = pm; p < p1; ++p) dma1(dc, p);
            if (ISB && ei >= 0 && ei < 4) early(ei < 2 ? 3 * ei + 2 : 2 * ei + 3);
            SBAR();
            if (!ISB) { HM(acc2, FW2[ks], FX[ks]); SBAR(); if (ld && HRD) FW2[lk] = rdw(lc, lk, 2); SBAR(); }
        }
    }
    STAMP(5);
    const unsigned vrow = L_VB + (32 * tb + n) * 112 + hi * 52 + 8;

    if (ISB) {
        if (QP) __builtin_amdgcn_s_setprio(QP);
#pragma unroll
        for (int u = 0; u < 6; ++u) LW(unsigned, vrow + 4 * u) = cvtpk(acc1[4 + 2 * u], acc1[5 + 2 * u]);
        asm volatile("s_waitcnt vmcnt(0) lgkmcnt(0)" ::: "memory");
        lds_inc(C_PHI, lane); lds_inc(C_LAND0, lane);
        const float gate = __uint_as_float(__builtin_amdgcn_raw_buffer_load_b32(rws, WS_G + (128 * ti + 32 * tb + n) * 4, 0, 16));
        u32x4 QA, QB; u32x2 QRA0, QRA1, QRB0, QRB1; f32x16 MA, MB;
        {
            const float sc = 0.23570226039551584f * 1.4426950408889634f;
            float q[20];
#pragma unroll
            for (int i = 0; i < 16; ++i) q[i] = acc0[i] * sc;
#pragma unroll
            for (int i = 0; i < 4; ++i) q[16 + i] = acc1[i] * sc;
            float n2 = hi ? q[18] * q[18] + q[19] * q[19] : q[0] * q[0] + q[1] * q[1];
#pragma unroll
            for (int i = 2; i < 18; ++i) n2 = __builtin_fmaf(q[i], q[i], n2);
            unsigned cc[10], D[9];
#pragma unroll
            for (int u = 0; u < 10; ++u) cc[u] = cvtpk(q[2 * u], q[2 * u + 1]);
#pragma unroll
            for (int u = 0; u < 9; ++u) D[u] = hi ? cc[u + 1] : cc[u];
#pragma unroll
            for (int u = 0; u < 4; ++u) { const auto sw = __builtin_amdgcn_permlane32_swap(D[u], D[u + 4], false, false); QA[u] = sw[0]; QB[u] = sw[1]; }
            QRA0 = (u32x2){hi ? 0u : D[8], 0u}; QRA1 = (u32x2){0u, hi ? 0u : D[8]};
            QRB0 = (u32x2){hi ? D[8] : 0u, 0u}; QRB1 = (u32x2){0u, hi ? D[8] : 0u};
            SBAR();
            lds_wait<1>(C_LAND0, 4, ws);
            const f32x4 km4 = LA(f32x4, L_KMAX + lane * 16);
            const u32x4 ku = __builtin_bit_cast(u32x4, km4);
            const float km = __uint_as_float(wave_umax(__builtin_elementwise_max(__builtin_elementwise_max(ku[0], ku[1]), __builtin_elementwise_max(ku[2], ku[3]))));
            const float m = -1.01f * __builtin_amdgcn_sqrtf(n2 * km);
            const auto sw = __builtin_amdgcn_permlane32_swap(__float_as_uint(m), __float_as_uint(m), false, false);
            const float ma = __uint_as_float(sw[0]), mb = __uint_as_float(sw[1]);
#pragma unroll
            for (int i = 0; i < 16; ++i) { MA[i] = ma; MB[i] = mb; }
            {
                const f32x2 pa = {ma, ma}, pb = {mb, mb};
                u64 wa_[8], wb_[8];
#pragma unroll
                for (int i = 0; i < 8; ++i) {
                    asm volatile("v_mov_b64 %0, %1" : "=v"(wa_[i]) : "v"(__builtin_bit_cast(u64, pa)));
                    asm volatile("v_mov_b64 %0, %1" : "=v"(wb_[i]) : "v"(__builtin_bit_cast(u64, pb)));
                    MA[2 * i] = __uint_as_float((unsigned)wa_[i]); MA[2 * i + 1] = __uint_as_float((unsigned)(wa_[i] >> 32));
                    MB[2 * i] = __uint_as_float((unsigned)wb_[i]); MB[2 * i + 1] = __uint_as_float((unsigned)(wb_[i] >> 32));
                }
            }
            PIN(MA); PIN(MB); PIN(QA); PIN(QB); PIN(QRA0); PIN(QRA1); PIN(QRB0); PIN(QRB1);
        }
        STAMP(7);
        if (GP) __builtin_amdgcn_s_setprio(1);
        if (!QH) att_init();
        f32x16 S0A, S0B, S1A, S1B;
        u32x4 KMa, KMb, V0a, V0b, V1a; u32x2 KRX, KRY;
        float e0, e1, e2, e3;
        KRX = LA(u32x2, rp); KMa = LA(u32x4, kp);
        S0A = MFMA(KMa, QA, MA); S0B = MFMA(KMa, QB, MB); S0A = MFMA8(KRX, QRA0, S0A); S0B = MFMA8(KRX, QRB0, S0B);
        SBAR();
        KMb = LA(u32x4, kp + KB_BYTES); V0a = LA(u32x4, vp); V1a = LA(u32x4, vp + 608);
        __builtin_amdgcn_s_waitcnt(0xC07F);
        SBAR();
#define LD(dst, T, p) dst = LA(T, p)
#define NOP_ (void)0
#define SLOTB(MF, POST, d0, d1, d2, d3, S, g, Pq, i, a0, a1, a2, a3) do { MF; SBAR(); POST; SBAR(); \
        d0 = EX(S[4 * (g)]); d1 = EX(S[4 * (g) + 1]); PIN(d0); PIN(d1); SBAR(); \
        Pq[i] = cvtpk(a0, a1); Pq[i + 1] = cvtpk(a2, a3); PIN(Pq); SBAR(); \
        d2 = EX(S[4 * (g) + 2]); d3 = EX(S[4 * (g) + 3]); PIN(d2); PIN(d3); SBAR(); } while (0)
#define E_ e0, e1, e2, e3
#define F_ f0, f1, f2, f3
#define SLOT_(...) SLOTB(__VA_ARGS__)
#define WAITL __builtin_amdgcn_s_waitcnt(0xC07F)
#define PERIOD(SA, SB, NA, NB, KMu, KMl, KRu, QRAx, QRBx, V0u, V1u, V0l, V1l, va, ka, PA3) do { \
        SLOT_(NA = MFMA(KMu, QA, MA),     LD(KMl, u32x4, ka),              E_, SA, 0, P1, 2, F_); \
        SLOT_(OB = MFMA(V1l, P1, OB),     LD(V1l, u32x4, (va) + 608),      F_, SA, 1, P0, 0, E_); \
        SLOT_(NA = MFMA8(KRu, QRAx, NA),  LD(V0l, u32x4, va),              E_, SA, 2, P0, 2, F_); \
        SLOT_(OA = MFMA(V0u, P0, OA),     PA3,                             F_, SA, 3, P1, 0, E_); \
        SLOT_(NB = MFMA(KMu, QB, MB),     NOP_,                            E_, SB, 0, P1, 2, F_); \
        SLOT_(OA = MFMA(V1u, P1, OA),     NOP_,                            F_, SB, 1, P0, 0, E_); \
        SLOT_(NB = MFMA8(KRu, QRBx, NB),  WAITL,                           E_, SB, 2, P0, 2, F_); \
        SLOT_(OB = MFMA(V0u, P0, OB),     NOP_,                            F_, SB, 3, P1, 0, E_); \
    } while (0)
#pragma unroll 1
        for (int seg = 0; seg < 5; ++seg) {
            if ((0x12u >> seg) & 1) lds_wait<1>(seg == 1 ? C_LAND1 : C_LAND2, 4, ws);
            if ((0x0cu >> seg) & 1) lds_inc(seg == 2 ? C_BPROG1 : C_BPROG, lane);
            const int ns = SG ? (int)((0x52423u >> (4 * seg)) & 15) : (seg == 0 ? 3 : (seg == 1 ? 2 : (seg == 2 ? 4 : (seg == 3 ? 2 : 5))));
            unsigned dl = seg == 4 ? 0u - 15 * STEP_BYTES : STEP_BYTES;
            int s = ns;
#pragma unroll 1
            do {
                PERIOD(S0A, S0B, S1A, S1B, KMb, KMa, KRX, QRA1, QRB1, V0a, V1a, V0b, V1b, vp + KB_BYTES, kp + 2 * KB_BYTES, LD(KRY, u32x2, rp + 256));
                PERIOD(S1A, S1B, S0A, S0B, KMa, KMb, KRY, QRA0, QRB0, V0b, V1b, V0a, V1a, vp + 2 * KB_BYTES, kp + 3 * KB_BYTES, NOP_);
                rp += dl; kp += dl; PIN(rp); PIN(kp); SBAR();
                PERIOD(S0A, S0B, S1A, S1B, KMb, KMa, KRY, QRA1, QRB1, V0a, V1a, V0b, V1b, vp + 3 * KB_BYTES, kp, LD(KRX, u32x2, rp));
                vp += dl; PIN(vp); SBAR();
                PERIOD(S1A, S1B, S0A, S0B, KMa, KMb, KRX, QRA0, QRB0, V0b, V1b, V0a, V1a, vp, kp + KB_BYTES, NOP_);
                dl = STEP_BYTES;
            } while (--s);
        }
        P1[2] = cvtpk(f0, f1); P1[3] = cvtpk(f2, f3); PIN(P1); SBAR();
        if (BPRIO) __builtin_amdgcn_s_setprio(BPRIO);
        OB = MFMA(V1b, P1, OB);
        STAMP(10);
        auto rowsum = [&](const f32x16& O) { const auto sw = __builtin_amdgcn_permlane32_swap(__float_as_uint(O[10]), __float_as_uint(O[10]), false, false); return __uint_as_float(sw[0]); };
        float lA = rowsum(OA), lB = rowsum(OB);
        if (EX1 ? __builtin_expect(__any(!(lA >= 0x1p-80f) || !(lB >= 0x1p-80f)), 0) : __any(!(lA >= 0x1p-80f) || !(lB >= 0x1p-80f))) {
            const unsigned vo = WS_KVIMG + 1024 + hi * 304 + (n < 18 ? n : 18) * 16;
#pragma unroll 1
            for (int which = 0; which < 2; ++which) {
                const u32x4 Q = which ? QB : QA; const u32x2 R0 = which ? QRB0 : QRA0, R1 = which ? QRB1 : QRA1;
                float mx = -__builtin_inff();
#pragma unroll 1
                for (int kb = 0; kb < 64; ++kb) {
                    const unsigned bo = (kb >> 2) * STEP_BYTES;
                    const u32x4 km = __builtin_bit_cast(u32x4, __builtin_amdgcn_raw_buffer_load_b128(rws, WS_KVIMG + bo + (kb & 3) * KB_BYTES + lane * 16, 0, 16));
                    const u32x2 kr = __builtin_bit_cast(u32x2, __builtin_amdgcn_raw_buffer_load_b64(rws, WS_KVIMG + bo + KR_OFF + ((kb >> 1) & 1) * 256 + n * 8, 0, 16));
                    f32x16 S = {}; S = MFMA(km, Q, S); S = MFMA8(kr, (kb & 1) ? R1 : R0, S);
#pragma unroll
                    for (int i = 0; i < 16; ++i) mx = __builtin_fmaxf(mx, S[i]);
                }
                { const auto sw = __builtin_amdgcn_permlane32_swap(__float_as_uint(mx), __float_as_uint(mx), false, false); mx = __builtin_fmaxf(__uint_as_float(sw[0]), __uint_as_float(sw[1])); }
                f32x16 O = {}, C;
#pragma unroll
                for (int i = 0; i < 16; ++i) C[i] = -mx;
#pragma unroll 1
                for (int kb = 0; kb < 64; ++kb) {
                    const unsigned bo = (kb >> 2) * STEP_BYTES, bb = bo + (kb & 3) * KB_BYTES;
                    const u32x4 km = __builtin_bit_cast(u32x4, __builtin_amdgcn_raw_buffer_load_b128(rws, WS_KVIMG + bb + lane * 16, 0, 16));
                    const u32x2 kr = __builtin_bit_cast(u32x2, __builtin_amdgcn_raw_buffer_load_b64(rws, WS_KVIMG + bo + KR_OFF + ((kb >> 1) & 1) * 256 + n * 8, 0, 16));
                    const u32x4 v0 = __builtin_bit_cast(u32x4, __builtin_amdgcn_raw_buffer_load_b128(rws, vo + bb, 0, 16));
                    const u32x4 v1 = __builtin_bit_cast(u32x4, __builtin_amdgcn_raw_buffer_load_b128(rws, vo + bb + 608, 0, 16));
                    f32x16 S = MFMA(km, Q, C); S = MFMA8(kr, (kb & 1) ? R1 : R0, S);
                    float p[16];
#pragma unroll
                    for (int i = 0; i < 16; ++i) p[i] = EX(S[i]);
                    const u32x4 p0 = {cvtpk(p[0], p[1]), cvtpk(p[2], p[3]), cvtpk(p[4], p[5]), cvtpk(p[6], p[7])};
                    const u32x4 p1 = {cvtpk(p[8], p[9]), cvtpk(p[10], p[11]), cvtpk(p[12], p[13]), cvtpk(p[14], p[15])};
                    O = MFMA(v0, p0, O); O = MFMA(v1, p1, O);
                }
                const float lo = rowsum(O);
                if (which) { OB = O; lB = lo; } else { OA = O; lA = lo; }
            }
        }
        {
            const float ga = (1.f - gate) * __builtin_amdgcn_rcpf(lA), gb = (1.f - gate) * __builtin_amdgcn_rcpf(lB);
            auto word = [&](float lo, float hi_) { const unsigned w = cvtpk(lo, hi_); return w < POISON ? w : POISON - 1; };
            const u32x4 fa = {word(OA[0] * ga, OA[1] * ga), word(OA[2] * ga, OA[3] * ga), word(OA[4] * ga, OA[5] * ga), word(OA[6] * ga, OA[7] * ga)};
            const u32x4 fb = {word(OB[0] * gb, OB[1] * gb), word(OB[2] * gb, OB[3] * gb), word(OB[4] * gb, OB[5] * gb), word(OB[6] * gb, OB[7] * gb)};
            const u32x2 rr = {word(OA[8] * ga, OA[9] * ga), word(OB[8] * gb, OB[9] * gb)};
            __builtin_amdgcn_raw_buffer_store_b128(fa, rws, obm_, 0, 16);
            __builtin_amdgcn_raw_buffer_store_b128(fb, rws, obm_ + 1024, 0, 16);
            if (!hi) __builtin_amdgcn_raw_buffer_store_b64(rr, rws, obr_, 0, 16);
            lds_inc(C_BDONE, lane);
        }
        asm volatile(".Lgm_end:" ::: "memory");
        STAMP(11);
    } else {
        __builtin_amdgcn_s_setprio(PK);
        {
            unsigned ph[18];
            features(acc0, acc2[0], acc2[1], acc2[2], acc2[3], wrow, ph);
            const unsigned pa = L_PHIK + (32 * tb + n) * 160 + hi * 80;
#pragma unroll
            for (int u = 0; u < 4; ++u) LW(u32x4, pa + 16 * u) = (u32x4){ph[4 * u], ph[4 * u + 1], ph[4 * u + 2], ph[4 * u + 3]};
            LW(u32x2, pa + 64) = (u32x2){ph[16], ph[17]};
            LW(unsigned, vrow + 24) = cvtpk(acc2[8], acc2[9]); LW(unsigned, vrow + 28) = cvtpk(acc2[10], acc2[11]);
            LW(unsigned, vrow + 32) = cvtpk(acc2[12], acc2[13]); LW(unsigned, vrow + 36) = cvtpk(acc2[14], acc2[15]);
            LW(unsigned, vrow + 36 + 4 * hi) = 0x00003f80u;
            asm volatile("s_waitcnt lgkmcnt(0)" ::: "memory");
            lds_inc(C_PHI, lane);
        }
        WAITV(0);
        lds_inc(C_LAND1, lane);
        const float gate = __uint_as_float(__builtin_amdgcn_raw_buffer_load_b32(rws, WS_G + (128 * ti + 32 * tb + n) * 4, 0, 16));
        STAMP(7);
        lds_wait<2>(C_PHI, 8, ws);
        __builtin_amdgcn_s_setprio(2);
        unsigned gen = 0;
        {
            const int hh = tb & 1, i = lane & 15;
            const int tok = 8 * g4 + (i >> 2), gr = i & 3;
            const unsigned va = L_VB + tok * 112 + hh * 56 + 8 + gr * 8;
            const int ntask = tb < 2 ? 2 : 1;
#pragma unroll 1
            for (int t = 0; t < ntask; ++t) {
                const int mbf = tb < 2 ? 2 * t : 1;
                const unsigned pa = L_PHIK + tok * 160 + hh * 80 + mbf * 32 + gr * 8;
                f32x4 d0 = {}, d1 = {};
#pragma unroll
                for (int ks = 0; ks < 4; ++ks) {
                    const u32x4 a = trd2(pa + ks * 32 * 160, pa + ks * 32 * 160 + 4 * 160);
                    const u32x4 b0 = trd2(va + ks * 32 * 112, va + ks * 32 * 112 + 4 * 112);
                    const u32x4 b1 = trd2(va + ks * 32 * 112 + 32, va + ks * 32 * 112 + 4 * 112 + 32);
                    d0 = MFMA16(a, b0, d0); d1 = MFMA16(a, b1, d1);
                }
                const unsigned ko = WS_KVP + (unsigned)(16 * tj + ti) * 5472 + hh * 2736 + (16 * mbf + 4 * g4) * 4;
                if (mbf < 2 || g4 == 0) {
                    __builtin_amdgcn_raw_buffer_store_b128(__builtin_bit_cast(u32x4, d0), rws, ko + n16 * 144, 0, 16);
                    if (n16 < 3) __builtin_amdgcn_raw_buffer_store_b128(__builtin_bit_cast(u32x4, d1), rws, ko + (16 + n16) * 144, 0, 16);
                }
            }
        }
        WAITV(0);
        if (!LE) lds_inc(C_LAND1, lane);
        async_bar(gen, lane, ws);
        if (tb == 0 && lane == 0) put_flag(ws, WS_FKVP + (16 * tj + ti) * FLS, tag);
        STAMP(8);
        if (tb == 3) {
            const unsigned l0 = b * 21 + lane % 21;
            u64 p2 = (u64)P.woA, p3 = (u64)P.woB;
            asm volatile("" : "+s"(p2), "+s"(p3));
            const u64 ad = (lane < 21 ? p2 : p3) + (l0 < 5183 ? l0 : 5183) * 128;
            asm volatile("global_load_dword %0, %1, off" : "=&v"(pf) : "v"(ad) : "memory");
        }
#define OTOUCH 1
        unsigned pg = 0;
        if (OTOUCH == 1 && tb == 2) {
            const unsigned ro = (unsigned)(64 * (4 * x8 + (r & 3)) + (lane & 7) * 9) * 1152 + (r >> 2) * 144;
            if (lane < 8) asm volatile("global_load_dword %0, %1, %2" : "=&v"(pf) : "v"(ro), "s"(P.out) : "memory");
        }
        if (OTOUCH == 2 && tb == 2) {
            const unsigned ro = (unsigned)(64 * (4 * x8 + (r & 3)) + lane) * 1152 + (r >> 2) * 144;
            asm volatile("global_load_dword %0, %2, %3\n\tglobal_load_dword %1, %2, %3 offset:140" : "=&v"(pf), "=&v"(pg) : "v"(ro), "s"(P.out) : "memory");
        }
        for (int p = tb; p < 37; p += 4) dma_s_sc1(p * 1024, ws + WS_KVIMG + p * 1024, lane * 16);
        __builtin_amdgcn_s_setprio(PQ);
        unsigned pq[18];
        features(acc1, acc2[4], acc2[5], acc2[6], acc2[7], wrow == L_ZERO ? wrow : wrow + 2560, pq);
        WAITV(0);
        PIN(pf); PIN(pg);
        unsigned pfo = WS_FKVP + 16 * tq * FLS + (lane < 16 ? lane : 0) * FLS;
        const int it1 = 256 + tb * 22 + lane;
        const bool ok1 = lane < 22 && it1 < 342;
        unsigned ko0 = WS_KVP + (unsigned)(16 * tq) * 5472 + (tb * 64 + lane) * 16, ko1 = WS_KVP + (unsigned)(16 * tq) * 5472 + (ok1 ? it1 : 0) * 16;
        PIN(pfo); PIN(ko0); PIN(ko1);
        lds_inc(C_LAND2, lane);
        if (tb == 0) { poll_at(ws, pfo, tag); lds_inc(C_F1, lane); } else lds_wait<2>(C_F1, 1, ws);
        __builtin_amdgcn_s_setprio(PR);
        STAMP(9);
        {
            const int at = tb * 64 + lane;
#if !(PF && ONEP == 1)
            const int it1 = 256 + tb * 22 + lane;
            const bool ok1 = lane < 22 && it1 < 342;
            const unsigned ko0 = WS_KVP + (unsigned)(16 * tq) * 5472 + at * 16, ko1 = WS_KVP + (unsigned)(16 * tq) * 5472 + (ok1 ? it1 : 0) * 16;
#endif
            f32x4 v[16], w[16];
#pragma unroll
            for (int i = 0; i < 16; ++i) v[i] = __builtin_bit_cast(f32x4, __builtin_amdgcn_raw_buffer_load_b128(rws, ko0 + i * 5472, 0, 16));
#pragma unroll
            for (int i = 0; i < 16; ++i) w[i] = __builtin_bit_cast(f32x4, __builtin_amdgcn_raw_buffer_load_b128(rws, ko1 + i * 5472, 0, 16));
            SBAR();
            auto put = [&](const f32x4 (&p)[16], int it, bool ok) {
                f32x4 s = p[0];
#pragma unroll
                for (int i = 1; i < 16; ++i) s += p[i];
                const int hh = it >= 171, rem = it - 171 * hh, d = (rem * 7282) >> 16, fq = rem - 9 * d;
                if (ok) LW(u32x2, L_KVT + hh * 1600 + d * 80 + fq * 8) = (u32x2){cvtpk(s[0], s[1]), cvtpk(s[2], s[3])};
            };
            put(v, at, true); put(w, it1, ok1);
            if (tb == 3 && lane < 10) LW(u32x4, L_KVT + (lane >= 5) * 1600 + 19 * 80 + (lane % 5) * 16) = (u32x4){0u, 0u, 0u, 0u};
        }
        async_bar(gen, lane, ws);
        __builtin_amdgcn_s_setprio(PO);
        {
            const int hm = (n >> 2) & 1, d0 = 4 * (n >> 3) + (n & 3);
            const unsigned a0 = L_KVT + (hm == hi ? hm * 1600 + d0 * 80 : 19 * 80);
            const unsigned a1 = L_KVT + (hm == hi && d0 < 3 ? hm * 1600 + (16 + d0) * 80 : 19 * 80);
            f32x16 N0 = {}, N1 = {};
#pragma unroll
            for (int s = 0; s < 4; ++s) {
                const u32x4 bq = {pq[4 * s], pq[4 * s + 1], pq[4 * s + 2], pq[4 * s + 3]};
                N0 = MFMA(LA(u32x4, a0 + 16 * s), bq, N0); N1 = MFMA(LA(u32x4, a1 + 16 * s), bq, N1);
            }
            const u32x2 br = {pq[16], pq[17]};
            N0 = MFMA8(LA(u32x2, a0 + 64), br, N0); N1 = MFMA8(LA(u32x2, a1 + 64), br, N1);
            const float sc = gate / (N1[2] + 1e-6f);
            const unsigned fo = WS_OAF + (unsigned)(4 * ti + tb) * 36 * 1024;
            const u32x4 f0 = {cvtpk(N0[0] * sc, N0[1] * sc), cvtpk(N0[2] * sc, N0[3] * sc), cvtpk(N0[4] * sc, N0[5] * sc), cvtpk(N0[6] * sc, N0[7] * sc)};
            const u32x4 f1 = {cvtpk(N0[8] * sc, N0[9] * sc), cvtpk(N0[10] * sc, N0[11] * sc), cvtpk(N0[12] * sc, N0[13] * sc), cvtpk(N0[14] * sc, N0[15] * sc)};
            __builtin_amdgcn_raw_buffer_store_b128(f0, rws, fo + (2 * tq) * 1024 + lane * 16, 0, 16);
            __builtin_amdgcn_raw_buffer_store_b128(f1, rws, fo + (2 * tq + 1) * 1024 + lane * 16, 0, 16);
            __builtin_amdgcn_raw_buffer_store_b32(cvtpk(N1[0] * sc, N1[1] * sc), rws, fo + (32 + (tq >> 2)) * 1024 + (((tq >> 1) & 1) * 32 + n) * 16 + (2 * (tq & 1) + hi) * 4, 0, 16);
        }
        WAITV(0);
        async_bar(gen, lane, ws);
        if (tb == 0 && lane == 0) put_flag(ws, WS_FOA + (16 * ti + tq) * FLS, tag);
        __builtin_amdgcn_s_setprio(2);
        STAMP(10);
        const int tt = 4 * x8 + (r & 3), cg = r >> 2;
        if (!WH) wo_init();
        auto stage = [&](const char* w) {
            const char* wsl = w + cg * 144;
#pragma unroll
            for (int i = 0; i < 21; ++i) {
                const int p = tb + 4 * i;
                if (i < 20 || tb == 0) dma_s(L_WOS + p * 1024, wsl + (i / 9) * 256 * 1152, wo[i % 9]);
            }
        };
        auto stage_part = [&](const char* w, int part) {
            const char* wsl = w + cg * 144;
#pragma unroll
            for (int i = 0; i < 21; ++i) {
                const int p = tb + 4 * i;
                const bool low = i < 10, mid = i == 10, top = i > 10 && i < 20;
                const bool on = part == 0 ? (low || (mid && tb == 0)) : part == 1 ? (top || (mid && tb != 0) || (i == 20 && tb == 0)) : part == 2 ? low : (mid || top || (i == 20 && tb == 0));
                if (on) dma_s(L_WOS + p * 1024, wsl + (i / 9) * 256 * 1152, wo[i % 9]);
            }
        };
        const int i16 = lane & 15, gr = i16 & 3, hk = g4 & 1, fh = g4 >> 1;
        const unsigned fro = (unsigned)(2 * tt + (tb >> 1)) * 36 * 1024 + fh * 1024 + (hk * 32 + n16 + 16 * (tb & 1)) * 16;
        const unsigned par = BC ? (i16 >> 2) & 1 : 0, psh = gr * 8 - par * 16, d4 = BC ? (par ? 128 : 272) - (gr & 2) * 8 : 128;
        auto cbo = [&](unsigned a, int cb) { return cb < 4 ? a + cb * 32 : a + d4; };
        const unsigned remo = L_WOS + (18 * (8 * fh + 4 * hk + (i16 >> 3)) + 16 + ((i16 >> 2) & 1)) * 144 + psh;
        f32x4 T[5] = {};
        auto wfrag = [&](int ks, int cb, unsigned maino, unsigned r4s) {
            const unsigned a = cbo(ks < 16 ? maino : remo, cb) + (ks < 16 ? ks * 36 * 144 : (ks - 16) * 16 * 18 * 144);
            return trd2(a, a + (ks < 16 ? r4s : 2 * 18 * 144));
        };
        auto gemm = [&](const u32x4 (&bf)[18], unsigned maino, unsigned r4s, int nq, auto ord) {
            u32x4 wf[2][5];
#pragma unroll
            for (int cb = 0; cb < 5; ++cb) wf[0][cb] = wfrag(ord(0), cb, maino, r4s);
            SBAR();
#pragma unroll
            for (int q = 0; q < nq; ++q) {
#pragma unroll
                for (int cb = 0; cb < 5; ++cb) {
                    T[cb] = MFMA16(wf[q & 1][cb], bf[ord(q)], T[cb]); SBAR();
                    if (q < nq - 1) wf[(q + 1) & 1][cb] = wfrag(ord(q + 1), cb, maino, r4s);
                    SBAR();
                }
            }
        };
        u32x4 bf[18];
        const unsigned mainA = L_WOS + (18 * hk + 8 * fh + (i16 >> 2)) * 144 + psh;
        lds_wait<4>(C_BPROG1, 4, ws);
        stage_part(P.woA, 0);
        if (tb == 0) { poll_at(ws, WS_FOA + 16 * (tt >> 1) * FLS + (lane < 16 ? lane : 0) * FLS, tag); lds_inc(C_F2, lane); } else lds_wait<2>(C_F2, 1, ws);
#pragma unroll
        for (int ks = 0; ks < 18; ++ks) bf[ks] = __builtin_bit_cast(u32x4, __builtin_amdgcn_raw_buffer_load_b128(rws, WS_OAF + fro + ks * 2048, 0, 16));
        SBAR();
        lds_wait<BPS>(C_BPROG, 4, ws);
        stage_part(P.woA, 1);
        asm volatile("s_waitcnt vmcnt(10)" : "+v"(bf[0]), "+v"(bf[1]), "+v"(bf[2]), "+v"(bf[3]), "+v"(bf[4]), "+v"(bf[5]), "+v"(bf[6]), "+v"(bf[7]), "+v"(bf[8]),
                     "+v"(bf[9]), "+v"(bf[10]), "+v"(bf[11]), "+v"(bf[12]), "+v"(bf[13]), "+v"(bf[14]), "+v"(bf[15]), "+v"(bf[16]), "+v"(bf[17]) :: "memory");
        async_bar(gen, lane, ws);
        STAMP(11);
        __builtin_amdgcn_s_setprio(GP - 1);
        gemm(bf, mainA, 4 * 144, 9, [](int q) { return q < 8 ? q : 16; });
        __builtin_amdgcn_s_setprio(3);
        WAITV(0);
        async_bar(gen, lane, ws);
        stage_part(P.woB, 2);
        __builtin_amdgcn_s_setprio(GP - 1);
        gemm(bf, mainA, 4 * 144, 9, [](int q) { return q < 8 ? 8 + q : 17; });
        __builtin_amdgcn_s_setprio(3);
        async_bar(gen, lane, ws);
        STAMP(12);
        if (OTOUCH == 3 && tb == 2) {
            const unsigned ro = (unsigned)(64 * (4 * x8 + (r & 3)) + (lane & 7) * 9) * 1152 + (r >> 2) * 144;
            if (lane < 8) asm volatile("global_load_dword %0, %1, %2" : "=&v"(pg) : "v"(ro), "s"(P.out) : "memory");
        }
        if (SPL) stage_part(P.woB, 3); else stage(P.woB);
        WAITV(0);
        PIN(pg);
        async_bar(gen, lane, ws);
        STAMP(13);
        unsigned oo[3];
#pragma unroll
        for (int j = 0; j < 3; ++j) { const unsigned p = 64 * j + lane; oo[j] = (unsigned)(64 * tt + 16 * tb) * 1152 + cg * 144 + p * 16 + ((p * 7282) >> 16) * 1008; PIN(oo[j]); }
        {
            const unsigned maino = L_WOS + (18 * fh + 4 * hk + (i16 >> 2)) * 144 + psh;
            const unsigned remg = L_WOS + (36 * g4 + 16 + 18 * (i16 >> 3) + ((i16 >> 2) & 1)) * 144 + psh;
            const unsigned rro = WS_OBF + (unsigned)(2 * tt + (tb >> 1)) * 36 * 1024 + 32 * 1024 + ((g4 >> 1) * 32 + 16 * (tb & 1) + n16) * 16 + (g4 & 1) * 8;
            f32x4 G[4][5];
#pragma unroll
            for (int cb = 0; cb < 5; ++cb) {
                G[0][cb] = T[cb];
                asm volatile("" : "=v"(G[1][cb])); asm volatile("" : "=v"(G[2][cb])); asm volatile("" : "=v"(G[3][cb]));
            }
            u32x2 br[4] = {};
            unsigned have = 0, done = 0;
            bool bdone = BW && !EF && CK1;
            const unsigned fob = WS_OBF + fro + 4096;
#define FETCH(g) asm volatile("global_load_dwordx4 %0, %5, %7 offset:-4096 sc1\n\tglobal_load_dwordx4 %1, %5, %7 offset:-2048 sc1\n\t" \
                             "global_load_dwordx4 %2, %5, %7 sc1\n\tglobal_load_dwordx4 %3, %5, %7 offset:2048 sc1\n\tglobal_load_dwordx2 %4, %6, %7 offset:%8 sc1" \
                             : "+v"(bf[4 * g]), "+v"(bf[4 * g + 1]), "+v"(bf[4 * g + 2]), "+v"(bf[4 * g + 3]), "+v"(br[g]) \
                             : "v"(fob + g * 8192), "v"(rro), "s"(ws), "n"(g * 1024) : "memory")
#define LANDED() asm volatile("s_waitcnt vmcnt(0)" : "+v"(bf[0]), "+v"(bf[1]), "+v"(bf[2]), "+v"(bf[3]), "+v"(bf[4]), "+v"(bf[5]), "+v"(bf[6]), "+v"(bf[7]), \
                             "+v"(bf[8]), "+v"(bf[9]), "+v"(bf[10]), "+v"(bf[11]), "+v"(bf[12]), "+v"(bf[13]), "+v"(bf[14]), "+v"(bf[15]), \
                             "+v"(br[0]), "+v"(br[1]), "+v"(br[2]), "+v"(br[3]) :: "memory")
            if (BW) lds_wait<2>(C_BDONE, 4, ws);
            if (BD) __builtin_amdgcn_s_sleep(BD);
            FETCH(0); FETCH(1); FETCH(2); FETCH(3);
#pragma unroll 1
            for (unsigned s = 0;; ++s) {
                constexpr bool PGRP = PG && CK1 && BW && RF && SR == 0 && !EF;
                if (!PGRP) LANDED();
                auto check = [&](int g) {
                    if (!((have >> g) & 1)) {
                        unsigned m = br[g][0] > br[g][1] ? br[g][0] : br[g][1];
#pragma unroll
                        for (int u = 0; u < 4; ++u) {
#pragma unroll
                            for (int e = 0; e < 4; ++e) { const unsigned w = bf[4 * g + u][e]; m = m > w ? m : w; }
                        }
                        if (__all(m != POISON)) have |= 1u << g;
                    }
                };
                if (!(CK1 && BW && RF && SR == 0)) {
#pragma unroll
                    for (int g = 0; g < 4; ++g) check(g);
                }
                if (!bdone) { bdone = uni(__hip_atomic_load((AS3 unsigned*)(uintptr_t)C_BDONE, __ATOMIC_RELAXED, __HIP_MEMORY_SCOPE_WORKGROUP)) >= 4; if (bdone) STAMP(14); }
                if (bdone) {
#pragma unroll
                    for (int g = 0; g < 4; ++g) {
                        if (PGRP) asm volatile("s_waitcnt vmcnt(%5)" : "+v"(bf[4 * g]), "+v"(bf[4 * g + 1]), "+v"(bf[4 * g + 2]), "+v"(bf[4 * g + 3]), "+v"(br[g]) : "n"(15 - 5 * g) : "memory");
                        if (CK1 && BW && RF && SR == 0) check(g);
                        if (((have & ~done) >> g) & 1) {
                            u32x4 wf[2][5]; u32x2 wr[5];
#pragma unroll
                            for (int cb = 0; cb < 5; ++cb) wf[0][cb] = wfrag(4 * g, cb, maino, 8 * 144);
                            SBAR();
#pragma unroll
                            for (int u = 0; u < 4; ++u) {
#pragma unroll
                                for (int cb = 0; cb < 5; ++cb) {
                                    G[g][cb] = MFMA16(wf[u & 1][cb], bf[4 * g + u], g > 0 && u == 0 ? (f32x4){} : G[g][cb]); SBAR();

                                    if (u < 3) wf[(u + 1) & 1][cb] = wfrag(4 * g + u + 1, cb, maino, 8 * 144);

                                    else wr[cb] = trd(cbo(remg, cb) + g * 144 * 144);
                                    SBAR();
                                }
                            }
#pragma unroll
                            for (int cb = 0; cb < 5; ++cb) G[g][cb] = MFMA1616(wr[cb], br[g], G[g][cb]);
                            done |= 1u << g;
                        }
                    }
                    if (done == 15) {
                        break;
                    }
                }
                if (RF > 1) __builtin_amdgcn_s_sleep(RF);
                if (!(have & 1)) FETCH(0);
                if (!(have & 2)) FETCH(1);
                if (!(have & 4)) FETCH(2);
                if (!(have & 8)) FETCH(3);
                if (PGRP) LANDED();
                if (s > SPIN_MAX) {
                    LANDED();
                    if (lane == 0) __hip_atomic_store((gu32*)(ws + WS_TMO), 0x300u, __ATOMIC_RELAXED, __HIP_MEMORY_SCOPE_AGENT);
                    break;
                }
                if (bdone) { if (!OVL) __builtin_amdgcn_s_sleep(1); }
                else {
                    for (int w = 0; w < TIGHT; ++w) {
                        __builtin_amdgcn_s_sleep(2);
                        if (uni(__hip_atomic_load((AS3 unsigned*)(uintptr_t)C_BDONE, __ATOMIC_RELAXED, __HIP_MEMORY_SCOPE_WORKGROUP)) >= 4) break;
                    }
                }
            }
#pragma unroll
            for (int cb = 0; cb < 5; ++cb) T[cb] = (G[0][cb] + G[1][cb]) + (G[2][cb] + G[3][cb]);
        }
        STAMP(19);
#define OUTAUX 0
        const __amdgpu_buffer_rsrc_t rout = __builtin_amdgcn_make_buffer_rsrc((void*)P.out, (short)0, 2048 * 1152, 0x00020000);
        const unsigned so = tb * 2304;
#pragma unroll
        for (int cb = 0; cb < 5; ++cb) {
            const u32x2 o = {cvtpk(T[cb][0], T[cb][1]), cvtpk(T[cb][2], T[cb][3])};
            if (cb < 4 || g4 < 2) LW(u32x2, so + n16 * 144 + g4 * 8 + cb * 32) = o;
        }
#pragma unroll
        for (int j = 0; j < 3; ++j) {
            if (j < 2 || lane < 16) __builtin_amdgcn_raw_buffer_store_b128(LA(u32x4, so + (64 * j + lane) * 16), rout, oo[j], 0, OUTAUX);
        }
    }
}

__global__ __launch_bounds__(512, 2) void gated_mix_kernel(const void* x, const void* wqkv, const void* wphi, const void* woA, const void* wq, const void* wk,
                                                           const void* wv, const void* woB, const void* gate, void* out, void* ws) {
    extern __shared__ __attribute__((aligned(16))) char lds[];
    const int lane = threadIdx.x & 63, wid = uni(threadIdx.x >> 6);
    unsigned pf = 0;
    {
        u64 pc; unsigned len;
        const unsigned off = (((blockIdx.x >> 3) % CT) * 8 + wid + 8 * CT * lane) * 128;
        u64 sv;
        asm volatile("s_getpc_b64 %0\n.Lgm_begin:\n\ts_mov_b32 %1, .Lgm_end-.Lgm_begin\n\tv_cmp_gt_u32 vcc, %1, %4\n\ts_and_saveexec_b64 %2, vcc\n\t"
                     "global_load_dword %3, %4, %0\n\ts_mov_b64 exec, %2"
                     : "=&s"(pc), "=&s"(len), "=&s"(sv), "+v"(pf) : "v"(off) : "vcc", "scc", "memory");
    }
    const Ptrs P = {(const char*)x, (const char*)wqkv, (const char*)wphi, (const char*)woA, (const char*)wq, (const char*)wk, (const char*)wv, (const char*)woB, (const char*)gate, (char*)out, (char*)ws};
    if (wid < 4) role_main<true>(P, wid, lane, pf); else role_main<false>(P, wid, lane, pf);
}
}

extern "C" void kernel_launch(void* const* d_in, const int* in_sizes, int n_in, void* d_out, int out_size, void* d_ws, size_t ws_size, hipStream_t stream) {
    (void)in_sizes; (void)n_in; (void)out_size; (void)ws_size;
    (void)hipFuncSetAttribute((const void*)fk::gated_mix_kernel, hipFuncAttributeMaxDynamicSharedMemorySize, fk::LDS_BYTES);
    hipLaunchKernelGGL(fk::gated_mix_kernel, dim3(256), dim3(512), fk::LDS_BYTES, stream,
                       (const void*)d_in[0], (const void*)d_in[1], (const void*)d_in[2], (const void*)d_in[3], (const void*)d_in[5], (const void*)d_in[6],
                       (const void*)d_in[7], (const void*)d_in[8], (const void*)d_in[9], d_out, d_ws);
}
```
